```python
import torch

def _softmax(x, axis=-1):
    return torch.softmax(x, dim=axis)
def _gelu(x):
    return torch.nn.functional.gelu(x, approximate="tanh")
def _silu(x):
    return torch.nn.functional.silu(x)
def _sigmoid(x):
    return torch.sigmoid(x)
def _layernorm(x, axis=-1, eps=1e-5):
    m = x.mean(dim=axis, keepdim=True)
    v = x.var(dim=axis, keepdim=True, unbiased=False)
    return (x - m) / torch.sqrt(v + eps)
def _scatter_add(tgt, idx, src, axis=0):
    return tgt.clone().index_add_(axis, idx, src)
def _segment_sum(src, seg, n):
    out = torch.zeros((n,) + src.shape[1:], dtype=src.dtype)
    return out.index_add_(0, seg, src)
def _pad(x, pw):
    flat = tuple(v for pair in reversed(pw) for v in pair)
    return torch.nn.functional.pad(x, flat)

kernel_name = 'concat_heads__mha_block_cached__gqa_block_cached__near_ridge'

def setup_inputs():

    _w = (lambda a: (lambda t: t.to(torch.bfloat16) if t.is_floating_point() else t)(torch.as_tensor(a)))
    def _A_setup():
        B, Sq, D = 16, 144, 640
        H, Sk, Dh = 32, 512, 192
        return {
            "x": _w((lambda *s: torch.randn(*s, dtype=torch.bfloat16) * 0.8)(B, Sq, D)),
            "k_cache": _w((lambda *s: torch.randn(*s, dtype=torch.bfloat16) * 0.8)(B, H, Sk, Dh)),
            "v_cache": _w((lambda *s: torch.randn(*s, dtype=torch.bfloat16) * 0.05)(B, H, Sk, Dh)),
            "wq": _w((lambda *s: torch.randn(*s, dtype=torch.bfloat16) * 0.8)(D, H * Dh) * (D ** -0.5)),
            "wo": _w((lambda *s: torch.randn(*s, dtype=torch.bfloat16) * 0.05)(H * Dh, D) * ((H * Dh) ** -0.5)),
        }
    _A_in = _A_setup()

    def _B_setup():
        B, Sq, D = 16, 144, 640
        Hq, Hkv, Sk, Dh = 32, 32, 512, 192
        assert Hq % Hkv == 0, (Hq, Hkv)
        return {
            "x": _w((lambda *s: torch.randn(*s, dtype=torch.bfloat16) * 0.8)(B, Sq, D)),
            "k_cache": _w((lambda *s: torch.randn(*s, dtype=torch.bfloat16) * 0.8)(B, Hkv, Sk, Dh)),
            "v_cache": _w((lambda *s: torch.randn(*s, dtype=torch.bfloat16) * 0.05)(B, Hkv, Sk, Dh)),
            "wq": _w((lambda *s: torch.randn(*s, dtype=torch.bfloat16) * 0.8)(D, Hq * Dh) * (D ** -0.5)),
            "wo": _w((lambda *s: torch.randn(*s, dtype=torch.bfloat16) * 0.05)(Hq * Dh, D) * ((Hq * Dh) ** -0.5)),
        }
    _B_in = _B_setup()

    return {
        "A_x": _A_in["x"],
        "A_k_cache": _A_in["k_cache"],
        "A_v_cache": _A_in["v_cache"],
        "A_wq": _A_in["wq"],
        "A_wo": _A_in["wo"],
        "B_x": _B_in["x"],
        "B_k_cache": _B_in["k_cache"],
        "B_v_cache": _B_in["v_cache"],
        "B_wq": _B_in["wq"],
        "B_wo": _B_in["wo"],
    }

def reference(A_x, A_k_cache, A_v_cache, A_wq, A_wo, B_x, B_k_cache, B_v_cache, B_wq, B_wo):
    def _A_ref(x, k_cache, v_cache, wq, wo):
        B, Sq, D = x.shape
        H, Dh = 32, 192
        q = (lambda x, *d: x.permute(*d))((x @ wq).reshape(B, Sq, H, Dh), 0, 2, 1, 3)
        scores = torch.einsum("bhqd,bhkd->bhqk", q, k_cache) * (Dh ** -0.5)
        p = _softmax(scores, axis=-1)
        o = torch.einsum("bhqk,bhkd->bhqd", p, v_cache)
        return (lambda x, *d: x.permute(*d))(o, 0, 2, 1, 3).reshape(B, Sq, H * Dh) @ wo

    def _B_ref(x, k_cache, v_cache, wq, wo):
        B, Sq, D = x.shape
        Hq, Dh = 32, 192
        Hkv = k_cache.shape[1]
        rep = Hq // Hkv
        q = (lambda x, *d: x.permute(*d))((x @ wq).reshape(B, Sq, Hq, Dh), 0, 2, 1, 3)
        k = (lambda x, n, axis: torch.repeat_interleave(x, n, dim=axis))(k_cache, rep, axis=1)
        v = (lambda x, n, axis: torch.repeat_interleave(x, n, dim=axis))(v_cache, rep, axis=1)
        scores = torch.einsum("bhqd,bhkd->bhqk", q, k) * (Dh ** -0.5)
        p = _softmax(scores, axis=-1)
        o = torch.einsum("bhqk,bhkd->bhqd", p, v)
        return (lambda x, *d: x.permute(*d))(o, 0, 2, 1, 3).reshape(B, Sq, Hq * Dh) @ wo

    a = _A_ref(A_x, A_k_cache, A_v_cache, A_wq, A_wo)
    b = _B_ref(B_x, B_k_cache, B_v_cache, B_wq, B_wo)
    return torch.concatenate([a, b], axis=-1)
```

```cpp
#include <hip/hip_runtime.h>
#include <hip/hip_bf16.h>
#include <stdint.h>

typedef __attribute__((ext_vector_type(8))) short bf16x8;
typedef __attribute__((ext_vector_type(4))) float f32x4;
typedef unsigned short u16;

constexpr int NB = 16, SQ = 144, DM = 640, NH = 32, SK = 512, DH = 192, HD = NH * DH;

__device__ __forceinline__ u16 f2bf(float x) {
  unsigned u = __float_as_uint(x);
  u += 0x7fffu + ((u >> 16) & 1u);
  return (u16)(u >> 16);
}
__device__ __forceinline__ float bf2f(u16 x) { return __uint_as_float(((unsigned)x) << 16); }

__global__ __launch_bounds__(256) void gemm_simple(const u16* __restrict__ A, int lda, const u16* __restrict__ B, int ldb,
                                                   u16* __restrict__ C, int ldc, int K) {
  __shared__ u16 sA[64][40];
  __shared__ u16 sB[64][40];
  const int tid = threadIdx.x, wid = tid >> 6, lane = tid & 63;
  const int m0 = blockIdx.y * 64, n0 = blockIdx.x * 64;
  const int wm = (wid >> 1) * 32, wn = (wid & 1) * 32;
  f32x4 acc[2][2] = {};
  for (int k0 = 0; k0 < K; k0 += 32) {
    {
      int r = tid >> 2, c = (tid & 3) * 8;
      *(bf16x8*)&sA[r][c] = *(const bf16x8*)&A[(size_t)(m0 + r) * lda + k0 + c];
      int kr = tid >> 3, nc = (tid & 7) * 8;
      bf16x8 v = *(const bf16x8*)&B[(size_t)(k0 + kr) * ldb + n0 + nc];
      for (int j = 0; j < 8; ++j) sB[nc + j][kr] = (u16)v[j];
    }
    __syncthreads();
    bf16x8 a[2], b[2];
    for (int i = 0; i < 2; ++i) {
      a[i] = *(const bf16x8*)&sA[wm + i * 16 + (lane & 15)][(lane >> 4) * 8];
      b[i] = *(const bf16x8*)&sB[wn + i * 16 + (lane & 15)][(lane >> 4) * 8];
    }
    for (int i = 0; i < 2; ++i)
      for (int j = 0; j < 2; ++j) acc[i][j] = __builtin_amdgcn_mfma_f32_16x16x32_bf16(a[i], b[j], acc[i][j], 0, 0, 0);
    __syncthreads();
  }
  for (int i = 0; i < 2; ++i)
    for (int j = 0; j < 2; ++j)
      for (int r = 0; r < 4; ++r)
        C[(size_t)(m0 + wm + i * 16 + (lane >> 4) * 4 + r) * ldc + n0 + wn + j * 16 + (lane & 15)] = f2bf(acc[i][j][r]);
}

__global__ __launch_bounds__(256) void attn_simple(const u16* __restrict__ Q, const u16* __restrict__ Kc, const u16* __restrict__ Vc,
                                                   u16* __restrict__ O) {
  __shared__ float sS[16][SK + 4];
  __shared__ u16 sP[16][SK + 8];
  const int tid = threadIdx.x, wid = tid >> 6, lane = tid & 63;
  const int qt = blockIdx.x, h = blockIdx.y, b = blockIdx.z;
  const u16* q = Q + ((size_t)(b * SQ + qt * 16)) * HD + h * DH;
  const u16* k = Kc + ((size_t)(b * NH + h)) * SK * DH;
  const u16* v = Vc + ((size_t)(b * NH + h)) * SK * DH;
  bf16x8 qf[6];
  for (int s = 0; s < 6; ++s) qf[s] = *(const bf16x8*)&q[(size_t)(lane & 15) * HD + s * 32 + (lane >> 4) * 8];
  for (int t = 0; t < 8; ++t) {
    int key0 = wid * 128 + t * 16;
    f32x4 acc = {};
    for (int s = 0; s < 6; ++s) {
      bf16x8 kf = *(const bf16x8*)&k[(size_t)(key0 + (lane & 15)) * DH + s * 32 + (lane >> 4) * 8];
      acc = __builtin_amdgcn_mfma_f32_16x16x32_bf16(qf[s], kf, acc, 0, 0, 0);
    }
    for (int r = 0; r < 4; ++r) sS[(lane >> 4) * 4 + r][key0 + (lane & 15)] = acc[r];
  }
  __syncthreads();
  const float scale = 0.07216878364870322f;
  __shared__ float sInv[16];
  for (int rr = 0; rr < 4; ++rr) {
    int r = wid * 4 + rr;
    float vals[8], mx = -1e30f;
    for (int j = 0; j < 8; ++j) { vals[j] = sS[r][lane + 64 * j] * scale; mx = fmaxf(mx, vals[j]); }
    for (int o = 32; o > 0; o >>= 1) mx = fmaxf(mx, __shfl_xor(mx, o));
    float sum = 0.f;
    for (int j = 0; j < 8; ++j) { vals[j] = __expf(vals[j] - mx); sum += vals[j]; }
    for (int o = 32; o > 0; o >>= 1) sum += __shfl_xor(sum, o);
    float inv = 1.f / sum;
    for (int j = 0; j < 8; ++j) sP[r][lane + 64 * j] = f2bf(vals[j] * inv);
  }
  __syncthreads();
  for (int t = 0; t < 3; ++t) {
    int d0 = wid * 48 + t * 16;
    f32x4 acc = {};
    for (int s = 0; s < 16; ++s) {
      bf16x8 pf = *(const bf16x8*)&sP[lane & 15][s * 32 + (lane >> 4) * 8];
      bf16x8 vf;
      for (int j = 0; j < 8; ++j) vf[j] = (short)v[(size_t)(s * 32 + (lane >> 4) * 8 + j) * DH + d0 + (lane & 15)];
      acc = __builtin_amdgcn_mfma_f32_16x16x32_bf16(pf, vf, acc, 0, 0, 0);
    }
    for (int r = 0; r < 4; ++r)
      O[((size_t)(b * SQ + qt * 16 + (lane >> 4) * 4 + r)) * HD + h * DH + d0 + (lane & 15)] = f2bf(acc[r]);
  }
  (void)sInv;
}

extern "C" void kernel_launch(void* const* d_in, const int* in_sizes, int n_in, void* d_out, int out_size, void* d_ws,
                              size_t ws_size, hipStream_t stream) {
  u16* ws = (u16*)d_ws;
  const size_t QN = (size_t)NB * SQ * HD;
  for (int p = 0; p < 2; ++p) {
    const u16* x = (const u16*)d_in[p * 5 + 0];
    const u16* kc = (const u16*)d_in[p * 5 + 1];
    const u16* vc = (const u16*)d_in[p * 5 + 2];
    const u16* wq = (const u16*)d_in[p * 5 + 3];
    const u16* wo = (const u16*)d_in[p * 5 + 4];
    u16* Q = ws + (size_t)p * 2 * QN;
    u16* O = Q + QN;
    gemm_simple<<<dim3(HD / 64, NB * SQ / 64), 256, 0, stream>>>(x, DM, wq, HD, Q, HD, DM);
    attn_simple<<<dim3(9, NH, NB), 256, 0, stream>>>(Q, kc, vc, O);
    gemm_simple<<<dim3(DM / 64, NB * SQ / 64), 256, 0, stream>>>(O, HD, wo, DM, (u16*)d_out + p * DM, 2 * DM, HD);
  }
}
```

```cpp
#include <hip/hip_runtime.h>
#include <stdint.h>
typedef unsigned short u16;
constexpr unsigned LDS_BYTES = 163840;

__global__ __launch_bounds__(512, 2) void fused_kernel(const u16* xA, const u16* kA, const u16* vA, const u16* wqA,
                                                       const u16* xB, const u16* kB, const u16* vB, const u16* wqB, u16* Ows,
                                                       const u16* woA, const u16* woB, unsigned* flags, u16* out, float* slab) {
  asm volatile(
R"A(s_load_dwordx16 s[36:51],%0,0
s_load_dwordx2 s[52:53],%0,64
s_load_dwordx2 s[90:91],%0,88
s_mov_b32 s54,%1
v_mov_b32 v1,%2
s_waitcnt lgkmcnt(0)
s_load_dword s30,s[90:91],0x8000
v_lshrrev_b32 v1,6,v1
s_nop 0
v_readfirstlane_b32 s84,v1
s_mov_b32 s55,0x3f803f80
s_and_b32 s88,s54,7
s_lshr_b32 s89,s54,3
s_cmp_ge_u32 s88,4
s_cselect_b64 s[36:37],s[44:45],s[36:37]
s_cselect_b64 s[38:39],s[46:47],s[38:39]
s_cselect_b64 s[40:41],s[48:49],s[40:41]
s_cselect_b64 s[42:43],s[50:51],s[42:43]
s_cselect_b32 s90,0x1b00000,0
s_add_u32 s52,s52,s90
s_addc_u32 s53,s53,0
s_lshr_b32 s47,s84,2
s_lshl_b32 s90,s47,1
s_and_b32 s84,s84,3
s_xor_b32 s84,s84,s90
s_and_b32 s85,s89,3
s_lshl_b32 s85,s85,1
s_and_b32 s90,s88,1
s_add_u32 s85,s85,s90
s_lshl_b32 s85,s85,1
s_add_u32 s85,s85,s47
s_and_b32 s88,s88,3
s_lshl_b32 s86,s88,3
s_lshr_b32 s90,s89,2
s_add_u32 s86,s86,s90
s_mul_i32 s51,s47,36864
s_mul_i32 s48,s84,3072
s_add_u32 s48,s48,s51
s_add_u32 s49,s48,73728
s_lshl_b32 s50,s47,13
s_add_u32 s50,s50,147456
s_mov_b32 s87,0
s_mov_b32 s33,0
s_mov_b32 s35,0
s_mov_b32 s34,1
v_mbcnt_lo_u32_b32 v0,-1,0
v_mbcnt_hi_u32_b32 v0,-1,v0
v_lshrrev_b32 v1,3,v0
v_and_b32 v2,7,v0
s_lshl_b32 s88,s84,3
v_add_u32 v3,s88,v1
s_and_b32 s89,s84,1
v_and_b32 v0,2,v1
s_lshl_b32 s90,s89,2
v_or_b32 v0,s90,v0
v_xor_b32 v0,v0,v2
v_lshlrev_b32 v0,4,v0
s_movk_i32 s91,0x3000
v_mad_u32_u24 v253,v3,s91,v0
v_lshrrev_b32 v0,1,v1
v_or_b32 v0,s90,v0
v_xor_b32 v0,v0,v2
v_lshlrev_b32 v0,4,v0
s_movk_i32 s91,1280
v_mad_u32_u24 v254,v3,s91,v0
v_mbcnt_lo_u32_b32 v0,-1,0
v_mbcnt_hi_u32_b32 v0,-1,v0
v_lshrrev_b32 v1,3,v0
v_and_b32 v2,7,v0
s_lshl_b32 s88,s84,3
v_add_u32 v3,s88,v1
s_movk_i32 s91,384
v_mul_u32_u24 v3,s91,v3
s_and_b32 s89,s84,1
s_lshl_b32 s89,s89,2
v_lshrrev_b32 v1,1,v1
v_or_b32 v0,s89,v1
v_xor_b32 v0,v0,v2
v_lshl_add_u32 v251,v0,4,v3
v_lshlrev_b32 v1,1,v1
v_xor_b32 v1,v1,v2
v_lshl_add_u32 v252,v1,4,v3
s_mov_b32 s44,1
LCOLD:
s_mul_i32 s88,s85,184320
s_add_u32 s56,s36,s88
s_addc_u32 s57,s37,0
s_and_b32 s57,s57,0xffff
s_mov_b32 s58,0x2d000
s_mov_b32 s59,0x20000
s_mul_i32 s88,s86,384
s_sub_u32 s89,0x780000,s88
s_add_u32 s60,s42,s88
s_addc_u32 s61,s43,0
s_and_b32 s61,s61,0xffff
s_mov_b32 s62,s89
s_mov_b32 s63,0x20000
s_lshl_b32 s88,s85,5
s_add_u32 s88,s88,s86
s_mul_i32 s88,s88,196608
s_add_u32 s64,s38,s88
s_addc_u32 s65,s39,0
s_and_b32 s65,s65,0xffff
s_mov_b32 s66,0x30000
s_mov_b32 s67,0x20000
s_add_u32 s68,s40,s88
s_addc_u32 s69,s41,0
s_and_b32 s69,s69,0xffff
s_mov_b32 s70,0x30000
s_mov_b32 s71,0x20000
s_mul_i32 s88,s85,1728
s_mul_i32 s89,s86,6
s_add_u32 s88,s88,s89
s_lshl_b32 s88,s88,10
s_add_u32 s72,s52,s88
s_addc_u32 s73,s53,0
s_and_b32 s73,s73,0xffff
s_mov_b32 s74,0x181800
s_mov_b32 s75,0x20000
s_mov_b32 s96,s48
s_cmp_eq_u32 s47,0
s_cselect_b32 s94,4,1
s_mul_i32 s94,s94,393216
s_mov_b32 m0,s96
s_nop 0
buffer_load_dwordx4 v253,s[60:63],s94 offen lds
s_add_u32 m0,s96,896
s_nop 0
buffer_load_dwordx4 v253,s[60:63],s94 offen offset:128 lds
s_add_u32 m0,s96,1792
s_nop 0
buffer_load_dwordx4 v253,s[60:63],s94 offen offset:256 lds
s_add_u32 s96,s96,12288
s_cmp_eq_u32 s47,0
s_cselect_b32 s94,5,2
s_mul_i32 s94,s94,393216
s_mov_b32 m0,s96
s_nop 0
buffer_load_dwordx4 v253,s[60:63],s94 offen lds
s_add_u32 m0,s96,896
s_nop 0
buffer_load_dwordx4 v253,s[60:63],s94 offen offset:128 lds
s_add_u32 m0,s96,1792
s_nop 0
buffer_load_dwordx4 v253,s[60:63],s94 offen offset:256 lds
s_add_u32 s96,s96,12288
s_cmp_eq_u32 s47,0
s_cselect_b32 s94,0,3
s_mul_i32 s94,s94,393216
s_mov_b32 m0,s96
s_nop 0
buffer_load_dwordx4 v253,s[60:63],s94 offen lds
s_add_u32 m0,s96,896
s_nop 0
buffer_load_dwordx4 v253,s[60:63],s94 offen offset:128 lds
s_add_u32 m0,s96,1792
s_nop 0
buffer_load_dwordx4 v253,s[60:63],s94 offen offset:256 lds
s_add_u32 s96,s96,12288
s_lshl_b32 s97,s84,10
s_add_u32 s97,s97,s51
s_add_u32 s97,s97,73728
s_mov_b32 s95,0
s_mov_b32 m0,s97
s_mov_b32 s88,s95
buffer_load_dwordx4 v254,s[56:59],s88 offen lds
s_add_u32 m0,s97,4096
s_add_u32 s88,s95,40960
buffer_load_dwordx4 v254,s[56:59],s88 offen lds
s_add_u32 m0,s97,8192
s_add_u32 s88,s95,81920
buffer_load_dwordx4 v254,s[56:59],s88 offen lds
s_add_u32 m0,s97,12288
s_add_u32 s88,s95,122880
buffer_load_dwordx4 v254,s[56:59],s88 offen lds
s_add_u32 m0,s97,16384
s_add_u32 s88,s95,163840
s_cmp_ge_u32 s84,2
s_cbranch_scc1 LX1
buffer_load_dwordx4 v254,s[56:59],s88 offen lds
LX1:
s_waitcnt lgkmcnt(0)
s_add_u32 s30,s30,1
s_waitcnt vmcnt(0)
LITEM:
s_xor_b32 s90,s33,1
s_mul_i32 s90,s90,18432
s_lshl_b32 s97,s84,10
s_add_u32 s97,s97,s51
s_add_u32 s97,s97,73728
s_add_u32 s97,s97,s90
s_movk_i32 s95,128
v_mbcnt_lo_u32_b32 v0,-1,0
v_mbcnt_hi_u32_b32 v0,-1,v0
v_and_b32 v1,15,v0
v_lshrrev_b32 v2,4,v0
v_lshrrev_b32 v3,2,v1
s_movk_i32 s88,3072
v_mul_u32_u24 v164,s88,v2
v_lshl_add_u32 v164,v3,7,v164
v_and_b32 v165,3,v0
v_lshl_add_u32 v164,v165,3,v164
v_lshrrev_b32 v165,1,v3
v_and_b32 v166,1,v2
v_lshl_or_b32 v165,v166,1,v165
s_mul_i32 s88,s84,3
s_add_u32 s88,s88,0
s_and_b32 s89,s88,3
s_lshr_b32 s88,s88,2
s_lshl_b32 s88,s88,10
v_xor_b32 v160,s89,v165
v_lshl_add_u32 v160,v160,5,v164
v_add_u32 v160,s88,v160
s_mul_i32 s88,s84,3
s_add_u32 s88,s88,1
s_and_b32 s89,s88,3
s_lshr_b32 s88,s88,2
s_lshl_b32 s88,s88,10
v_xor_b32 v161,s89,v165
v_lshl_add_u32 v161,v161,5,v164
v_add_u32 v161,s88,v161
s_mul_i32 s88,s84,3
s_add_u32 s88,s88,2
s_and_b32 s89,s88,3
s_lshr_b32 s88,s88,2
s_lshl_b32 s88,s88,10
v_xor_b32 v162,s89,v165
v_lshl_add_u32 v162,v162,5,v164
v_add_u32 v162,s88,v162
s_mov_b32 s98,24576
v_add_u32 v164,s98,v160
v_add_u32 v165,s98,v161
v_add_u32 v166,s98,v162
v_lshrrev_b32 v3,3,v1
v_lshlrev_b32 v168,10,v3
v_and_b32 v3,7,v1
v_lshl_add_u32 v168,v3,7,v168
v_lshrrev_b32 v3,1,v1
v_xor_b32 v3,v3,v2
v_lshl_add_u32 v168,v3,4,v168
s_mul_i32 s88,s33,18432
s_add_u32 s88,s88,s51
s_add_u32 s88,s88,73728
v_add_u32 v168,s88,v168
v_xor_b32 v169,64,v168
s_cmp_eq_u32 s33,0
s_movk_i32 s88,18432
s_cselect_b32 s46,s88,0xffffb800
s_sub_u32 s76,s48,s51
s_mul_i32 s88,s47,12288
s_add_u32 s76,s76,s88
s_add_u32 s96,s76,24576
s_add_u32 s77,s76,73728
s_add_u32 s94,s47,6
s_mul_i32 s94,s94,393216
s_waitcnt vmcnt(12) lgkmcnt(0)
s_barrier
ds_read_b64_tr_b16 v[112:113],v164
ds_read_b64_tr_b16 v[114:115],v164 offset:512
ds_read_b128 v[148:151],v168
ds_read_b64_tr_b16 v[116:117],v165
ds_read_b64_tr_b16 v[118:119],v165 offset:512
ds_read_b64_tr_b16 v[120:121],v166
ds_read_b64_tr_b16 v[122:123],v166 offset:512
ds_read_b128 v[152:155],v168 offset:2048
ds_read_b128 v[156:159],v168 offset:4096
ds_read_b128 v[136:139],v168 offset:6144
ds_read_b128 v[140:143],v168 offset:8192
ds_read_b128 v[144:147],v168 offset:10240
s_waitcnt lgkmcnt(9)
v_mfma_f32_16x16x32_bf16 v[4:7],v[112:115],v[148:151],0
s_mov_b32 m0,s97
s_mov_b32 s88,s95
s_waitcnt lgkmcnt(7)
v_mfma_f32_16x16x32_bf16 v[40:43],v[116:119],v[148:151],0
buffer_load_dwordx4 v254,s[56:59],s88 offen lds
s_waitcnt lgkmcnt(5)
v_mfma_f32_16x16x32_bf16 v[76:79],v[120:123],v[148:151],0
ds_read_b128 v[148:151],v168 offset:12288
ds_read_b64_tr_b16 v[124:125],v164 offset:12288
s_add_u32 m0,s97,4096
s_add_u32 s88,s95,40960
s_waitcnt lgkmcnt(6)
v_mfma_f32_16x16x32_bf16 v[8:11],v[112:115],v[152:155],0
buffer_load_dwordx4 v254,s[56:59],s88 offen lds
v_mfma_f32_16x16x32_bf16 v[44:47],v[116:119],v[152:155],0
ds_read_b64_tr_b16 v[126:127],v164 offset:12800
s_add_u32 m0,s97,8192
s_add_u32 s88,s95,81920
v_mfma_f32_16x16x32_bf16 v[80:83],v[120:123],v[152:155],0
ds_read_b128 v[152:155],v168 offset:14336
buffer_load_dwordx4 v254,s[56:59],s88 offen lds
s_waitcnt lgkmcnt(7)
v_mfma_f32_16x16x32_bf16 v[12:15],v[112:115],v[156:159],0
ds_read_b64_tr_b16 v[128:129],v165 offset:12288
s_add_u32 m0,s97,12288
s_add_u32 s88,s95,122880
v_mfma_f32_16x16x32_bf16 v[48:51],v[116:119],v[156:159],0
buffer_load_dwordx4 v254,s[56:59],s88 offen lds
v_mfma_f32_16x16x32_bf16 v[84:87],v[120:123],v[156:159],0
ds_read_b128 v[156:159],v168 offset:16384
ds_read_b64_tr_b16 v[130:131],v165 offset:12800
s_add_u32 m0,s97,16384
s_add_u32 s88,s95,163840
s_waitcnt lgkmcnt(9)
v_mfma_f32_16x16x32_bf16 v[16:19],v[112:115],v[136:139],0
s_cmp_ge_u32 s84,2
s_cbranch_scc1 LX2
buffer_load_dwordx4 v254,s[56:59],s88 offen lds
LX2:
v_mfma_f32_16x16x32_bf16 v[52:55],v[116:119],v[136:139],0
ds_read_b64_tr_b16 v[132:133],v166 offset:12288
v_mfma_f32_16x16x32_bf16 v[88:91],v[120:123],v[136:139],0
ds_read_b128 v[136:139],v169
s_waitcnt lgkmcnt(10)
v_mfma_f32_16x16x32_bf16 v[20:23],v[112:115],v[140:143],0
ds_read_b64_tr_b16 v[134:135],v166 offset:12800
v_mfma_f32_16x16x32_bf16 v[56:59],v[116:119],v[140:143],0
v_mfma_f32_16x16x32_bf16 v[92:95],v[120:123],v[140:143],0
ds_read_b128 v[140:143],v169 offset:2048
s_waitcnt lgkmcnt(11)
v_mfma_f32_16x16x32_bf16 v[24:27],v[112:115],v[144:147],0
v_mfma_f32_16x16x32_bf16 v[60:63],v[116:119],v[144:147],0
v_mfma_f32_16x16x32_bf16 v[96:99],v[120:123],v[144:147],0
ds_read_b128 v[144:147],v169 offset:4096
s_waitcnt lgkmcnt(11)
v_mfma_f32_16x16x32_bf16 v[28:31],v[112:115],v[148:151],0
v_mfma_f32_16x16x32_bf16 v[64:67],v[116:119],v[148:151],0
v_mfma_f32_16x16x32_bf16 v[100:103],v[120:123],v[148:151],0
ds_read_b128 v[148:151],v169 offset:6144
s_waitcnt lgkmcnt(9)
v_mfma_f32_16x16x32_bf16 v[32:35],v[112:115],v[152:155],0
v_mfma_f32_16x16x32_bf16 v[68:71],v[116:119],v[152:155],0
v_mfma_f32_16x16x32_bf16 v[104:107],v[120:123],v[152:155],0
ds_read_b128 v[152:155],v169 offset:8192
s_waitcnt lgkmcnt(8)
v_mfma_f32_16x16x32_bf16 v[36:39],v[112:115],v[156:159],0
v_mfma_f32_16x16x32_bf16 v[72:75],v[116:119],v[156:159],0
v_mfma_f32_16x16x32_bf16 v[108:111],v[120:123],v[156:159],0
ds_read_b128 v[156:159],v169 offset:10240
s_waitcnt lgkmcnt(6)
v_mfma_f32_16x16x32_bf16 v[4:7],v[124:127],v[136:139],v[4:7]
v_mfma_f32_16x16x32_bf16 v[40:43],v[128:131],v[136:139],v[40:43]
s_waitcnt lgkmcnt(5)
v_mfma_f32_16x16x32_bf16 v[76:79],v[132:135],v[136:139],v[76:79]
ds_read_b128 v[136:139],v169 offset:12288
s_waitcnt lgkmcnt(5)
v_mfma_f32_16x16x32_bf16 v[8:11],v[124:127],v[140:143],v[8:11]
v_mfma_f32_16x16x32_bf16 v[44:47],v[128:131],v[140:143],v[44:47]
v_mfma_f32_16x16x32_bf16 v[80:83],v[132:135],v[140:143],v[80:83]
ds_read_b128 v[140:143],v169 offset:14336
s_waitcnt lgkmcnt(5)
v_mfma_f32_16x16x32_bf16 v[12:15],v[124:127],v[144:147],v[12:15]
v_mfma_f32_16x16x32_bf16 v[48:51],v[128:131],v[144:147],v[48:51]
v_mfma_f32_16x16x32_bf16 v[84:87],v[132:135],v[144:147],v[84:87]
ds_read_b128 v[144:147],v169 offset:16384
s_waitcnt lgkmcnt(5)
v_mfma_f32_16x16x32_bf16 v[16:19],v[124:127],v[148:151],v[16:19]
s_add_u32 s98,s98,24576
s_cmp_eq_u32 s98,73728
s_cmov_b32 s98,0
v_mfma_f32_16x16x32_bf16 v[52:55],v[128:131],v[148:151],v[52:55]
v_add_u32 v164,s98,v160
v_mfma_f32_16x16x32_bf16 v[88:91],v[132:135],v[148:151],v[88:91]
v_add_u32 v165,s98,v161
s_waitcnt lgkmcnt(4)
v_mfma_f32_16x16x32_bf16 v[20:23],v[124:127],v[152:155],v[20:23]
v_add_u32 v166,s98,v162
v_mfma_f32_16x16x32_bf16 v[56:59],v[128:131],v[152:155],v[56:59]
s_add_u32 s95,s95,128
s_sub_u32 s97,s97,s46
v_mfma_f32_16x16x32_bf16 v[92:95],v[132:135],v[152:155],v[92:95]
v_add_u32 v168,s46,v168
s_waitcnt lgkmcnt(3)
v_mfma_f32_16x16x32_bf16 v[24:27],v[124:127],v[156:159],v[24:27]
v_add_u32 v169,s46,v169
v_mfma_f32_16x16x32_bf16 v[60:63],v[128:131],v[156:159],v[60:63]
s_sub_u32 s46,0,s46
v_mfma_f32_16x16x32_bf16 v[96:99],v[132:135],v[156:159],v[96:99]
s_mov_b32 s45,7
s_waitcnt vmcnt(0)
s_cmp_lg_u32 s87,1
s_cbranch_scc1 LNOWQ
s_lshl_b32 s88,s54,1
s_add_u32 s88,s88,s47
s_lshl_b32 s88,s88,2
s_add_u32 s88,s88,s84
s_and_b32 s89,s88,1023
s_mul_i32 s89,s89,7680
v_mbcnt_lo_u32_b32 v0,-1,0
v_mbcnt_hi_u32_b32 v0,-1,v0
v_lshlrev_b32 v0,7,v0
s_cmp_ge_u32 s88,1024
s_cselect_b32 s90,8,0
s_add_u32 s90,s90,72
s_load_dwordx2 s[90:91],%0,s90
s_waitcnt lgkmcnt(0)
s_add_u32 s64,s90,s89
s_addc_u32 s65,s91,0
s_and_b32 s65,s65,0xffff
)A"
R"A(s_mov_b32 s66,0x1e00
s_mov_b32 s67,0x20000
buffer_load_dword v202,v0,s[64:67],0 offen
s_mul_i32 s88,s85,184320
s_add_u32 s56,s36,s88
s_addc_u32 s57,s37,0
s_and_b32 s57,s57,0xffff
s_mov_b32 s58,0x2d000
s_mov_b32 s59,0x20000
s_mul_i32 s88,s86,384
s_sub_u32 s89,0x780000,s88
s_add_u32 s60,s42,s88
s_addc_u32 s61,s43,0
s_and_b32 s61,s61,0xffff
s_mov_b32 s62,s89
s_mov_b32 s63,0x20000
s_lshl_b32 s88,s85,5
s_add_u32 s88,s88,s86
s_mul_i32 s88,s88,196608
s_add_u32 s64,s38,s88
s_addc_u32 s65,s39,0
s_and_b32 s65,s65,0xffff
s_mov_b32 s66,0x30000
s_mov_b32 s67,0x20000
s_add_u32 s68,s40,s88
s_addc_u32 s69,s41,0
s_and_b32 s69,s69,0xffff
s_mov_b32 s70,0x30000
s_mov_b32 s71,0x20000
s_mul_i32 s88,s85,1728
s_mul_i32 s89,s86,6
s_add_u32 s88,s88,s89
s_lshl_b32 s88,s88,10
s_add_u32 s72,s52,s88
s_addc_u32 s73,s53,0
s_and_b32 s73,s73,0xffff
s_mov_b32 s74,0x181800
s_mov_b32 s75,0x20000
LNOWQ:
LQLOOP:
s_waitcnt vmcnt(3) lgkmcnt(0)
s_barrier
ds_read_b64_tr_b16 v[112:113],v164
ds_read_b64_tr_b16 v[114:115],v164 offset:512
ds_read_b128 v[148:151],v168
v_mfma_f32_16x16x32_bf16 v[28:31],v[124:127],v[136:139],v[28:31]
ds_read_b64_tr_b16 v[116:117],v165
s_mov_b32 m0,s97
s_mov_b32 s88,s95
v_mfma_f32_16x16x32_bf16 v[64:67],v[128:131],v[136:139],v[64:67]
ds_read_b64_tr_b16 v[118:119],v165 offset:512
buffer_load_dwordx4 v254,s[56:59],s88 offen lds
v_mfma_f32_16x16x32_bf16 v[100:103],v[132:135],v[136:139],v[100:103]
ds_read_b64_tr_b16 v[120:121],v166
s_add_u32 m0,s97,4096
s_add_u32 s88,s95,40960
v_mfma_f32_16x16x32_bf16 v[32:35],v[124:127],v[140:143],v[32:35]
ds_read_b64_tr_b16 v[122:123],v166 offset:512
buffer_load_dwordx4 v254,s[56:59],s88 offen lds
v_mfma_f32_16x16x32_bf16 v[68:71],v[128:131],v[140:143],v[68:71]
ds_read_b128 v[152:155],v168 offset:2048
s_add_u32 m0,s97,8192
s_add_u32 s88,s95,81920
v_mfma_f32_16x16x32_bf16 v[104:107],v[132:135],v[140:143],v[104:107]
ds_read_b128 v[156:159],v168 offset:4096
ds_read_b128 v[136:139],v168 offset:6144
buffer_load_dwordx4 v254,s[56:59],s88 offen lds
v_mfma_f32_16x16x32_bf16 v[36:39],v[124:127],v[144:147],v[36:39]
ds_read_b128 v[140:143],v168 offset:8192
s_add_u32 m0,s97,12288
s_add_u32 s88,s95,122880
v_mfma_f32_16x16x32_bf16 v[72:75],v[128:131],v[144:147],v[72:75]
buffer_load_dwordx4 v254,s[56:59],s88 offen lds
v_mfma_f32_16x16x32_bf16 v[108:111],v[132:135],v[144:147],v[108:111]
ds_read_b128 v[144:147],v168 offset:10240
s_add_u32 m0,s97,16384
s_add_u32 s88,s95,163840
s_waitcnt lgkmcnt(9)
v_mfma_f32_16x16x32_bf16 v[4:7],v[112:115],v[148:151],v[4:7]
s_cmp_ge_u32 s84,2
s_cbranch_scc1 LX3
buffer_load_dwordx4 v254,s[56:59],s88 offen lds
LX3:
s_waitcnt lgkmcnt(7)
v_mfma_f32_16x16x32_bf16 v[40:43],v[116:119],v[148:151],v[40:43]
s_mov_b32 m0,s96
s_waitcnt lgkmcnt(5)
v_mfma_f32_16x16x32_bf16 v[76:79],v[120:123],v[148:151],v[76:79]
ds_read_b128 v[148:151],v168 offset:12288
ds_read_b64_tr_b16 v[124:125],v164 offset:12288
buffer_load_dwordx4 v253,s[60:63],s94 offen lds
s_waitcnt lgkmcnt(6)
v_mfma_f32_16x16x32_bf16 v[8:11],v[112:115],v[152:155],v[8:11]
s_add_u32 m0,s96,896
v_mfma_f32_16x16x32_bf16 v[44:47],v[116:119],v[152:155],v[44:47]
ds_read_b64_tr_b16 v[126:127],v164 offset:12800
buffer_load_dwordx4 v253,s[60:63],s94 offen offset:128 lds
v_mfma_f32_16x16x32_bf16 v[80:83],v[120:123],v[152:155],v[80:83]
ds_read_b128 v[152:155],v168 offset:14336
s_add_u32 m0,s96,1792
s_waitcnt lgkmcnt(7)
v_mfma_f32_16x16x32_bf16 v[12:15],v[112:115],v[156:159],v[12:15]
ds_read_b64_tr_b16 v[128:129],v165 offset:12288
buffer_load_dwordx4 v253,s[60:63],s94 offen offset:256 lds
v_mfma_f32_16x16x32_bf16 v[48:51],v[116:119],v[156:159],v[48:51]
v_mfma_f32_16x16x32_bf16 v[84:87],v[120:123],v[156:159],v[84:87]
ds_read_b128 v[156:159],v168 offset:16384
ds_read_b64_tr_b16 v[130:131],v165 offset:12800
s_waitcnt lgkmcnt(9)
v_mfma_f32_16x16x32_bf16 v[16:19],v[112:115],v[136:139],v[16:19]
v_mfma_f32_16x16x32_bf16 v[52:55],v[116:119],v[136:139],v[52:55]
ds_read_b64_tr_b16 v[132:133],v166 offset:12288
v_mfma_f32_16x16x32_bf16 v[88:91],v[120:123],v[136:139],v[88:91]
ds_read_b128 v[136:139],v169
s_waitcnt lgkmcnt(10)
v_mfma_f32_16x16x32_bf16 v[20:23],v[112:115],v[140:143],v[20:23]
ds_read_b64_tr_b16 v[134:135],v166 offset:12800
v_mfma_f32_16x16x32_bf16 v[56:59],v[116:119],v[140:143],v[56:59]
v_mfma_f32_16x16x32_bf16 v[92:95],v[120:123],v[140:143],v[92:95]
ds_read_b128 v[140:143],v169 offset:2048
s_waitcnt lgkmcnt(11)
v_mfma_f32_16x16x32_bf16 v[24:27],v[112:115],v[144:147],v[24:27]
v_mfma_f32_16x16x32_bf16 v[60:63],v[116:119],v[144:147],v[60:63]
v_mfma_f32_16x16x32_bf16 v[96:99],v[120:123],v[144:147],v[96:99]
ds_read_b128 v[144:147],v169 offset:4096
s_waitcnt lgkmcnt(11)
v_mfma_f32_16x16x32_bf16 v[28:31],v[112:115],v[148:151],v[28:31]
v_mfma_f32_16x16x32_bf16 v[64:67],v[116:119],v[148:151],v[64:67]
v_mfma_f32_16x16x32_bf16 v[100:103],v[120:123],v[148:151],v[100:103]
ds_read_b128 v[148:151],v169 offset:6144
s_waitcnt lgkmcnt(9)
v_mfma_f32_16x16x32_bf16 v[32:35],v[112:115],v[152:155],v[32:35]
v_mfma_f32_16x16x32_bf16 v[68:71],v[116:119],v[152:155],v[68:71]
v_mfma_f32_16x16x32_bf16 v[104:107],v[120:123],v[152:155],v[104:107]
ds_read_b128 v[152:155],v169 offset:8192
s_waitcnt lgkmcnt(8)
v_mfma_f32_16x16x32_bf16 v[36:39],v[112:115],v[156:159],v[36:39]
v_mfma_f32_16x16x32_bf16 v[72:75],v[116:119],v[156:159],v[72:75]
v_mfma_f32_16x16x32_bf16 v[108:111],v[120:123],v[156:159],v[108:111]
ds_read_b128 v[156:159],v169 offset:10240
s_waitcnt lgkmcnt(6)
v_mfma_f32_16x16x32_bf16 v[4:7],v[124:127],v[136:139],v[4:7]
v_mfma_f32_16x16x32_bf16 v[40:43],v[128:131],v[136:139],v[40:43]
s_waitcnt lgkmcnt(5)
v_mfma_f32_16x16x32_bf16 v[76:79],v[132:135],v[136:139],v[76:79]
ds_read_b128 v[136:139],v169 offset:12288
s_waitcnt lgkmcnt(5)
v_mfma_f32_16x16x32_bf16 v[8:11],v[124:127],v[140:143],v[8:11]
v_mfma_f32_16x16x32_bf16 v[44:47],v[128:131],v[140:143],v[44:47]
v_mfma_f32_16x16x32_bf16 v[80:83],v[132:135],v[140:143],v[80:83]
ds_read_b128 v[140:143],v169 offset:14336
s_waitcnt lgkmcnt(5)
v_mfma_f32_16x16x32_bf16 v[12:15],v[124:127],v[144:147],v[12:15]
v_mfma_f32_16x16x32_bf16 v[48:51],v[128:131],v[144:147],v[48:51]
v_mfma_f32_16x16x32_bf16 v[84:87],v[132:135],v[144:147],v[84:87]
ds_read_b128 v[144:147],v169 offset:16384
s_waitcnt lgkmcnt(5)
v_mfma_f32_16x16x32_bf16 v[16:19],v[124:127],v[148:151],v[16:19]
s_add_u32 s98,s98,24576
s_cmp_eq_u32 s98,73728
s_cmov_b32 s98,0
v_mfma_f32_16x16x32_bf16 v[52:55],v[128:131],v[148:151],v[52:55]
v_add_u32 v164,s98,v160
v_mfma_f32_16x16x32_bf16 v[88:91],v[132:135],v[148:151],v[88:91]
v_add_u32 v165,s98,v161
s_waitcnt lgkmcnt(4)
v_mfma_f32_16x16x32_bf16 v[20:23],v[124:127],v[152:155],v[20:23]
v_add_u32 v166,s98,v162
v_mfma_f32_16x16x32_bf16 v[56:59],v[128:131],v[152:155],v[56:59]
s_add_u32 s95,s95,128
s_sub_u32 s97,s97,s46
v_mfma_f32_16x16x32_bf16 v[92:95],v[132:135],v[152:155],v[92:95]
v_add_u32 v168,s46,v168
s_waitcnt lgkmcnt(3)
v_mfma_f32_16x16x32_bf16 v[24:27],v[124:127],v[156:159],v[24:27]
v_add_u32 v169,s46,v169
v_mfma_f32_16x16x32_bf16 v[60:63],v[128:131],v[156:159],v[60:63]
s_sub_u32 s46,0,s46
s_add_u32 s94,s94,786432
s_add_u32 s96,s96,24576
s_cmp_eq_u32 s96,s77
s_cselect_b32 s96,s76,s96
s_sub_u32 s45,s45,1
v_mfma_f32_16x16x32_bf16 v[96:99],v[132:135],v[156:159],v[96:99]
s_cmp_lg_u32 s45,0
s_cbranch_scc1 LQLOOP
s_cmp_eq_u32 s87,0
s_cbranch_scc1 LNOSIG0
s_or_b32 s88,s84,s47
s_cbranch_scc1 LNOSIG0
s_lshl_b32 s88,s54,6
s_load_dwordx2 s[90:91],%0,88
s_waitcnt lgkmcnt(0)
s_add_u32 s76,s90,s88
s_addc_u32 s77,s91,0
s_and_b32 s77,s77,0xffff
s_mov_b32 s78,0x40
s_mov_b32 s79,0x20000
v_mov_b32 v0,s30
v_mov_b32 v1,0
s_mov_b64 exec,1
buffer_store_dword v0,v1,s[76:79],0 offen sc1
s_mov_b64 exec,-1
LNOSIG0:
s_waitcnt vmcnt(3) lgkmcnt(0)
s_barrier
ds_read_b64_tr_b16 v[112:113],v164
ds_read_b64_tr_b16 v[114:115],v164 offset:512
ds_read_b128 v[148:151],v168
v_mfma_f32_16x16x32_bf16 v[28:31],v[124:127],v[136:139],v[28:31]
ds_read_b64_tr_b16 v[116:117],v165
s_mov_b32 m0,s97
s_mov_b32 s88,s95
v_mfma_f32_16x16x32_bf16 v[64:67],v[128:131],v[136:139],v[64:67]
ds_read_b64_tr_b16 v[118:119],v165 offset:512
buffer_load_dwordx4 v254,s[56:59],s88 offen lds
v_mfma_f32_16x16x32_bf16 v[100:103],v[132:135],v[136:139],v[100:103]
ds_read_b64_tr_b16 v[120:121],v166
s_add_u32 m0,s97,4096
s_add_u32 s88,s95,40960
v_mfma_f32_16x16x32_bf16 v[32:35],v[124:127],v[140:143],v[32:35]
ds_read_b64_tr_b16 v[122:123],v166 offset:512
buffer_load_dwordx4 v254,s[56:59],s88 offen lds
v_mfma_f32_16x16x32_bf16 v[68:71],v[128:131],v[140:143],v[68:71]
ds_read_b128 v[152:155],v168 offset:2048
s_add_u32 m0,s97,8192
s_add_u32 s88,s95,81920
v_mfma_f32_16x16x32_bf16 v[104:107],v[132:135],v[140:143],v[104:107]
ds_read_b128 v[156:159],v168 offset:4096
ds_read_b128 v[136:139],v168 offset:6144
buffer_load_dwordx4 v254,s[56:59],s88 offen lds
v_mfma_f32_16x16x32_bf16 v[36:39],v[124:127],v[144:147],v[36:39]
ds_read_b128 v[140:143],v168 offset:8192
s_add_u32 m0,s97,12288
s_add_u32 s88,s95,122880
v_mfma_f32_16x16x32_bf16 v[72:75],v[128:131],v[144:147],v[72:75]
buffer_load_dwordx4 v254,s[56:59],s88 offen lds
v_mfma_f32_16x16x32_bf16 v[108:111],v[132:135],v[144:147],v[108:111]
ds_read_b128 v[144:147],v168 offset:10240
s_add_u32 m0,s97,16384
s_add_u32 s88,s95,163840
s_waitcnt lgkmcnt(9)
v_mfma_f32_16x16x32_bf16 v[4:7],v[112:115],v[148:151],v[4:7]
s_cmp_ge_u32 s84,2
s_cbranch_scc1 LX4
buffer_load_dwordx4 v254,s[56:59],s88 offen lds
LX4:
s_waitcnt lgkmcnt(7)
v_mfma_f32_16x16x32_bf16 v[40:43],v[116:119],v[148:151],v[40:43]
s_waitcnt lgkmcnt(5)
v_mfma_f32_16x16x32_bf16 v[76:79],v[120:123],v[148:151],v[76:79]
ds_read_b128 v[148:151],v168 offset:12288
ds_read_b64_tr_b16 v[124:125],v164 offset:12288
s_waitcnt lgkmcnt(6)
v_mfma_f32_16x16x32_bf16 v[8:11],v[112:115],v[152:155],v[8:11]
v_mfma_f32_16x16x32_bf16 v[44:47],v[116:119],v[152:155],v[44:47]
ds_read_b64_tr_b16 v[126:127],v164 offset:12800
v_mfma_f32_16x16x32_bf16 v[80:83],v[120:123],v[152:155],v[80:83]
ds_read_b128 v[152:155],v168 offset:14336
s_waitcnt lgkmcnt(7)
v_mfma_f32_16x16x32_bf16 v[12:15],v[112:115],v[156:159],v[12:15]
ds_read_b64_tr_b16 v[128:129],v165 offset:12288
v_mfma_f32_16x16x32_bf16 v[48:51],v[116:119],v[156:159],v[48:51]
v_mfma_f32_16x16x32_bf16 v[84:87],v[120:123],v[156:159],v[84:87]
ds_read_b128 v[156:159],v168 offset:16384
ds_read_b64_tr_b16 v[130:131],v165 offset:12800
s_waitcnt lgkmcnt(9)
v_mfma_f32_16x16x32_bf16 v[16:19],v[112:115],v[136:139],v[16:19]
v_mfma_f32_16x16x32_bf16 v[52:55],v[116:119],v[136:139],v[52:55]
ds_read_b64_tr_b16 v[132:133],v166 offset:12288
v_mfma_f32_16x16x32_bf16 v[88:91],v[120:123],v[136:139],v[88:91]
ds_read_b128 v[136:139],v169
s_waitcnt lgkmcnt(10)
v_mfma_f32_16x16x32_bf16 v[20:23],v[112:115],v[140:143],v[20:23]
ds_read_b64_tr_b16 v[134:135],v166 offset:12800
v_mfma_f32_16x16x32_bf16 v[56:59],v[116:119],v[140:143],v[56:59]
v_mfma_f32_16x16x32_bf16 v[92:95],v[120:123],v[140:143],v[92:95]
ds_read_b128 v[140:143],v169 offset:2048
s_waitcnt lgkmcnt(11)
v_mfma_f32_16x16x32_bf16 v[24:27],v[112:115],v[144:147],v[24:27]
v_mfma_f32_16x16x32_bf16 v[60:63],v[116:119],v[144:147],v[60:63]
v_mfma_f32_16x16x32_bf16 v[96:99],v[120:123],v[144:147],v[96:99]
ds_read_b128 v[144:147],v169 offset:4096
s_waitcnt lgkmcnt(11)
v_mfma_f32_16x16x32_bf16 v[28:31],v[112:115],v[148:151],v[28:31]
v_mfma_f32_16x16x32_bf16 v[64:67],v[116:119],v[148:151],v[64:67]
v_mfma_f32_16x16x32_bf16 v[100:103],v[120:123],v[148:151],v[100:103]
ds_read_b128 v[148:151],v169 offset:6144
s_waitcnt lgkmcnt(9)
v_mfma_f32_16x16x32_bf16 v[32:35],v[112:115],v[152:155],v[32:35]
v_mfma_f32_16x16x32_bf16 v[68:71],v[116:119],v[152:155],v[68:71]
v_mfma_f32_16x16x32_bf16 v[104:107],v[120:123],v[152:155],v[104:107]
ds_read_b128 v[152:155],v169 offset:8192
s_waitcnt lgkmcnt(8)
v_mfma_f32_16x16x32_bf16 v[36:39],v[112:115],v[156:159],v[36:39]
v_mfma_f32_16x16x32_bf16 v[72:75],v[116:119],v[156:159],v[72:75]
v_mfma_f32_16x16x32_bf16 v[108:111],v[120:123],v[156:159],v[108:111]
ds_read_b128 v[156:159],v169 offset:10240
s_waitcnt lgkmcnt(6)
v_mfma_f32_16x16x32_bf16 v[4:7],v[124:127],v[136:139],v[4:7]
v_mfma_f32_16x16x32_bf16 v[40:43],v[128:131],v[136:139],v[40:43]
s_waitcnt lgkmcnt(5)
v_mfma_f32_16x16x32_bf16 v[76:79],v[132:135],v[136:139],v[76:79]
ds_read_b128 v[136:139],v169 offset:12288
s_waitcnt lgkmcnt(5)
v_mfma_f32_16x16x32_bf16 v[8:11],v[124:127],v[140:143],v[8:11]
v_mfma_f32_16x16x32_bf16 v[44:47],v[128:131],v[140:143],v[44:47]
v_mfma_f32_16x16x32_bf16 v[80:83],v[132:135],v[140:143],v[80:83]
ds_read_b128 v[140:143],v169 offset:14336
s_waitcnt lgkmcnt(5)
v_mfma_f32_16x16x32_bf16 v[12:15],v[124:127],v[144:147],v[12:15]
v_mfma_f32_16x16x32_bf16 v[48:51],v[128:131],v[144:147],v[48:51]
v_mfma_f32_16x16x32_bf16 v[84:87],v[132:135],v[144:147],v[84:87]
ds_read_b128 v[144:147],v169 offset:16384
s_waitcnt lgkmcnt(5)
v_mfma_f32_16x16x32_bf16 v[16:19],v[124:127],v[148:151],v[16:19]
s_add_u32 s98,s98,24576
s_cmp_eq_u32 s98,73728
s_cmov_b32 s98,0
v_mfma_f32_16x16x32_bf16 v[52:55],v[128:131],v[148:151],v[52:55]
v_add_u32 v164,s98,v160
v_mfma_f32_16x16x32_bf16 v[88:91],v[132:135],v[148:151],v[88:91]
v_add_u32 v165,s98,v161
s_waitcnt lgkmcnt(4)
v_mfma_f32_16x16x32_bf16 v[20:23],v[124:127],v[152:155],v[20:23]
v_add_u32 v166,s98,v162
v_mfma_f32_16x16x32_bf16 v[56:59],v[128:131],v[152:155],v[56:59]
s_add_u32 s95,s95,128
s_sub_u32 s97,s97,s46
v_mfma_f32_16x16x32_bf16 v[92:95],v[132:135],v[152:155],v[92:95]
v_add_u32 v168,s46,v168
s_waitcnt lgkmcnt(3)
v_mfma_f32_16x16x32_bf16 v[24:27],v[124:127],v[156:159],v[24:27]
v_add_u32 v169,s46,v169
v_mfma_f32_16x16x32_bf16 v[60:63],v[128:131],v[156:159],v[60:63]
s_sub_u32 s46,0,s46
v_mfma_f32_16x16x32_bf16 v[96:99],v[132:135],v[156:159],v[96:99]
s_mov_b32 s94,0
s_mov_b32 s95,0
s_mul_i32 s98,s47,12288
s_add_u32 s96,s48,s98
s_mul_i32 s88,s35,12288
s_add_u32 s97,s88,s49
s_waitcnt vmcnt(0) lgkmcnt(0)
s_barrier
ds_read_b64_tr_b16 v[112:113],v164
ds_read_b64_tr_b16 v[114:115],v164 offset:512
ds_read_b128 v[148:151],v168
v_mfma_f32_16x16x32_bf16 v[28:31],v[124:127],v[136:139],v[28:31]
ds_read_b64_tr_b16 v[116:117],v165
s_mov_b32 m0,s96
v_mfma_f32_16x16x32_bf16 v[64:67],v[128:131],v[136:139],v[64:67]
ds_read_b64_tr_b16 v[118:119],v165 offset:512
s_cmp_eq_u32 s34,4
s_cbranch_scc1 LG5
buffer_load_dwordx4 v251,s[64:67],s94 offen nt lds
LG5:
v_mfma_f32_16x16x32_bf16 v[100:103],v[132:135],v[136:139],v[100:103]
ds_read_b64_tr_b16 v[120:121],v166
s_add_u32 m0,s96,896
v_mfma_f32_16x16x32_bf16 v[32:35],v[124:127],v[140:143],v[32:35]
ds_read_b64_tr_b16 v[122:123],v166 offset:512
s_cmp_eq_u32 s34,4
s_cbranch_scc1 LG6
buffer_load_dwordx4 v251,s[64:67],s94 offen offset:128 nt lds
LG6:
v_mfma_f32_16x16x32_bf16 v[68:71],v[128:131],v[140:143],v[68:71]
ds_read_b128 v[152:155],v168 offset:2048
s_add_u32 m0,s96,1792
v_mfma_f32_16x16x32_bf16 v[104:107],v[132:135],v[140:143],v[104:107]
ds_read_b128 v[156:159],v168 offset:4096
ds_read_b128 v[136:139],v168 offset:6144
s_cmp_eq_u32 s34,4
s_cbranch_scc1 LG7
buffer_load_dwordx4 v251,s[64:67],s94 offen offset:256 nt lds
LG7:
v_mfma_f32_16x16x32_bf16 v[36:39],v[124:127],v[144:147],v[36:39]
ds_read_b128 v[140:143],v168 offset:8192
s_mov_b32 m0,s97
v_mfma_f32_16x16x32_bf16 v[72:75],v[128:131],v[144:147],v[72:75]
s_cmp_eq_u32 s34,4
s_cbranch_scc1 LG8
buffer_load_dwordx4 v252,s[68:71],s95 offen nt lds
LG8:
v_mfma_f32_16x16x32_bf16 v[108:111],v[132:135],v[144:147],v[108:111]
ds_read_b128 v[144:147],v168 offset:10240
s_add_u32 m0,s97,896
s_waitcnt lgkmcnt(9)
v_mfma_f32_16x16x32_bf16 v[4:7],v[112:115],v[148:151],v[4:7]
s_cmp_eq_u32 s34,4
s_cbranch_scc1 LG9
buffer_load_dwordx4 v252,s[68:71],s95 offen offset:128 nt lds
LG9:
s_waitcnt lgkmcnt(7)
v_mfma_f32_16x16x32_bf16 v[40:43],v[116:119],v[148:151],v[40:43]
s_add_u32 m0,s97,1792
s_waitcnt lgkmcnt(5)
v_mfma_f32_16x16x32_bf16 v[76:79],v[120:123],v[148:151],v[76:79]
ds_read_b128 v[148:151],v168 offset:12288
ds_read_b64_tr_b16 v[124:125],v164 offset:12288
)A"
R"A(s_cmp_eq_u32 s34,4
s_cbranch_scc1 LG10
buffer_load_dwordx4 v252,s[68:71],s95 offen offset:256 nt lds
LG10:
s_waitcnt lgkmcnt(6)
v_mfma_f32_16x16x32_bf16 v[8:11],v[112:115],v[152:155],v[8:11]
v_mfma_f32_16x16x32_bf16 v[44:47],v[116:119],v[152:155],v[44:47]
ds_read_b64_tr_b16 v[126:127],v164 offset:12800
v_mfma_f32_16x16x32_bf16 v[80:83],v[120:123],v[152:155],v[80:83]
ds_read_b128 v[152:155],v168 offset:14336
s_waitcnt lgkmcnt(7)
v_mfma_f32_16x16x32_bf16 v[12:15],v[112:115],v[156:159],v[12:15]
ds_read_b64_tr_b16 v[128:129],v165 offset:12288
v_mfma_f32_16x16x32_bf16 v[48:51],v[116:119],v[156:159],v[48:51]
v_mfma_f32_16x16x32_bf16 v[84:87],v[120:123],v[156:159],v[84:87]
ds_read_b128 v[156:159],v168 offset:16384
ds_read_b64_tr_b16 v[130:131],v165 offset:12800
s_waitcnt lgkmcnt(9)
v_mfma_f32_16x16x32_bf16 v[16:19],v[112:115],v[136:139],v[16:19]
v_mfma_f32_16x16x32_bf16 v[52:55],v[116:119],v[136:139],v[52:55]
ds_read_b64_tr_b16 v[132:133],v166 offset:12288
v_mfma_f32_16x16x32_bf16 v[88:91],v[120:123],v[136:139],v[88:91]
ds_read_b128 v[136:139],v169
s_waitcnt lgkmcnt(10)
v_mfma_f32_16x16x32_bf16 v[20:23],v[112:115],v[140:143],v[20:23]
ds_read_b64_tr_b16 v[134:135],v166 offset:12800
v_mfma_f32_16x16x32_bf16 v[56:59],v[116:119],v[140:143],v[56:59]
v_mfma_f32_16x16x32_bf16 v[92:95],v[120:123],v[140:143],v[92:95]
ds_read_b128 v[140:143],v169 offset:2048
s_waitcnt lgkmcnt(11)
v_mfma_f32_16x16x32_bf16 v[24:27],v[112:115],v[144:147],v[24:27]
v_mfma_f32_16x16x32_bf16 v[60:63],v[116:119],v[144:147],v[60:63]
v_mfma_f32_16x16x32_bf16 v[96:99],v[120:123],v[144:147],v[96:99]
ds_read_b128 v[144:147],v169 offset:4096
s_waitcnt lgkmcnt(11)
v_mfma_f32_16x16x32_bf16 v[28:31],v[112:115],v[148:151],v[28:31]
v_mfma_f32_16x16x32_bf16 v[64:67],v[116:119],v[148:151],v[64:67]
v_mfma_f32_16x16x32_bf16 v[100:103],v[120:123],v[148:151],v[100:103]
ds_read_b128 v[148:151],v169 offset:6144
s_waitcnt lgkmcnt(9)
v_mfma_f32_16x16x32_bf16 v[32:35],v[112:115],v[152:155],v[32:35]
v_mfma_f32_16x16x32_bf16 v[68:71],v[116:119],v[152:155],v[68:71]
v_mfma_f32_16x16x32_bf16 v[104:107],v[120:123],v[152:155],v[104:107]
ds_read_b128 v[152:155],v169 offset:8192
s_waitcnt lgkmcnt(8)
v_mfma_f32_16x16x32_bf16 v[36:39],v[112:115],v[156:159],v[36:39]
v_mfma_f32_16x16x32_bf16 v[72:75],v[116:119],v[156:159],v[72:75]
v_mfma_f32_16x16x32_bf16 v[108:111],v[120:123],v[156:159],v[108:111]
ds_read_b128 v[156:159],v169 offset:10240
s_waitcnt lgkmcnt(6)
v_mfma_f32_16x16x32_bf16 v[4:7],v[124:127],v[136:139],v[4:7]
v_mfma_f32_16x16x32_bf16 v[40:43],v[128:131],v[136:139],v[40:43]
s_waitcnt lgkmcnt(5)
v_mfma_f32_16x16x32_bf16 v[76:79],v[132:135],v[136:139],v[76:79]
ds_read_b128 v[136:139],v169 offset:12288
s_waitcnt lgkmcnt(5)
v_mfma_f32_16x16x32_bf16 v[8:11],v[124:127],v[140:143],v[8:11]
v_mfma_f32_16x16x32_bf16 v[44:47],v[128:131],v[140:143],v[44:47]
v_mfma_f32_16x16x32_bf16 v[80:83],v[132:135],v[140:143],v[80:83]
ds_read_b128 v[140:143],v169 offset:14336
s_waitcnt lgkmcnt(5)
v_mfma_f32_16x16x32_bf16 v[12:15],v[124:127],v[144:147],v[12:15]
v_mfma_f32_16x16x32_bf16 v[48:51],v[128:131],v[144:147],v[48:51]
v_mfma_f32_16x16x32_bf16 v[84:87],v[132:135],v[144:147],v[84:87]
ds_read_b128 v[144:147],v169 offset:16384
s_waitcnt lgkmcnt(5)
v_mfma_f32_16x16x32_bf16 v[16:19],v[124:127],v[148:151],v[16:19]
s_add_u32 s94,s94,12288
s_add_u32 s89,s48,36864
s_add_u32 s96,s96,12288
s_cmp_eq_u32 s96,s89
s_cselect_b32 s96,s48,s96
s_add_u32 s95,s95,12288
s_add_u32 s89,s49,36864
s_add_u32 s97,s97,12288
s_cmp_eq_u32 s97,s89
s_cselect_b32 s97,s49,s97
v_mfma_f32_16x16x32_bf16 v[52:55],v[128:131],v[148:151],v[52:55]
v_mfma_f32_16x16x32_bf16 v[88:91],v[132:135],v[148:151],v[88:91]
s_waitcnt lgkmcnt(4)
v_mfma_f32_16x16x32_bf16 v[20:23],v[124:127],v[152:155],v[20:23]
v_mfma_f32_16x16x32_bf16 v[56:59],v[128:131],v[152:155],v[56:59]
v_mfma_f32_16x16x32_bf16 v[92:95],v[132:135],v[152:155],v[92:95]
s_waitcnt lgkmcnt(3)
v_mfma_f32_16x16x32_bf16 v[24:27],v[124:127],v[156:159],v[24:27]
v_mfma_f32_16x16x32_bf16 v[60:63],v[128:131],v[156:159],v[60:63]
v_mfma_f32_16x16x32_bf16 v[96:99],v[132:135],v[156:159],v[96:99]
s_waitcnt lgkmcnt(2)
v_mfma_f32_16x16x32_bf16 v[28:31],v[124:127],v[136:139],v[28:31]
v_mfma_f32_16x16x32_bf16 v[64:67],v[128:131],v[136:139],v[64:67]
v_mfma_f32_16x16x32_bf16 v[100:103],v[132:135],v[136:139],v[100:103]
s_waitcnt lgkmcnt(1)
v_mfma_f32_16x16x32_bf16 v[32:35],v[124:127],v[140:143],v[32:35]
v_mfma_f32_16x16x32_bf16 v[68:71],v[128:131],v[140:143],v[68:71]
v_mfma_f32_16x16x32_bf16 v[104:107],v[132:135],v[140:143],v[104:107]
s_waitcnt lgkmcnt(0)
v_mfma_f32_16x16x32_bf16 v[36:39],v[124:127],v[144:147],v[36:39]
v_mfma_f32_16x16x32_bf16 v[72:75],v[128:131],v[144:147],v[72:75]
v_mfma_f32_16x16x32_bf16 v[108:111],v[132:135],v[144:147],v[108:111]
s_waitcnt lgkmcnt(0)
s_barrier
s_mov_b32 s88,0x3dd53b95
s_mov_b32 s89,0x3dd53b95
s_cmp_eq_u32 s35,0
s_cselect_b32 s90,12288,0
s_add_u32 s90,s90,s51
s_add_u32 s90,s90,73728
s_add_u32 s91,s47,1
s_mul_i32 s91,s91,12288
s_add_u32 s91,s91,s51
s_cmp_eq_u32 s47,0
s_cselect_b32 s92,24576,0
s_add_u32 s92,s92,s51
v_mbcnt_lo_u32_b32 v0,-1,0
v_mbcnt_hi_u32_b32 v0,-1,v0
v_and_b32 v1,15,v0
v_lshrrev_b32 v2,4,v0
v_lshrrev_b32 v3,1,v2
v_lshl_add_u32 v1,v3,4,v1
v_lshlrev_b32 v1,4,v1
v_and_b32 v2,1,v2
v_lshl_add_u32 v1,v2,3,v1
s_mul_i32 s93,s84,3
s_add_u32 s93,s93,0
s_lshl_b32 s93,s93,9
v_add_u32 v0,s93,v1
v_add_u32 v160,s90,v0
v_add_u32 v161,s91,v0
v_add_u32 v162,s92,v0
v_add_u32 v163,s50,v0
s_mul_i32 s93,s84,3
s_add_u32 s93,s93,1
s_lshl_b32 s93,s93,9
v_add_u32 v0,s93,v1
v_add_u32 v164,s90,v0
v_add_u32 v165,s91,v0
v_add_u32 v166,s92,v0
v_add_u32 v167,s50,v0
s_mul_i32 s93,s84,3
s_add_u32 s93,s93,2
s_lshl_b32 s93,s93,9
v_add_u32 v0,s93,v1
v_add_u32 v168,s90,v0
v_add_u32 v169,s91,v0
v_add_u32 v170,s92,v0
v_add_u32 v171,s50,v0
v_pk_mul_f32 v[36:37],v[36:37],s[88:89]
v_pk_mul_f32 v[38:39],v[38:39],s[88:89]
v_cvt_pk_bf16_f32 v36,v36,v37
v_cvt_pk_bf16_f32 v37,v38,v39
ds_write_b64 v163,v[36:37]
v_pk_mul_f32 v[72:73],v[72:73],s[88:89]
v_pk_mul_f32 v[74:75],v[74:75],s[88:89]
v_cvt_pk_bf16_f32 v72,v72,v73
v_cvt_pk_bf16_f32 v73,v74,v75
ds_write_b64 v167,v[72:73]
v_pk_mul_f32 v[108:109],v[108:109],s[88:89]
v_pk_mul_f32 v[110:111],v[110:111],s[88:89]
v_cvt_pk_bf16_f32 v108,v108,v109
v_cvt_pk_bf16_f32 v109,v110,v111
ds_write_b64 v171,v[108:109]
v_pk_mul_f32 v[4:5],v[4:5],s[88:89]
v_pk_mul_f32 v[6:7],v[6:7],s[88:89]
v_cvt_pk_bf16_f32 v4,v4,v5
v_cvt_pk_bf16_f32 v5,v6,v7
ds_write_b64 v160,v[4:5]
v_pk_mul_f32 v[40:41],v[40:41],s[88:89]
v_pk_mul_f32 v[42:43],v[42:43],s[88:89]
v_cvt_pk_bf16_f32 v40,v40,v41
v_cvt_pk_bf16_f32 v41,v42,v43
ds_write_b64 v164,v[40:41]
v_pk_mul_f32 v[76:77],v[76:77],s[88:89]
v_pk_mul_f32 v[78:79],v[78:79],s[88:89]
v_cvt_pk_bf16_f32 v76,v76,v77
v_cvt_pk_bf16_f32 v77,v78,v79
ds_write_b64 v168,v[76:77]
v_pk_mul_f32 v[8:9],v[8:9],s[88:89]
v_pk_mul_f32 v[10:11],v[10:11],s[88:89]
v_cvt_pk_bf16_f32 v8,v8,v9
v_cvt_pk_bf16_f32 v9,v10,v11
ds_write_b64 v160,v[8:9] offset:6144
v_pk_mul_f32 v[44:45],v[44:45],s[88:89]
v_pk_mul_f32 v[46:47],v[46:47],s[88:89]
v_cvt_pk_bf16_f32 v44,v44,v45
v_cvt_pk_bf16_f32 v45,v46,v47
ds_write_b64 v164,v[44:45] offset:6144
v_pk_mul_f32 v[80:81],v[80:81],s[88:89]
v_pk_mul_f32 v[82:83],v[82:83],s[88:89]
v_cvt_pk_bf16_f32 v80,v80,v81
v_cvt_pk_bf16_f32 v81,v82,v83
ds_write_b64 v168,v[80:81] offset:6144
v_pk_mul_f32 v[12:13],v[12:13],s[88:89]
v_pk_mul_f32 v[14:15],v[14:15],s[88:89]
v_cvt_pk_bf16_f32 v12,v12,v13
v_cvt_pk_bf16_f32 v13,v14,v15
ds_write_b64 v160,v[12:13] offset:12288
v_pk_mul_f32 v[48:49],v[48:49],s[88:89]
v_pk_mul_f32 v[50:51],v[50:51],s[88:89]
v_cvt_pk_bf16_f32 v48,v48,v49
v_cvt_pk_bf16_f32 v49,v50,v51
ds_write_b64 v164,v[48:49] offset:12288
v_pk_mul_f32 v[84:85],v[84:85],s[88:89]
v_pk_mul_f32 v[86:87],v[86:87],s[88:89]
v_cvt_pk_bf16_f32 v84,v84,v85
v_cvt_pk_bf16_f32 v85,v86,v87
ds_write_b64 v168,v[84:85] offset:12288
v_pk_mul_f32 v[16:17],v[16:17],s[88:89]
v_pk_mul_f32 v[18:19],v[18:19],s[88:89]
v_cvt_pk_bf16_f32 v16,v16,v17
v_cvt_pk_bf16_f32 v17,v18,v19
ds_write_b64 v160,v[16:17] offset:18432
v_pk_mul_f32 v[52:53],v[52:53],s[88:89]
v_pk_mul_f32 v[54:55],v[54:55],s[88:89]
v_cvt_pk_bf16_f32 v52,v52,v53
v_cvt_pk_bf16_f32 v53,v54,v55
ds_write_b64 v164,v[52:53] offset:18432
v_pk_mul_f32 v[88:89],v[88:89],s[88:89]
v_pk_mul_f32 v[90:91],v[90:91],s[88:89]
v_cvt_pk_bf16_f32 v88,v88,v89
v_cvt_pk_bf16_f32 v89,v90,v91
ds_write_b64 v168,v[88:89] offset:18432
v_pk_mul_f32 v[20:21],v[20:21],s[88:89]
v_pk_mul_f32 v[22:23],v[22:23],s[88:89]
v_cvt_pk_bf16_f32 v20,v20,v21
v_cvt_pk_bf16_f32 v21,v22,v23
ds_write_b64 v161,v[20:21]
v_pk_mul_f32 v[56:57],v[56:57],s[88:89]
v_pk_mul_f32 v[58:59],v[58:59],s[88:89]
v_cvt_pk_bf16_f32 v56,v56,v57
v_cvt_pk_bf16_f32 v57,v58,v59
ds_write_b64 v165,v[56:57]
v_pk_mul_f32 v[92:93],v[92:93],s[88:89]
v_pk_mul_f32 v[94:95],v[94:95],s[88:89]
v_cvt_pk_bf16_f32 v92,v92,v93
v_cvt_pk_bf16_f32 v93,v94,v95
ds_write_b64 v169,v[92:93]
v_pk_mul_f32 v[24:25],v[24:25],s[88:89]
v_pk_mul_f32 v[26:27],v[26:27],s[88:89]
v_cvt_pk_bf16_f32 v24,v24,v25
v_cvt_pk_bf16_f32 v25,v26,v27
ds_write_b64 v161,v[24:25] offset:6144
v_pk_mul_f32 v[60:61],v[60:61],s[88:89]
v_pk_mul_f32 v[62:63],v[62:63],s[88:89]
v_cvt_pk_bf16_f32 v60,v60,v61
v_cvt_pk_bf16_f32 v61,v62,v63
ds_write_b64 v165,v[60:61] offset:6144
v_pk_mul_f32 v[96:97],v[96:97],s[88:89]
v_pk_mul_f32 v[98:99],v[98:99],s[88:89]
v_cvt_pk_bf16_f32 v96,v96,v97
v_cvt_pk_bf16_f32 v97,v98,v99
ds_write_b64 v169,v[96:97] offset:6144
v_pk_mul_f32 v[28:29],v[28:29],s[88:89]
v_pk_mul_f32 v[30:31],v[30:31],s[88:89]
v_cvt_pk_bf16_f32 v28,v28,v29
v_cvt_pk_bf16_f32 v29,v30,v31
ds_write_b64 v162,v[28:29]
v_pk_mul_f32 v[64:65],v[64:65],s[88:89]
v_pk_mul_f32 v[66:67],v[66:67],s[88:89]
v_cvt_pk_bf16_f32 v64,v64,v65
v_cvt_pk_bf16_f32 v65,v66,v67
ds_write_b64 v166,v[64:65]
v_pk_mul_f32 v[100:101],v[100:101],s[88:89]
v_pk_mul_f32 v[102:103],v[102:103],s[88:89]
v_cvt_pk_bf16_f32 v100,v100,v101
v_cvt_pk_bf16_f32 v101,v102,v103
ds_write_b64 v170,v[100:101]
v_pk_mul_f32 v[32:33],v[32:33],s[88:89]
v_pk_mul_f32 v[34:35],v[34:35],s[88:89]
v_cvt_pk_bf16_f32 v32,v32,v33
v_cvt_pk_bf16_f32 v33,v34,v35
ds_write_b64 v162,v[32:33] offset:6144
v_pk_mul_f32 v[68:69],v[68:69],s[88:89]
v_pk_mul_f32 v[70:71],v[70:71],s[88:89]
v_cvt_pk_bf16_f32 v68,v68,v69
v_cvt_pk_bf16_f32 v69,v70,v71
ds_write_b64 v166,v[68:69] offset:6144
v_pk_mul_f32 v[104:105],v[104:105],s[88:89]
v_pk_mul_f32 v[106:107],v[106:107],s[88:89]
v_cvt_pk_bf16_f32 v104,v104,v105
v_cvt_pk_bf16_f32 v105,v106,v107
ds_write_b64 v170,v[104:105] offset:6144
LDISP:
s_cmp_eq_u32 s84,1
s_cbranch_scc1 W1_START
s_cmp_eq_u32 s84,2
s_cbranch_scc1 W2_START
s_cmp_eq_u32 s84,3
s_cbranch_scc1 W3_START
W0_START:
v_mbcnt_lo_u32_b32 v0,-1,0
v_mbcnt_hi_u32_b32 v0,-1,v0
v_and_b32 v1,15,v0
v_lshrrev_b32 v2,4,v0
v_lshrrev_b32 v0,3,v1
s_movk_i32 s88,3072
v_mul_u32_u24 v244,s88,v0
v_and_b32 v0,7,v1
v_lshl_add_u32 v244,v0,7,v244
v_lshrrev_b32 v0,1,v1
v_xor_b32 v0,v0,v2
v_lshl_add_u32 v244,v0,4,v244
v_xor_b32 v245,64,v244
s_add_u32 s88,s51,s98
v_add_u32 v244,s88,v244
v_add_u32 v245,s88,v245
v_mbcnt_lo_u32_b32 v0,-1,0
v_mbcnt_hi_u32_b32 v0,-1,v0
v_and_b32 v1,15,v0
v_lshrrev_b32 v2,4,v0
v_lshlrev_b32 v250,4,v0
v_add_u32 v250,s50,v250
s_movk_i32 s88,3072
v_lshrrev_b32 v3,1,v2
v_mul_u32_u24 v246,s88,v3
v_and_b32 v2,1,v2
v_lshrrev_b32 v1,2,v1
v_lshl_add_u32 v3,v2,2,v1
v_lshl_add_u32 v246,v3,7,v246
v_lshrrev_b32 v1,1,v1
v_lshl_or_b32 v1,v2,1,v1
v_xor_b32 v1,0,v1
v_lshl_add_u32 v246,v1,5,v246
v_and_b32 v0,3,v0
v_lshl_add_u32 v246,v0,3,v246
v_xor_b32 v247,32,v246
v_xor_b32 v248,64,v246
v_xor_b32 v249,0x60,v246
s_mul_i32 s99,s35,12288
s_add_u32 s88,s99,s51
s_add_u32 s88,s88,73728
v_add_u32 v246,s88,v246
v_add_u32 v247,s88,v247
v_add_u32 v248,s88,v248
v_add_u32 v249,s88,v249
s_xor_b32 s90,s85,2
s_mul_i32 s88,s90,184320
s_add_u32 s76,s36,s88
s_addc_u32 s77,s37,0
s_and_b32 s77,s77,0xffff
s_mov_b32 s78,0x2d000
s_mov_b32 s79,0x20000
s_mul_i32 s88,s86,384
s_sub_u32 s89,0x780000,s88
s_add_u32 s80,s42,s88
s_addc_u32 s81,s43,0
s_and_b32 s81,s81,0xffff
s_mov_b32 s82,s89
s_mov_b32 s83,0x20000
s_mov_b32 s44,0
s_waitcnt vmcnt(3) lgkmcnt(0)
s_barrier
s_cmp_eq_u32 s35,0
s_cselect_b32 s90,12288,0
s_add_u32 s90,s90,s51
s_add_u32 s90,s90,73728
s_add_u32 s91,s47,1
s_mul_i32 s91,s91,12288
s_add_u32 s91,s91,s51
s_cmp_eq_u32 s47,0
s_cselect_b32 s92,24576,0
s_add_u32 s92,s92,s51
v_mbcnt_lo_u32_b32 v0,-1,0
v_mbcnt_hi_u32_b32 v0,-1,v0
v_lshlrev_b32 v0,4,v0
v_add_u32 v1,s90,v0
ds_read_b128 v[112:115],v1
ds_read_b128 v[116:119],v1 offset:1024
ds_read_b128 v[120:123],v1 offset:2048
ds_read_b128 v[124:127],v1 offset:3072
ds_read_b128 v[128:131],v1 offset:4096
ds_read_b128 v[132:135],v1 offset:5120
v_add_u32 v2,s90,v0
ds_read_b128 v[136:139],v2 offset:6144
ds_read_b128 v[140:143],v2 offset:7168
ds_read_b128 v[144:147],v2 offset:8192
ds_read_b128 v[148:151],v2 offset:9216
ds_read_b128 v[152:155],v2 offset:10240
ds_read_b128 v[156:159],v2 offset:11264
ds_read_b128 v[4:7],v244
ds_read_b128 v[8:11],v245
ds_read_b128 v[12:15],v244 offset:1024
ds_read_b128 v[16:19],v245 offset:1024
ds_read_b128 v[20:23],v244 offset:2048
ds_read_b128 v[24:27],v245 offset:2048
ds_read_b128 v[28:31],v250
s_waitcnt lgkmcnt(6)
v_mfma_f32_16x16x32_bf16 v[36:39],v[4:7],v[112:115],0
v_mfma_f32_16x16x32_bf16 v[40:43],v[4:7],v[136:139],0
s_waitcnt lgkmcnt(0)
v_mfma_f32_16x16x32_bf16 v[44:47],v[4:7],v[28:31],0
ds_read_b128 v[32:35],v250 offset:1024
v_mfma_f32_16x16x32_bf16 v[36:39],v[8:11],v[116:119],v[36:39]
v_mfma_f32_16x16x32_bf16 v[40:43],v[8:11],v[140:143],v[40:43]
s_waitcnt lgkmcnt(0)
v_mfma_f32_16x16x32_bf16 v[44:47],v[8:11],v[32:35],v[44:47]
ds_read_b128 v[28:31],v250 offset:2048
v_mfma_f32_16x16x32_bf16 v[36:39],v[12:15],v[120:123],v[36:39]
v_mfma_f32_16x16x32_bf16 v[40:43],v[12:15],v[144:147],v[40:43]
s_waitcnt lgkmcnt(0)
v_mfma_f32_16x16x32_bf16 v[44:47],v[12:15],v[28:31],v[44:47]
)A"
R"A(ds_read_b128 v[32:35],v250 offset:3072
v_mfma_f32_16x16x32_bf16 v[36:39],v[16:19],v[124:127],v[36:39]
v_mfma_f32_16x16x32_bf16 v[40:43],v[16:19],v[148:151],v[40:43]
s_waitcnt lgkmcnt(0)
v_mfma_f32_16x16x32_bf16 v[44:47],v[16:19],v[32:35],v[44:47]
ds_read_b128 v[28:31],v250 offset:4096
v_mfma_f32_16x16x32_bf16 v[36:39],v[20:23],v[128:131],v[36:39]
v_mfma_f32_16x16x32_bf16 v[40:43],v[20:23],v[152:155],v[40:43]
s_waitcnt lgkmcnt(0)
v_mfma_f32_16x16x32_bf16 v[44:47],v[20:23],v[28:31],v[44:47]
ds_read_b128 v[32:35],v250 offset:5120
v_mfma_f32_16x16x32_bf16 v[36:39],v[24:27],v[132:135],v[36:39]
v_mfma_f32_16x16x32_bf16 v[40:43],v[24:27],v[156:159],v[40:43]
s_waitcnt lgkmcnt(0)
v_mfma_f32_16x16x32_bf16 v[44:47],v[24:27],v[32:35],v[44:47]
s_nop 4
v_max_f32 v0,v36,v37
v_max3_f32 v0,v0,v38,v39
v_max_f32 v1,v40,v41
v_max3_f32 v1,v1,v42,v43
v_max_f32 v2,v44,v45
v_max3_f32 v2,v2,v46,v47
v_mov_b32 v3,v0
s_nop 1
v_permlane32_swap_b32 v0,v3
v_max_f32 v0,v0,v3
v_mov_b32 v3,v0
s_nop 1
v_permlane16_swap_b32 v0,v3
v_max_f32 v0,v0,v3
v_mov_b32 v3,v1
s_nop 1
v_permlane32_swap_b32 v1,v3
v_max_f32 v1,v1,v3
v_mov_b32 v3,v1
s_nop 1
v_permlane16_swap_b32 v1,v3
v_max_f32 v1,v1,v3
v_mov_b32 v3,v2
s_nop 1
v_permlane32_swap_b32 v2,v3
v_max_f32 v2,v2,v3
v_mov_b32 v3,v2
s_nop 1
v_permlane16_swap_b32 v2,v3
v_max_f32 v2,v2,v3
v_xor_b32 v160,0x80000000,v0
v_xor_b32 v164,0x80000000,v1
v_xor_b32 v161,0x80000000,v0
v_xor_b32 v165,0x80000000,v1
v_xor_b32 v162,0x80000000,v0
v_xor_b32 v166,0x80000000,v1
v_xor_b32 v163,0x80000000,v0
v_xor_b32 v167,0x80000000,v1
v_xor_b32 v240,0x80000000,v2
s_waitcnt lgkmcnt(0)
s_barrier
s_mov_b32 m0,s96
s_nop 0
buffer_load_dwordx4 v251,s[64:67],s94 offen nt lds
s_add_u32 m0,s96,896
s_nop 0
buffer_load_dwordx4 v251,s[64:67],s94 offen offset:128 nt lds
s_add_u32 m0,s96,1792
s_nop 0
buffer_load_dwordx4 v251,s[64:67],s94 offen offset:256 nt lds
s_add_u32 s94,s94,12288
s_add_u32 s89,s48,36864
s_add_u32 s96,s96,12288
s_cmp_eq_u32 s96,s89
s_cselect_b32 s96,s48,s96
W0_ATT:
v_mov_b32 v36,0
v_mov_b32 v37,0
v_mov_b32 v38,0
v_mov_b32 v39,0
v_mov_b32 v40,0
v_mov_b32 v41,0
v_mov_b32 v42,0
v_mov_b32 v43,0
v_mov_b32 v44,0
v_mov_b32 v45,0
v_mov_b32 v46,0
v_mov_b32 v47,0
v_mov_b32 v48,0
v_mov_b32 v49,0
v_mov_b32 v50,0
v_mov_b32 v51,0
v_mov_b32 v84,0
v_mov_b32 v85,0
v_mov_b32 v86,0
v_mov_b32 v87,0
v_mov_b32 v88,0
v_mov_b32 v89,0
v_mov_b32 v90,0
v_mov_b32 v91,0
v_mov_b32 v92,0
v_mov_b32 v93,0
v_mov_b32 v94,0
v_mov_b32 v95,0
v_mov_b32 v96,0
v_mov_b32 v97,0
v_mov_b32 v98,0
v_mov_b32 v99,0
v_mov_b32 v108,0
v_mov_b32 v109,0
v_mov_b32 v110,0
v_mov_b32 v111,0
v_mov_b32 v241,0
v_mov_b32 v242,0
v_mov_b32 v243,0
ds_read_b128 v[220:223],v244
ds_read_b128 v[224:227],v245
ds_read_b128 v[204:207],v244 offset:1024
ds_read_b128 v[208:211],v245 offset:1024
ds_read_b128 v[212:215],v244 offset:2048
ds_read_b128 v[216:219],v245 offset:2048
v_mov_b32 v236,v240
v_mov_b32 v237,v240
v_mov_b32 v238,v240
v_mov_b32 v239,v240
ds_read_b128 v[228:231],v250
ds_read_b128 v[232:235],v250 offset:1024
s_waitcnt lgkmcnt(7)
v_mfma_f32_16x16x32_bf16 v[168:171],v[220:223],v[112:115],v[160:163]
s_mov_b32 m0,s96
v_mfma_f32_16x16x32_bf16 v[172:175],v[220:223],v[136:139],v[164:167]
buffer_load_dwordx4 v251,s[64:67],s94 offen nt lds
s_waitcnt lgkmcnt(1)
v_mfma_f32_16x16x32_bf16 v[236:239],v[220:223],v[228:231],v[236:239]
ds_read_b128 v[220:223],v244 offset:6144
ds_read_b128 v[228:231],v250 offset:2048
v_mfma_f32_16x16x32_bf16 v[168:171],v[224:227],v[116:119],v[168:171]
v_mfma_f32_16x16x32_bf16 v[172:175],v[224:227],v[140:143],v[172:175]
s_add_u32 m0,s96,896
s_waitcnt lgkmcnt(2)
v_mfma_f32_16x16x32_bf16 v[236:239],v[224:227],v[232:235],v[236:239]
ds_read_b128 v[224:227],v245 offset:6144
ds_read_b128 v[232:235],v250 offset:3072
buffer_load_dwordx4 v251,s[64:67],s94 offen offset:128 nt lds
v_mfma_f32_16x16x32_bf16 v[168:171],v[204:207],v[120:123],v[168:171]
v_mfma_f32_16x16x32_bf16 v[172:175],v[204:207],v[144:147],v[172:175]
s_waitcnt lgkmcnt(2)
v_mfma_f32_16x16x32_bf16 v[236:239],v[204:207],v[228:231],v[236:239]
ds_read_b128 v[204:207],v244 offset:7168
ds_read_b128 v[228:231],v250 offset:4096
s_add_u32 m0,s96,1792
v_mfma_f32_16x16x32_bf16 v[168:171],v[208:211],v[124:127],v[168:171]
buffer_load_dwordx4 v251,s[64:67],s94 offen offset:256 nt lds
v_mfma_f32_16x16x32_bf16 v[172:175],v[208:211],v[148:151],v[172:175]
s_waitcnt lgkmcnt(2)
v_mfma_f32_16x16x32_bf16 v[236:239],v[208:211],v[232:235],v[236:239]
ds_read_b128 v[208:211],v245 offset:7168
ds_read_b128 v[232:235],v250 offset:5120
v_mfma_f32_16x16x32_bf16 v[168:171],v[212:215],v[128:131],v[168:171]
s_mov_b32 m0,s97
v_mfma_f32_16x16x32_bf16 v[172:175],v[212:215],v[152:155],v[172:175]
buffer_load_dwordx4 v252,s[68:71],s95 offen nt lds
s_waitcnt lgkmcnt(2)
v_mfma_f32_16x16x32_bf16 v[236:239],v[212:215],v[228:231],v[236:239]
ds_read_b128 v[212:215],v244 offset:8192
v_mfma_f32_16x16x32_bf16 v[168:171],v[216:219],v[132:135],v[168:171]
v_mfma_f32_16x16x32_bf16 v[172:175],v[216:219],v[156:159],v[172:175]
s_add_u32 m0,s97,896
s_waitcnt lgkmcnt(1)
v_mfma_f32_16x16x32_bf16 v[236:239],v[216:219],v[232:235],v[236:239]
ds_read_b128 v[216:219],v245 offset:8192
buffer_load_dwordx4 v252,s[68:71],s95 offen offset:128 nt lds
v_mfma_f32_16x16x32_bf16 v[176:179],v[220:223],v[112:115],v[160:163]
v_mfma_f32_16x16x32_bf16 v[180:183],v[220:223],v[136:139],v[164:167]
v_mfma_f32_16x16x32_bf16 v[176:179],v[224:227],v[116:119],v[176:179]
s_add_u32 m0,s97,1792
v_mfma_f32_16x16x32_bf16 v[180:183],v[224:227],v[140:143],v[180:183]
s_nop 0
v_exp_f32 v236,v236
v_exp_f32 v168,v168
buffer_load_dwordx4 v252,s[68:71],s95 offen offset:256 nt lds
s_mov_b32 s90,s98
s_add_u32 s98,s98,12288
s_cmp_eq_u32 s98,36864
s_cmov_b32 s98,0
s_sub_u32 s46,s98,s90
v_add_u32 v244,s46,v244
v_add_u32 v245,s46,v245
v_mfma_f32_16x16x32_bf16 v[176:179],v[204:207],v[120:123],v[176:179]
v_exp_f32 v237,v237
v_exp_f32 v169,v169
v_mfma_f32_16x16x32_bf16 v[180:183],v[204:207],v[144:147],v[180:183]
v_exp_f32 v238,v238
v_add_f32 v241,v241,v168
v_exp_f32 v170,v170
v_mfma_f32_16x16x32_bf16 v[176:179],v[208:211],v[124:127],v[176:179]
v_exp_f32 v239,v239
v_add_f32 v241,v241,v169
v_exp_f32 v171,v171
v_cvt_pk_bf16_f32 v184,v168,v169
v_mfma_f32_16x16x32_bf16 v[180:183],v[208:211],v[148:151],v[180:183]
v_add_f32 v241,v241,v170
v_exp_f32 v172,v172
s_waitcnt lgkmcnt(1)
v_mfma_f32_16x16x32_bf16 v[176:179],v[212:215],v[128:131],v[176:179]
v_cvt_pk_bf16_f32 v0,v236,v237
v_cvt_pk_bf16_f32 v1,v238,v239
v_add_f32 v241,v241,v171
v_exp_f32 v173,v173
v_cvt_pk_bf16_f32 v185,v170,v171
s_add_u32 s94,s94,12288
s_add_u32 s89,s48,36864
s_add_u32 s96,s96,12288
s_cmp_eq_u32 s96,s89
s_cselect_b32 s96,s48,s96
v_mfma_f32_16x16x32_bf16 v[180:183],v[212:215],v[152:155],v[180:183]
ds_write_b64 v250,v[0:1] offset:6144
v_add_f32 v242,v242,v172
v_exp_f32 v174,v174
s_waitcnt lgkmcnt(1)
v_mfma_f32_16x16x32_bf16 v[176:179],v[216:219],v[132:135],v[176:179]
v_add_f32 v242,v242,v173
v_exp_f32 v175,v175
v_cvt_pk_bf16_f32 v188,v172,v173
s_add_u32 s95,s95,12288
s_add_u32 s89,s49,36864
s_add_u32 s97,s97,12288
s_cmp_eq_u32 s97,s89
s_cselect_b32 s97,s49,s97
v_mfma_f32_16x16x32_bf16 v[180:183],v[216:219],v[156:159],v[180:183]
v_add_f32 v242,v242,v174
v_add_f32 v242,v242,v175
v_cvt_pk_bf16_f32 v189,v174,v175
s_waitcnt vmcnt(6) lgkmcnt(0)
s_barrier
ds_read_b128 v[220:223],v244
ds_read_b128 v[224:227],v245
ds_read_b128 v[204:207],v244 offset:1024
ds_read_b128 v[208:211],v245 offset:1024
ds_read_b128 v[212:215],v244 offset:2048
ds_read_b128 v[216:219],v245 offset:2048
ds_read_b128 v[200:203],v250 offset:6144
v_exp_f32 v176,v176
s_waitcnt lgkmcnt(6)
v_mfma_f32_16x16x32_bf16 v[168:171],v[220:223],v[112:115],v[160:163]
v_exp_f32 v177,v177
s_mov_b32 m0,s96
v_mfma_f32_16x16x32_bf16 v[172:175],v[220:223],v[136:139],v[164:167]
ds_read_b128 v[220:223],v244 offset:6144
v_add_f32 v241,v241,v176
v_exp_f32 v178,v178
buffer_load_dwordx4 v251,s[64:67],s94 offen nt lds
s_waitcnt lgkmcnt(6)
v_mfma_f32_16x16x32_bf16 v[168:171],v[224:227],v[116:119],v[168:171]
v_add_f32 v241,v241,v177
v_exp_f32 v179,v179
v_cvt_pk_bf16_f32 v186,v176,v177
v_mfma_f32_16x16x32_bf16 v[172:175],v[224:227],v[140:143],v[172:175]
ds_read_b128 v[224:227],v245 offset:6144
v_add_f32 v241,v241,v178
v_exp_f32 v180,v180
s_waitcnt lgkmcnt(6)
v_mfma_f32_16x16x32_bf16 v[168:171],v[204:207],v[120:123],v[168:171]
v_add_f32 v241,v241,v179
v_exp_f32 v181,v181
v_cvt_pk_bf16_f32 v187,v178,v179
s_add_u32 m0,s96,896
v_mfma_f32_16x16x32_bf16 v[172:175],v[204:207],v[144:147],v[172:175]
ds_read_b128 v[204:207],v244 offset:7168
v_add_f32 v242,v242,v180
v_exp_f32 v182,v182
buffer_load_dwordx4 v251,s[64:67],s94 offen offset:128 nt lds
s_waitcnt lgkmcnt(6)
v_mfma_f32_16x16x32_bf16 v[168:171],v[208:211],v[124:127],v[168:171]
s_waitcnt lgkmcnt(3)
v_dot2c_f32_bf16 v243,s55,v200
v_add_f32 v242,v242,v181
v_exp_f32 v183,v183
v_cvt_pk_bf16_f32 v190,v180,v181
v_mfma_f32_16x16x32_bf16 v[172:175],v[208:211],v[148:151],v[172:175]
ds_read_b128 v[208:211],v245 offset:7168
v_add_f32 v242,v242,v182
v_mfma_f32_16x16x32_bf16 v[168:171],v[212:215],v[128:131],v[168:171]
v_dot2c_f32_bf16 v243,s55,v201
v_add_f32 v242,v242,v183
v_cvt_pk_bf16_f32 v191,v182,v183
s_add_u32 m0,s96,1792
v_mfma_f32_16x16x32_bf16 v[172:175],v[212:215],v[152:155],v[172:175]
ds_read_b128 v[212:215],v244 offset:8192
buffer_load_dwordx4 v251,s[64:67],s94 offen offset:256 nt lds
v_mfma_f32_16x16x32_bf16 v[168:171],v[216:219],v[132:135],v[168:171]
v_dot2c_f32_bf16 v243,s55,v202
v_mfma_f32_16x16x32_bf16 v[172:175],v[216:219],v[156:159],v[172:175]
ds_read_b128 v[216:219],v245 offset:8192
s_waitcnt lgkmcnt(5)
v_mfma_f32_16x16x32_bf16 v[176:179],v[220:223],v[112:115],v[160:163]
v_dot2c_f32_bf16 v243,s55,v203
s_mov_b32 m0,s97
v_mfma_f32_16x16x32_bf16 v[180:183],v[220:223],v[136:139],v[164:167]
ds_read_b64_tr_b16 v[220:221],v246
ds_read_b64_tr_b16 v[222:223],v246 offset:6144
buffer_load_dwordx4 v252,s[68:71],s95 offen nt lds
s_waitcnt lgkmcnt(6)
v_mfma_f32_16x16x32_bf16 v[176:179],v[224:227],v[116:119],v[176:179]
v_mfma_f32_16x16x32_bf16 v[180:183],v[224:227],v[140:143],v[180:183]
ds_read_b64_tr_b16 v[224:225],v247
ds_read_b64_tr_b16 v[226:227],v247 offset:6144
v_exp_f32 v168,v168
s_mov_b32 s90,s98
s_add_u32 s98,s98,12288
s_cmp_eq_u32 s98,36864
s_cmov_b32 s98,0
s_sub_u32 s46,s98,s90
v_add_u32 v244,s46,v244
v_add_u32 v245,s46,v245
s_waitcnt lgkmcnt(7)
v_mfma_f32_16x16x32_bf16 v[176:179],v[204:207],v[120:123],v[176:179]
v_exp_f32 v169,v169
s_add_u32 m0,s97,896
v_mfma_f32_16x16x32_bf16 v[180:183],v[204:207],v[144:147],v[180:183]
ds_read_b64_tr_b16 v[204:205],v248
ds_read_b64_tr_b16 v[206:207],v248 offset:6144
v_add_f32 v241,v241,v168
v_exp_f32 v170,v170
buffer_load_dwordx4 v252,s[68:71],s95 offen offset:128 nt lds
s_waitcnt lgkmcnt(8)
v_mfma_f32_16x16x32_bf16 v[176:179],v[208:211],v[124:127],v[176:179]
v_add_f32 v241,v241,v169
v_exp_f32 v171,v171
v_cvt_pk_bf16_f32 v192,v168,v169
v_mfma_f32_16x16x32_bf16 v[180:183],v[208:211],v[148:151],v[180:183]
ds_read_b64_tr_b16 v[208:209],v249
ds_read_b64_tr_b16 v[210:211],v249 offset:6144
v_add_f32 v241,v241,v170
v_exp_f32 v172,v172
s_waitcnt lgkmcnt(9)
v_mfma_f32_16x16x32_bf16 v[176:179],v[212:215],v[128:131],v[176:179]
v_add_f32 v241,v241,v171
v_exp_f32 v173,v173
v_cvt_pk_bf16_f32 v193,v170,v171
s_add_u32 m0,s97,1792
v_mfma_f32_16x16x32_bf16 v[180:183],v[212:215],v[152:155],v[180:183]
ds_read_b64_tr_b16 v[212:213],v246 offset:1024
ds_read_b64_tr_b16 v[214:215],v246 offset:7168
v_add_f32 v242,v242,v172
v_exp_f32 v174,v174
buffer_load_dwordx4 v252,s[68:71],s95 offen offset:256 nt lds
s_waitcnt lgkmcnt(10)
v_mfma_f32_16x16x32_bf16 v[176:179],v[216:219],v[132:135],v[176:179]
v_add_f32 v242,v242,v173
v_exp_f32 v175,v175
v_cvt_pk_bf16_f32 v196,v172,v173
v_mfma_f32_16x16x32_bf16 v[180:183],v[216:219],v[156:159],v[180:183]
ds_read_b64_tr_b16 v[216:217],v247 offset:1024
ds_read_b64_tr_b16 v[218:219],v247 offset:7168
v_add_f32 v242,v242,v174
s_waitcnt lgkmcnt(10)
v_mfma_f32_16x16x32_bf16 v[4:7],v[220:223],v[184:187],0
v_add_f32 v242,v242,v175
v_cvt_pk_bf16_f32 v197,v174,v175
v_mfma_f32_16x16x32_bf16 v[52:55],v[220:223],v[188:191],0
v_mfma_f32_16x16x32_bf16 v[100:103],v[220:223],v[200:203],0
ds_read_b64_tr_b16 v[220:221],v248 offset:1024
ds_read_b64_tr_b16 v[222:223],v248 offset:7168
s_add_u32 s94,s94,12288
s_add_u32 s89,s48,36864
s_add_u32 s96,s96,12288
s_cmp_eq_u32 s96,s89
s_cselect_b32 s96,s48,s96
s_waitcnt lgkmcnt(10)
v_mfma_f32_16x16x32_bf16 v[8:11],v[224:227],v[184:187],0
v_mfma_f32_16x16x32_bf16 v[56:59],v[224:227],v[188:191],0
ds_read_b64_tr_b16 v[224:225],v249 offset:1024
ds_read_b64_tr_b16 v[226:227],v249 offset:7168
s_add_u32 s95,s95,12288
s_add_u32 s89,s49,36864
s_add_u32 s97,s97,12288
s_cmp_eq_u32 s97,s89
s_cselect_b32 s97,s49,s97
s_waitcnt lgkmcnt(10)
v_mfma_f32_16x16x32_bf16 v[12:15],v[204:207],v[184:187],0
v_mfma_f32_16x16x32_bf16 v[60:63],v[204:207],v[188:191],0
ds_read_b64_tr_b16 v[204:205],v246 offset:2048
ds_read_b64_tr_b16 v[206:207],v246 offset:8192
s_waitcnt lgkmcnt(10)
v_mfma_f32_16x16x32_bf16 v[16:19],v[208:211],v[184:187],0
v_mfma_f32_16x16x32_bf16 v[64:67],v[208:211],v[188:191],0
ds_read_b64_tr_b16 v[208:209],v247 offset:2048
ds_read_b64_tr_b16 v[210:211],v247 offset:8192
s_waitcnt lgkmcnt(10)
v_mfma_f32_16x16x32_bf16 v[20:23],v[212:215],v[184:187],0
v_mfma_f32_16x16x32_bf16 v[68:71],v[212:215],v[188:191],0
v_mfma_f32_16x16x32_bf16 v[104:107],v[212:215],v[200:203],0
ds_read_b64_tr_b16 v[212:213],v248 offset:2048
ds_read_b64_tr_b16 v[214:215],v248 offset:8192
s_waitcnt lgkmcnt(10)
v_mfma_f32_16x16x32_bf16 v[24:27],v[216:219],v[184:187],0
v_mfma_f32_16x16x32_bf16 v[72:75],v[216:219],v[188:191],0
ds_read_b64_tr_b16 v[216:217],v249 offset:2048
ds_read_b64_tr_b16 v[218:219],v249 offset:8192
s_waitcnt lgkmcnt(10)
v_mfma_f32_16x16x32_bf16 v[28:31],v[220:223],v[184:187],0
s_mov_b32 s90,s99
s_add_u32 s99,s99,12288
)A"
R"A(s_cmp_eq_u32 s99,36864
s_cmov_b32 s99,0
s_sub_u32 s46,s99,s90
v_add_u32 v246,s46,v246
v_add_u32 v247,s46,v247
v_add_u32 v248,s46,v248
v_add_u32 v249,s46,v249
v_mfma_f32_16x16x32_bf16 v[76:79],v[220:223],v[188:191],0
s_waitcnt lgkmcnt(8)
v_mfma_f32_16x16x32_bf16 v[32:35],v[224:227],v[184:187],0
v_mfma_f32_16x16x32_bf16 v[80:83],v[224:227],v[188:191],0
s_mov_b32 s45,6
W0_LOOP:
s_waitcnt vmcnt(6) lgkmcnt(0)
s_barrier
ds_read_b128 v[220:223],v244
ds_read_b128 v[224:227],v245
v_mfma_f32_16x16x32_bf16 v[36:39],v[204:207],v[184:187],v[36:39]
v_exp_f32 v176,v176
s_mov_b32 m0,s96
v_mfma_f32_16x16x32_bf16 v[84:87],v[204:207],v[188:191],v[84:87]
v_exp_f32 v177,v177
buffer_load_dwordx4 v251,s[64:67],s94 offen nt lds
v_mfma_f32_16x16x32_bf16 v[108:111],v[204:207],v[200:203],v[108:111]
ds_read_b128 v[204:207],v244 offset:1024
ds_read_b128 v[228:231],v250
ds_read_b128 v[232:235],v250 offset:1024
v_add_f32 v241,v241,v176
v_exp_f32 v178,v178
v_mfma_f32_16x16x32_bf16 v[40:43],v[208:211],v[184:187],v[40:43]
v_add_f32 v241,v241,v177
v_exp_f32 v179,v179
v_cvt_pk_bf16_f32 v194,v176,v177
v_mfma_f32_16x16x32_bf16 v[88:91],v[208:211],v[188:191],v[88:91]
ds_read_b128 v[208:211],v245 offset:1024
ds_read_b128 v[200:203],v250 offset:7168
v_add_f32 v241,v241,v178
v_exp_f32 v180,v180
s_add_u32 m0,s96,896
v_mfma_f32_16x16x32_bf16 v[44:47],v[212:215],v[184:187],v[44:47]
v_mov_b32 v236,v240
v_mov_b32 v237,v240
v_mov_b32 v238,v240
v_mov_b32 v239,v240
v_add_f32 v241,v241,v179
v_exp_f32 v181,v181
v_cvt_pk_bf16_f32 v195,v178,v179
buffer_load_dwordx4 v251,s[64:67],s94 offen offset:128 nt lds
v_mfma_f32_16x16x32_bf16 v[92:95],v[212:215],v[188:191],v[92:95]
ds_read_b128 v[212:215],v244 offset:2048
v_add_f32 v242,v242,v180
v_exp_f32 v182,v182
v_mfma_f32_16x16x32_bf16 v[48:51],v[216:219],v[184:187],v[48:51]
v_add_f32 v242,v242,v181
v_exp_f32 v183,v183
v_cvt_pk_bf16_f32 v198,v180,v181
v_mfma_f32_16x16x32_bf16 v[96:99],v[216:219],v[188:191],v[96:99]
ds_read_b128 v[216:219],v245 offset:2048
v_add_f32 v242,v242,v182
s_add_u32 m0,s96,1792
s_waitcnt lgkmcnt(8)
v_mfma_f32_16x16x32_bf16 v[168:171],v[220:223],v[112:115],v[160:163]
v_add_f32 v242,v242,v183
v_cvt_pk_bf16_f32 v199,v182,v183
buffer_load_dwordx4 v251,s[64:67],s94 offen offset:256 nt lds
v_mfma_f32_16x16x32_bf16 v[172:175],v[220:223],v[136:139],v[164:167]
s_waitcnt lgkmcnt(5)
v_mfma_f32_16x16x32_bf16 v[236:239],v[220:223],v[228:231],v[236:239]
ds_read_b128 v[220:223],v244 offset:6144
ds_read_b128 v[228:231],v250 offset:2048
v_mfma_f32_16x16x32_bf16 v[168:171],v[224:227],v[116:119],v[168:171]
s_mov_b32 m0,s97
v_mfma_f32_16x16x32_bf16 v[172:175],v[224:227],v[140:143],v[172:175]
buffer_load_dwordx4 v252,s[68:71],s95 offen nt lds
s_waitcnt lgkmcnt(6)
v_mfma_f32_16x16x32_bf16 v[236:239],v[224:227],v[232:235],v[236:239]
ds_read_b128 v[224:227],v245 offset:6144
ds_read_b128 v[232:235],v250 offset:3072
v_mfma_f32_16x16x32_bf16 v[168:171],v[204:207],v[120:123],v[168:171]
v_mfma_f32_16x16x32_bf16 v[172:175],v[204:207],v[144:147],v[172:175]
s_add_u32 m0,s97,896
s_waitcnt lgkmcnt(2)
v_mfma_f32_16x16x32_bf16 v[236:239],v[204:207],v[228:231],v[236:239]
ds_read_b128 v[204:207],v244 offset:7168
ds_read_b128 v[228:231],v250 offset:4096
buffer_load_dwordx4 v252,s[68:71],s95 offen offset:128 nt lds
v_mfma_f32_16x16x32_bf16 v[168:171],v[208:211],v[124:127],v[168:171]
v_dot2c_f32_bf16 v243,s55,v200
v_mfma_f32_16x16x32_bf16 v[172:175],v[208:211],v[148:151],v[172:175]
s_waitcnt lgkmcnt(2)
v_mfma_f32_16x16x32_bf16 v[236:239],v[208:211],v[232:235],v[236:239]
ds_read_b128 v[208:211],v245 offset:7168
ds_read_b128 v[232:235],v250 offset:5120
s_add_u32 m0,s97,1792
v_mfma_f32_16x16x32_bf16 v[168:171],v[212:215],v[128:131],v[168:171]
v_dot2c_f32_bf16 v243,s55,v201
buffer_load_dwordx4 v252,s[68:71],s95 offen offset:256 nt lds
v_mfma_f32_16x16x32_bf16 v[172:175],v[212:215],v[152:155],v[172:175]
s_waitcnt lgkmcnt(2)
v_mfma_f32_16x16x32_bf16 v[236:239],v[212:215],v[228:231],v[236:239]
ds_read_b128 v[212:215],v244 offset:8192
v_mfma_f32_16x16x32_bf16 v[168:171],v[216:219],v[132:135],v[168:171]
v_dot2c_f32_bf16 v243,s55,v202
v_mfma_f32_16x16x32_bf16 v[172:175],v[216:219],v[156:159],v[172:175]
s_waitcnt lgkmcnt(1)
v_mfma_f32_16x16x32_bf16 v[236:239],v[216:219],v[232:235],v[236:239]
ds_read_b128 v[216:219],v245 offset:8192
s_add_u32 s94,s94,12288
s_add_u32 s89,s48,36864
s_add_u32 s96,s96,12288
s_cmp_eq_u32 s96,s89
s_cselect_b32 s96,s48,s96
v_mfma_f32_16x16x32_bf16 v[176:179],v[220:223],v[112:115],v[160:163]
v_dot2c_f32_bf16 v243,s55,v203
v_mfma_f32_16x16x32_bf16 v[180:183],v[220:223],v[136:139],v[164:167]
ds_read_b64_tr_b16 v[220:221],v246
ds_read_b64_tr_b16 v[222:223],v246 offset:6144
s_add_u32 s95,s95,12288
s_add_u32 s89,s49,36864
s_add_u32 s97,s97,12288
s_cmp_eq_u32 s97,s89
s_cselect_b32 s97,s49,s97
v_mfma_f32_16x16x32_bf16 v[176:179],v[224:227],v[116:119],v[176:179]
v_mfma_f32_16x16x32_bf16 v[180:183],v[224:227],v[140:143],v[180:183]
ds_read_b64_tr_b16 v[224:225],v247
ds_read_b64_tr_b16 v[226:227],v247 offset:6144
v_exp_f32 v236,v236
v_exp_f32 v168,v168
s_sub_u32 s45,s45,1
s_mov_b32 s90,s98
s_add_u32 s98,s98,12288
s_cmp_eq_u32 s98,36864
s_cmov_b32 s98,0
s_sub_u32 s46,s98,s90
v_add_u32 v244,s46,v244
v_add_u32 v245,s46,v245
v_mfma_f32_16x16x32_bf16 v[176:179],v[204:207],v[120:123],v[176:179]
v_exp_f32 v237,v237
v_exp_f32 v169,v169
v_mfma_f32_16x16x32_bf16 v[180:183],v[204:207],v[144:147],v[180:183]
ds_read_b64_tr_b16 v[204:205],v248
ds_read_b64_tr_b16 v[206:207],v248 offset:6144
v_exp_f32 v238,v238
v_add_f32 v241,v241,v168
v_exp_f32 v170,v170
v_mfma_f32_16x16x32_bf16 v[176:179],v[208:211],v[124:127],v[176:179]
v_exp_f32 v239,v239
v_add_f32 v241,v241,v169
v_exp_f32 v171,v171
v_cvt_pk_bf16_f32 v184,v168,v169
v_mfma_f32_16x16x32_bf16 v[180:183],v[208:211],v[148:151],v[180:183]
ds_read_b64_tr_b16 v[208:209],v249
ds_read_b64_tr_b16 v[210:211],v249 offset:6144
v_add_f32 v241,v241,v170
v_exp_f32 v172,v172
s_waitcnt lgkmcnt(9)
v_mfma_f32_16x16x32_bf16 v[176:179],v[212:215],v[128:131],v[176:179]
v_cvt_pk_bf16_f32 v0,v236,v237
v_cvt_pk_bf16_f32 v1,v238,v239
v_add_f32 v241,v241,v171
v_exp_f32 v173,v173
v_cvt_pk_bf16_f32 v185,v170,v171
v_mfma_f32_16x16x32_bf16 v[180:183],v[212:215],v[152:155],v[180:183]
ds_read_b64_tr_b16 v[212:213],v246 offset:1024
ds_read_b64_tr_b16 v[214:215],v246 offset:7168
ds_write_b64 v250,v[0:1] offset:6144
v_add_f32 v242,v242,v172
v_exp_f32 v174,v174
s_waitcnt lgkmcnt(11)
v_mfma_f32_16x16x32_bf16 v[176:179],v[216:219],v[132:135],v[176:179]
v_add_f32 v242,v242,v173
v_exp_f32 v175,v175
v_cvt_pk_bf16_f32 v188,v172,v173
v_mfma_f32_16x16x32_bf16 v[180:183],v[216:219],v[156:159],v[180:183]
ds_read_b64_tr_b16 v[216:217],v247 offset:1024
ds_read_b64_tr_b16 v[218:219],v247 offset:7168
v_add_f32 v242,v242,v174
s_waitcnt lgkmcnt(11)
v_mfma_f32_16x16x32_bf16 v[4:7],v[220:223],v[192:195],v[4:7]
v_add_f32 v242,v242,v175
v_cvt_pk_bf16_f32 v189,v174,v175
v_mfma_f32_16x16x32_bf16 v[52:55],v[220:223],v[196:199],v[52:55]
v_mfma_f32_16x16x32_bf16 v[100:103],v[220:223],v[200:203],v[100:103]
ds_read_b64_tr_b16 v[220:221],v248 offset:1024
ds_read_b64_tr_b16 v[222:223],v248 offset:7168
s_waitcnt lgkmcnt(11)
v_mfma_f32_16x16x32_bf16 v[8:11],v[224:227],v[192:195],v[8:11]
v_mfma_f32_16x16x32_bf16 v[56:59],v[224:227],v[196:199],v[56:59]
ds_read_b64_tr_b16 v[224:225],v249 offset:1024
ds_read_b64_tr_b16 v[226:227],v249 offset:7168
s_waitcnt lgkmcnt(11)
v_mfma_f32_16x16x32_bf16 v[12:15],v[204:207],v[192:195],v[12:15]
v_mfma_f32_16x16x32_bf16 v[60:63],v[204:207],v[196:199],v[60:63]
ds_read_b64_tr_b16 v[204:205],v246 offset:2048
ds_read_b64_tr_b16 v[206:207],v246 offset:8192
s_waitcnt lgkmcnt(11)
v_mfma_f32_16x16x32_bf16 v[16:19],v[208:211],v[192:195],v[16:19]
v_mfma_f32_16x16x32_bf16 v[64:67],v[208:211],v[196:199],v[64:67]
ds_read_b64_tr_b16 v[208:209],v247 offset:2048
ds_read_b64_tr_b16 v[210:211],v247 offset:8192
s_waitcnt lgkmcnt(11)
v_mfma_f32_16x16x32_bf16 v[20:23],v[212:215],v[192:195],v[20:23]
v_mfma_f32_16x16x32_bf16 v[68:71],v[212:215],v[196:199],v[68:71]
v_mfma_f32_16x16x32_bf16 v[104:107],v[212:215],v[200:203],v[104:107]
ds_read_b64_tr_b16 v[212:213],v248 offset:2048
ds_read_b64_tr_b16 v[214:215],v248 offset:8192
s_waitcnt lgkmcnt(10)
v_mfma_f32_16x16x32_bf16 v[24:27],v[216:219],v[192:195],v[24:27]
v_mfma_f32_16x16x32_bf16 v[72:75],v[216:219],v[196:199],v[72:75]
ds_read_b64_tr_b16 v[216:217],v249 offset:2048
ds_read_b64_tr_b16 v[218:219],v249 offset:8192
s_waitcnt lgkmcnt(10)
v_mfma_f32_16x16x32_bf16 v[28:31],v[220:223],v[192:195],v[28:31]
s_mov_b32 s90,s99
s_add_u32 s99,s99,12288
s_cmp_eq_u32 s99,36864
s_cmov_b32 s99,0
s_sub_u32 s46,s99,s90
v_add_u32 v246,s46,v246
v_add_u32 v247,s46,v247
v_add_u32 v248,s46,v248
v_add_u32 v249,s46,v249
v_mfma_f32_16x16x32_bf16 v[76:79],v[220:223],v[196:199],v[76:79]
s_waitcnt lgkmcnt(8)
v_mfma_f32_16x16x32_bf16 v[32:35],v[224:227],v[192:195],v[32:35]
v_mfma_f32_16x16x32_bf16 v[80:83],v[224:227],v[196:199],v[80:83]
s_waitcnt vmcnt(6) lgkmcnt(0)
s_barrier
ds_read_b128 v[220:223],v244
ds_read_b128 v[224:227],v245
v_mfma_f32_16x16x32_bf16 v[36:39],v[204:207],v[192:195],v[36:39]
v_exp_f32 v176,v176
s_mov_b32 m0,s96
v_mfma_f32_16x16x32_bf16 v[84:87],v[204:207],v[196:199],v[84:87]
v_exp_f32 v177,v177
buffer_load_dwordx4 v251,s[64:67],s94 offen nt lds
v_mfma_f32_16x16x32_bf16 v[108:111],v[204:207],v[200:203],v[108:111]
ds_read_b128 v[204:207],v244 offset:1024
v_add_f32 v241,v241,v176
v_exp_f32 v178,v178
v_mfma_f32_16x16x32_bf16 v[40:43],v[208:211],v[192:195],v[40:43]
v_add_f32 v241,v241,v177
v_exp_f32 v179,v179
v_cvt_pk_bf16_f32 v186,v176,v177
v_mfma_f32_16x16x32_bf16 v[88:91],v[208:211],v[196:199],v[88:91]
ds_read_b128 v[208:211],v245 offset:1024
ds_read_b128 v[200:203],v250 offset:6144
v_add_f32 v241,v241,v178
v_exp_f32 v180,v180
s_add_u32 m0,s96,896
v_mfma_f32_16x16x32_bf16 v[44:47],v[212:215],v[192:195],v[44:47]
v_add_f32 v241,v241,v179
v_exp_f32 v181,v181
v_cvt_pk_bf16_f32 v187,v178,v179
buffer_load_dwordx4 v251,s[64:67],s94 offen offset:128 nt lds
v_mfma_f32_16x16x32_bf16 v[92:95],v[212:215],v[196:199],v[92:95]
ds_read_b128 v[212:215],v244 offset:2048
v_add_f32 v242,v242,v180
v_exp_f32 v182,v182
v_mfma_f32_16x16x32_bf16 v[48:51],v[216:219],v[192:195],v[48:51]
v_add_f32 v242,v242,v181
v_exp_f32 v183,v183
v_cvt_pk_bf16_f32 v190,v180,v181
v_mfma_f32_16x16x32_bf16 v[96:99],v[216:219],v[196:199],v[96:99]
ds_read_b128 v[216:219],v245 offset:2048
v_add_f32 v242,v242,v182
s_add_u32 m0,s96,1792
s_waitcnt lgkmcnt(6)
v_mfma_f32_16x16x32_bf16 v[168:171],v[220:223],v[112:115],v[160:163]
v_add_f32 v242,v242,v183
v_cvt_pk_bf16_f32 v191,v182,v183
buffer_load_dwordx4 v251,s[64:67],s94 offen offset:256 nt lds
v_mfma_f32_16x16x32_bf16 v[172:175],v[220:223],v[136:139],v[164:167]
ds_read_b128 v[220:223],v244 offset:6144
s_waitcnt lgkmcnt(6)
v_mfma_f32_16x16x32_bf16 v[168:171],v[224:227],v[116:119],v[168:171]
v_mfma_f32_16x16x32_bf16 v[172:175],v[224:227],v[140:143],v[172:175]
ds_read_b128 v[224:227],v245 offset:6144
s_mov_b32 m0,s97
s_waitcnt lgkmcnt(6)
v_mfma_f32_16x16x32_bf16 v[168:171],v[204:207],v[120:123],v[168:171]
buffer_load_dwordx4 v252,s[68:71],s95 offen nt lds
v_mfma_f32_16x16x32_bf16 v[172:175],v[204:207],v[144:147],v[172:175]
ds_read_b128 v[204:207],v244 offset:7168
s_waitcnt lgkmcnt(6)
v_mfma_f32_16x16x32_bf16 v[168:171],v[208:211],v[124:127],v[168:171]
s_waitcnt lgkmcnt(5)
v_dot2c_f32_bf16 v243,s55,v200
v_mfma_f32_16x16x32_bf16 v[172:175],v[208:211],v[148:151],v[172:175]
ds_read_b128 v[208:211],v245 offset:7168
s_add_u32 m0,s97,896
s_waitcnt lgkmcnt(5)
v_mfma_f32_16x16x32_bf16 v[168:171],v[212:215],v[128:131],v[168:171]
v_dot2c_f32_bf16 v243,s55,v201
buffer_load_dwordx4 v252,s[68:71],s95 offen offset:128 nt lds
v_mfma_f32_16x16x32_bf16 v[172:175],v[212:215],v[152:155],v[172:175]
ds_read_b128 v[212:215],v244 offset:8192
s_waitcnt lgkmcnt(5)
v_mfma_f32_16x16x32_bf16 v[168:171],v[216:219],v[132:135],v[168:171]
v_dot2c_f32_bf16 v243,s55,v202
v_mfma_f32_16x16x32_bf16 v[172:175],v[216:219],v[156:159],v[172:175]
ds_read_b128 v[216:219],v245 offset:8192
s_add_u32 m0,s97,1792
s_waitcnt lgkmcnt(5)
v_mfma_f32_16x16x32_bf16 v[176:179],v[220:223],v[112:115],v[160:163]
v_dot2c_f32_bf16 v243,s55,v203
buffer_load_dwordx4 v252,s[68:71],s95 offen offset:256 nt lds
v_mfma_f32_16x16x32_bf16 v[180:183],v[220:223],v[136:139],v[164:167]
ds_read_b64_tr_b16 v[220:221],v246
ds_read_b64_tr_b16 v[222:223],v246 offset:6144
s_waitcnt lgkmcnt(6)
v_mfma_f32_16x16x32_bf16 v[176:179],v[224:227],v[116:119],v[176:179]
v_mfma_f32_16x16x32_bf16 v[180:183],v[224:227],v[140:143],v[180:183]
ds_read_b64_tr_b16 v[224:225],v247
ds_read_b64_tr_b16 v[226:227],v247 offset:6144
v_exp_f32 v168,v168
s_mov_b32 s90,s98
s_add_u32 s98,s98,12288
s_cmp_eq_u32 s98,36864
s_cmov_b32 s98,0
s_sub_u32 s46,s98,s90
v_add_u32 v244,s46,v244
v_add_u32 v245,s46,v245
s_waitcnt lgkmcnt(7)
v_mfma_f32_16x16x32_bf16 v[176:179],v[204:207],v[120:123],v[176:179]
v_exp_f32 v169,v169
v_mfma_f32_16x16x32_bf16 v[180:183],v[204:207],v[144:147],v[180:183]
ds_read_b64_tr_b16 v[204:205],v248
ds_read_b64_tr_b16 v[206:207],v248 offset:6144
v_add_f32 v241,v241,v168
v_exp_f32 v170,v170
s_add_u32 s94,s94,12288
s_add_u32 s89,s48,36864
s_add_u32 s96,s96,12288
s_cmp_eq_u32 s96,s89
s_cselect_b32 s96,s48,s96
s_waitcnt lgkmcnt(8)
v_mfma_f32_16x16x32_bf16 v[176:179],v[208:211],v[124:127],v[176:179]
v_add_f32 v241,v241,v169
v_exp_f32 v171,v171
v_cvt_pk_bf16_f32 v192,v168,v169
v_mfma_f32_16x16x32_bf16 v[180:183],v[208:211],v[148:151],v[180:183]
ds_read_b64_tr_b16 v[208:209],v249
ds_read_b64_tr_b16 v[210:211],v249 offset:6144
v_add_f32 v241,v241,v170
v_exp_f32 v172,v172
s_add_u32 s95,s95,12288
s_add_u32 s89,s49,36864
s_add_u32 s97,s97,12288
s_cmp_eq_u32 s97,s89
s_cselect_b32 s97,s49,s97
s_waitcnt lgkmcnt(9)
v_mfma_f32_16x16x32_bf16 v[176:179],v[212:215],v[128:131],v[176:179]
v_add_f32 v241,v241,v171
v_exp_f32 v173,v173
v_cvt_pk_bf16_f32 v193,v170,v171
v_mfma_f32_16x16x32_bf16 v[180:183],v[212:215],v[152:155],v[180:183]
ds_read_b64_tr_b16 v[212:213],v246 offset:1024
ds_read_b64_tr_b16 v[214:215],v246 offset:7168
v_add_f32 v242,v242,v172
v_exp_f32 v174,v174
s_waitcnt lgkmcnt(10)
v_mfma_f32_16x16x32_bf16 v[176:179],v[216:219],v[132:135],v[176:179]
v_add_f32 v242,v242,v173
v_exp_f32 v175,v175
v_cvt_pk_bf16_f32 v196,v172,v173
v_mfma_f32_16x16x32_bf16 v[180:183],v[216:219],v[156:159],v[180:183]
ds_read_b64_tr_b16 v[216:217],v247 offset:1024
ds_read_b64_tr_b16 v[218:219],v247 offset:7168
v_add_f32 v242,v242,v174
s_waitcnt lgkmcnt(10)
v_mfma_f32_16x16x32_bf16 v[4:7],v[220:223],v[184:187],v[4:7]
v_add_f32 v242,v242,v175
v_cvt_pk_bf16_f32 v197,v174,v175
v_mfma_f32_16x16x32_bf16 v[52:55],v[220:223],v[188:191],v[52:55]
v_mfma_f32_16x16x32_bf16 v[100:103],v[220:223],v[200:203],v[100:103]
ds_read_b64_tr_b16 v[220:221],v248 offset:1024
ds_read_b64_tr_b16 v[222:223],v248 offset:7168
s_waitcnt lgkmcnt(10)
v_mfma_f32_16x16x32_bf16 v[8:11],v[224:227],v[184:187],v[8:11]
v_mfma_f32_16x16x32_bf16 v[56:59],v[224:227],v[188:191],v[56:59]
ds_read_b64_tr_b16 v[224:225],v249 offset:1024
ds_read_b64_tr_b16 v[226:227],v249 offset:7168
s_waitcnt lgkmcnt(10)
v_mfma_f32_16x16x32_bf16 v[12:15],v[204:207],v[184:187],v[12:15]
v_mfma_f32_16x16x32_bf16 v[60:63],v[204:207],v[188:191],v[60:63]
ds_read_b64_tr_b16 v[204:205],v246 offset:2048
ds_read_b64_tr_b16 v[206:207],v246 offset:8192
s_waitcnt lgkmcnt(10)
v_mfma_f32_16x16x32_bf16 v[16:19],v[208:211],v[184:187],v[16:19]
v_mfma_f32_16x16x32_bf16 v[64:67],v[208:211],v[188:191],v[64:67]
ds_read_b64_tr_b16 v[208:209],v247 offset:2048
ds_read_b64_tr_b16 v[210:211],v247 offset:8192
s_waitcnt lgkmcnt(10)
v_mfma_f32_16x16x32_bf16 v[20:23],v[212:215],v[184:187],v[20:23]
v_mfma_f32_16x16x32_bf16 v[68:71],v[212:215],v[188:191],v[68:71]
v_mfma_f32_16x16x32_bf16 v[104:107],v[212:215],v[200:203],v[104:107]
ds_read_b64_tr_b16 v[212:213],v248 offset:2048
ds_read_b64_tr_b16 v[214:215],v248 offset:8192
s_waitcnt lgkmcnt(10)
)A"
R"A(v_mfma_f32_16x16x32_bf16 v[24:27],v[216:219],v[184:187],v[24:27]
v_mfma_f32_16x16x32_bf16 v[72:75],v[216:219],v[188:191],v[72:75]
ds_read_b64_tr_b16 v[216:217],v249 offset:2048
ds_read_b64_tr_b16 v[218:219],v249 offset:8192
s_waitcnt lgkmcnt(10)
v_mfma_f32_16x16x32_bf16 v[28:31],v[220:223],v[184:187],v[28:31]
s_mov_b32 s90,s99
s_add_u32 s99,s99,12288
s_cmp_eq_u32 s99,36864
s_cmov_b32 s99,0
s_sub_u32 s46,s99,s90
v_add_u32 v246,s46,v246
v_add_u32 v247,s46,v247
v_add_u32 v248,s46,v248
v_add_u32 v249,s46,v249
v_mfma_f32_16x16x32_bf16 v[76:79],v[220:223],v[188:191],v[76:79]
s_waitcnt lgkmcnt(8)
v_mfma_f32_16x16x32_bf16 v[32:35],v[224:227],v[184:187],v[32:35]
v_mfma_f32_16x16x32_bf16 v[80:83],v[224:227],v[188:191],v[80:83]
s_cmp_lg_u32 s45,0
s_cbranch_scc1 W0_LOOP
s_cmp_eq_u32 s47,0
s_cselect_b32 s94,5,3
s_mul_i32 s94,s94,393216
s_waitcnt vmcnt(6) lgkmcnt(0)
s_barrier
ds_read_b128 v[220:223],v244
ds_read_b128 v[224:227],v245
v_mfma_f32_16x16x32_bf16 v[36:39],v[204:207],v[184:187],v[36:39]
v_exp_f32 v176,v176
s_mov_b32 m0,s96
v_mfma_f32_16x16x32_bf16 v[84:87],v[204:207],v[188:191],v[84:87]
v_exp_f32 v177,v177
s_cmp_lg_u32 s34,1
s_cbranch_scc1 LG11
buffer_load_dwordx4 v253,s[80:83],s94 offen lds
LG11:
v_mfma_f32_16x16x32_bf16 v[108:111],v[204:207],v[200:203],v[108:111]
ds_read_b128 v[204:207],v244 offset:1024
ds_read_b128 v[228:231],v250
ds_read_b128 v[232:235],v250 offset:1024
v_add_f32 v241,v241,v176
v_exp_f32 v178,v178
v_mfma_f32_16x16x32_bf16 v[40:43],v[208:211],v[184:187],v[40:43]
v_add_f32 v241,v241,v177
v_exp_f32 v179,v179
v_cvt_pk_bf16_f32 v194,v176,v177
v_mfma_f32_16x16x32_bf16 v[88:91],v[208:211],v[188:191],v[88:91]
ds_read_b128 v[208:211],v245 offset:1024
ds_read_b128 v[200:203],v250 offset:7168
v_add_f32 v241,v241,v178
v_exp_f32 v180,v180
s_add_u32 m0,s96,896
v_mfma_f32_16x16x32_bf16 v[44:47],v[212:215],v[184:187],v[44:47]
v_mov_b32 v236,v240
v_mov_b32 v237,v240
v_mov_b32 v238,v240
v_mov_b32 v239,v240
v_add_f32 v241,v241,v179
v_exp_f32 v181,v181
v_cvt_pk_bf16_f32 v195,v178,v179
s_cmp_lg_u32 s34,1
s_cbranch_scc1 LG12
buffer_load_dwordx4 v253,s[80:83],s94 offen offset:128 lds
LG12:
v_mfma_f32_16x16x32_bf16 v[92:95],v[212:215],v[188:191],v[92:95]
ds_read_b128 v[212:215],v244 offset:2048
v_add_f32 v242,v242,v180
v_exp_f32 v182,v182
v_mfma_f32_16x16x32_bf16 v[48:51],v[216:219],v[184:187],v[48:51]
v_add_f32 v242,v242,v181
v_exp_f32 v183,v183
v_cvt_pk_bf16_f32 v198,v180,v181
v_mfma_f32_16x16x32_bf16 v[96:99],v[216:219],v[188:191],v[96:99]
ds_read_b128 v[216:219],v245 offset:2048
v_add_f32 v242,v242,v182
s_add_u32 m0,s96,1792
s_waitcnt lgkmcnt(8)
v_mfma_f32_16x16x32_bf16 v[168:171],v[220:223],v[112:115],v[160:163]
v_add_f32 v242,v242,v183
v_cvt_pk_bf16_f32 v199,v182,v183
s_cmp_lg_u32 s34,1
s_cbranch_scc1 LG13
buffer_load_dwordx4 v253,s[80:83],s94 offen offset:256 lds
LG13:
v_mfma_f32_16x16x32_bf16 v[172:175],v[220:223],v[136:139],v[164:167]
s_waitcnt lgkmcnt(5)
v_mfma_f32_16x16x32_bf16 v[236:239],v[220:223],v[228:231],v[236:239]
ds_read_b128 v[220:223],v244 offset:6144
ds_read_b128 v[228:231],v250 offset:2048
v_mfma_f32_16x16x32_bf16 v[168:171],v[224:227],v[116:119],v[168:171]
s_mov_b32 m0,s97
v_mfma_f32_16x16x32_bf16 v[172:175],v[224:227],v[140:143],v[172:175]
buffer_load_dwordx4 v252,s[68:71],s95 offen nt lds
s_waitcnt lgkmcnt(6)
v_mfma_f32_16x16x32_bf16 v[236:239],v[224:227],v[232:235],v[236:239]
ds_read_b128 v[224:227],v245 offset:6144
ds_read_b128 v[232:235],v250 offset:3072
v_mfma_f32_16x16x32_bf16 v[168:171],v[204:207],v[120:123],v[168:171]
v_mfma_f32_16x16x32_bf16 v[172:175],v[204:207],v[144:147],v[172:175]
s_add_u32 m0,s97,896
s_waitcnt lgkmcnt(2)
v_mfma_f32_16x16x32_bf16 v[236:239],v[204:207],v[228:231],v[236:239]
ds_read_b128 v[204:207],v244 offset:7168
ds_read_b128 v[228:231],v250 offset:4096
buffer_load_dwordx4 v252,s[68:71],s95 offen offset:128 nt lds
v_mfma_f32_16x16x32_bf16 v[168:171],v[208:211],v[124:127],v[168:171]
v_dot2c_f32_bf16 v243,s55,v200
v_mfma_f32_16x16x32_bf16 v[172:175],v[208:211],v[148:151],v[172:175]
s_waitcnt lgkmcnt(2)
v_mfma_f32_16x16x32_bf16 v[236:239],v[208:211],v[232:235],v[236:239]
ds_read_b128 v[208:211],v245 offset:7168
ds_read_b128 v[232:235],v250 offset:5120
s_add_u32 m0,s97,1792
v_mfma_f32_16x16x32_bf16 v[168:171],v[212:215],v[128:131],v[168:171]
v_dot2c_f32_bf16 v243,s55,v201
buffer_load_dwordx4 v252,s[68:71],s95 offen offset:256 nt lds
v_mfma_f32_16x16x32_bf16 v[172:175],v[212:215],v[152:155],v[172:175]
s_waitcnt lgkmcnt(2)
v_mfma_f32_16x16x32_bf16 v[236:239],v[212:215],v[228:231],v[236:239]
ds_read_b128 v[212:215],v244 offset:8192
v_mfma_f32_16x16x32_bf16 v[168:171],v[216:219],v[132:135],v[168:171]
v_dot2c_f32_bf16 v243,s55,v202
v_mfma_f32_16x16x32_bf16 v[172:175],v[216:219],v[156:159],v[172:175]
s_waitcnt lgkmcnt(1)
v_mfma_f32_16x16x32_bf16 v[236:239],v[216:219],v[232:235],v[236:239]
ds_read_b128 v[216:219],v245 offset:8192
s_add_u32 s89,s48,36864
s_add_u32 s96,s96,12288
s_cmp_eq_u32 s96,s89
s_cselect_b32 s96,s48,s96
v_mfma_f32_16x16x32_bf16 v[176:179],v[220:223],v[112:115],v[160:163]
v_dot2c_f32_bf16 v243,s55,v203
v_mfma_f32_16x16x32_bf16 v[180:183],v[220:223],v[136:139],v[164:167]
ds_read_b64_tr_b16 v[220:221],v246
ds_read_b64_tr_b16 v[222:223],v246 offset:6144
s_cmp_eq_u32 s47,0
s_cselect_b32 s94,0,1
s_mul_i32 s94,s94,393216
v_mfma_f32_16x16x32_bf16 v[176:179],v[224:227],v[116:119],v[176:179]
v_mfma_f32_16x16x32_bf16 v[180:183],v[224:227],v[140:143],v[180:183]
ds_read_b64_tr_b16 v[224:225],v247
ds_read_b64_tr_b16 v[226:227],v247 offset:6144
v_exp_f32 v236,v236
v_exp_f32 v168,v168
s_mov_b32 s90,s98
s_add_u32 s98,s98,12288
s_cmp_eq_u32 s98,36864
s_cmov_b32 s98,0
s_sub_u32 s46,s98,s90
v_add_u32 v244,s46,v244
v_add_u32 v245,s46,v245
v_mfma_f32_16x16x32_bf16 v[176:179],v[204:207],v[120:123],v[176:179]
v_exp_f32 v237,v237
v_exp_f32 v169,v169
v_mfma_f32_16x16x32_bf16 v[180:183],v[204:207],v[144:147],v[180:183]
ds_read_b64_tr_b16 v[204:205],v248
ds_read_b64_tr_b16 v[206:207],v248 offset:6144
v_exp_f32 v238,v238
v_add_f32 v241,v241,v168
v_exp_f32 v170,v170
v_mfma_f32_16x16x32_bf16 v[176:179],v[208:211],v[124:127],v[176:179]
v_exp_f32 v239,v239
v_add_f32 v241,v241,v169
v_exp_f32 v171,v171
v_cvt_pk_bf16_f32 v184,v168,v169
v_mfma_f32_16x16x32_bf16 v[180:183],v[208:211],v[148:151],v[180:183]
ds_read_b64_tr_b16 v[208:209],v249
ds_read_b64_tr_b16 v[210:211],v249 offset:6144
v_add_f32 v241,v241,v170
v_exp_f32 v172,v172
s_waitcnt lgkmcnt(9)
v_mfma_f32_16x16x32_bf16 v[176:179],v[212:215],v[128:131],v[176:179]
v_cvt_pk_bf16_f32 v0,v236,v237
v_cvt_pk_bf16_f32 v1,v238,v239
v_add_f32 v241,v241,v171
v_exp_f32 v173,v173
v_cvt_pk_bf16_f32 v185,v170,v171
v_mfma_f32_16x16x32_bf16 v[180:183],v[212:215],v[152:155],v[180:183]
ds_read_b64_tr_b16 v[212:213],v246 offset:1024
ds_read_b64_tr_b16 v[214:215],v246 offset:7168
ds_write_b64 v250,v[0:1] offset:6144
v_add_f32 v242,v242,v172
v_exp_f32 v174,v174
s_waitcnt lgkmcnt(11)
v_mfma_f32_16x16x32_bf16 v[176:179],v[216:219],v[132:135],v[176:179]
v_add_f32 v242,v242,v173
v_exp_f32 v175,v175
v_cvt_pk_bf16_f32 v188,v172,v173
v_mfma_f32_16x16x32_bf16 v[180:183],v[216:219],v[156:159],v[180:183]
ds_read_b64_tr_b16 v[216:217],v247 offset:1024
ds_read_b64_tr_b16 v[218:219],v247 offset:7168
v_add_f32 v242,v242,v174
s_waitcnt lgkmcnt(11)
v_mfma_f32_16x16x32_bf16 v[4:7],v[220:223],v[192:195],v[4:7]
v_add_f32 v242,v242,v175
v_cvt_pk_bf16_f32 v189,v174,v175
v_mfma_f32_16x16x32_bf16 v[52:55],v[220:223],v[196:199],v[52:55]
v_mfma_f32_16x16x32_bf16 v[100:103],v[220:223],v[200:203],v[100:103]
ds_read_b64_tr_b16 v[220:221],v248 offset:1024
ds_read_b64_tr_b16 v[222:223],v248 offset:7168
s_waitcnt lgkmcnt(11)
v_mfma_f32_16x16x32_bf16 v[8:11],v[224:227],v[192:195],v[8:11]
v_mfma_f32_16x16x32_bf16 v[56:59],v[224:227],v[196:199],v[56:59]
ds_read_b64_tr_b16 v[224:225],v249 offset:1024
ds_read_b64_tr_b16 v[226:227],v249 offset:7168
s_waitcnt lgkmcnt(11)
v_mfma_f32_16x16x32_bf16 v[12:15],v[204:207],v[192:195],v[12:15]
v_mfma_f32_16x16x32_bf16 v[60:63],v[204:207],v[196:199],v[60:63]
ds_read_b64_tr_b16 v[204:205],v246 offset:2048
ds_read_b64_tr_b16 v[206:207],v246 offset:8192
s_waitcnt lgkmcnt(11)
v_mfma_f32_16x16x32_bf16 v[16:19],v[208:211],v[192:195],v[16:19]
v_mfma_f32_16x16x32_bf16 v[64:67],v[208:211],v[196:199],v[64:67]
ds_read_b64_tr_b16 v[208:209],v247 offset:2048
ds_read_b64_tr_b16 v[210:211],v247 offset:8192
s_waitcnt lgkmcnt(11)
v_mfma_f32_16x16x32_bf16 v[20:23],v[212:215],v[192:195],v[20:23]
v_mfma_f32_16x16x32_bf16 v[68:71],v[212:215],v[196:199],v[68:71]
v_mfma_f32_16x16x32_bf16 v[104:107],v[212:215],v[200:203],v[104:107]
ds_read_b64_tr_b16 v[212:213],v248 offset:2048
ds_read_b64_tr_b16 v[214:215],v248 offset:8192
s_waitcnt lgkmcnt(10)
v_mfma_f32_16x16x32_bf16 v[24:27],v[216:219],v[192:195],v[24:27]
v_mfma_f32_16x16x32_bf16 v[72:75],v[216:219],v[196:199],v[72:75]
ds_read_b64_tr_b16 v[216:217],v249 offset:2048
ds_read_b64_tr_b16 v[218:219],v249 offset:8192
s_waitcnt lgkmcnt(10)
v_mfma_f32_16x16x32_bf16 v[28:31],v[220:223],v[192:195],v[28:31]
s_mov_b32 s90,s99
s_add_u32 s99,s99,12288
s_cmp_eq_u32 s99,36864
s_cmov_b32 s99,0
s_sub_u32 s46,s99,s90
v_add_u32 v246,s46,v246
v_add_u32 v247,s46,v247
v_add_u32 v248,s46,v248
v_add_u32 v249,s46,v249
v_mfma_f32_16x16x32_bf16 v[76:79],v[220:223],v[196:199],v[76:79]
s_waitcnt lgkmcnt(8)
v_mfma_f32_16x16x32_bf16 v[32:35],v[224:227],v[192:195],v[32:35]
v_mfma_f32_16x16x32_bf16 v[80:83],v[224:227],v[196:199],v[80:83]
s_waitcnt vmcnt(3) lgkmcnt(0)
s_barrier
ds_read_b128 v[220:223],v244
ds_read_b128 v[224:227],v245
v_mfma_f32_16x16x32_bf16 v[36:39],v[204:207],v[192:195],v[36:39]
v_exp_f32 v176,v176
s_mov_b32 m0,s96
v_mfma_f32_16x16x32_bf16 v[84:87],v[204:207],v[196:199],v[84:87]
v_exp_f32 v177,v177
s_cmp_lg_u32 s34,1
s_cbranch_scc1 LG14
buffer_load_dwordx4 v253,s[80:83],s94 offen lds
LG14:
v_mfma_f32_16x16x32_bf16 v[108:111],v[204:207],v[200:203],v[108:111]
ds_read_b128 v[204:207],v244 offset:1024
v_add_f32 v241,v241,v176
v_exp_f32 v178,v178
v_mfma_f32_16x16x32_bf16 v[40:43],v[208:211],v[192:195],v[40:43]
v_add_f32 v241,v241,v177
v_exp_f32 v179,v179
v_cvt_pk_bf16_f32 v186,v176,v177
v_mfma_f32_16x16x32_bf16 v[88:91],v[208:211],v[196:199],v[88:91]
ds_read_b128 v[208:211],v245 offset:1024
ds_read_b128 v[200:203],v250 offset:6144
v_add_f32 v241,v241,v178
v_exp_f32 v180,v180
s_add_u32 m0,s96,896
v_mfma_f32_16x16x32_bf16 v[44:47],v[212:215],v[192:195],v[44:47]
v_add_f32 v241,v241,v179
v_exp_f32 v181,v181
v_cvt_pk_bf16_f32 v187,v178,v179
s_cmp_lg_u32 s34,1
s_cbranch_scc1 LG15
buffer_load_dwordx4 v253,s[80:83],s94 offen offset:128 lds
LG15:
v_mfma_f32_16x16x32_bf16 v[92:95],v[212:215],v[196:199],v[92:95]
ds_read_b128 v[212:215],v244 offset:2048
v_add_f32 v242,v242,v180
v_exp_f32 v182,v182
v_mfma_f32_16x16x32_bf16 v[48:51],v[216:219],v[192:195],v[48:51]
v_add_f32 v242,v242,v181
v_exp_f32 v183,v183
v_cvt_pk_bf16_f32 v190,v180,v181
v_mfma_f32_16x16x32_bf16 v[96:99],v[216:219],v[196:199],v[96:99]
ds_read_b128 v[216:219],v245 offset:2048
v_add_f32 v242,v242,v182
s_add_u32 m0,s96,1792
s_waitcnt lgkmcnt(6)
v_mfma_f32_16x16x32_bf16 v[168:171],v[220:223],v[112:115],v[160:163]
v_add_f32 v242,v242,v183
v_cvt_pk_bf16_f32 v191,v182,v183
s_cmp_lg_u32 s34,1
s_cbranch_scc1 LG16
buffer_load_dwordx4 v253,s[80:83],s94 offen offset:256 lds
LG16:
v_mfma_f32_16x16x32_bf16 v[172:175],v[220:223],v[136:139],v[164:167]
ds_read_b128 v[220:223],v244 offset:6144
s_waitcnt lgkmcnt(6)
v_mfma_f32_16x16x32_bf16 v[168:171],v[224:227],v[116:119],v[168:171]
v_mfma_f32_16x16x32_bf16 v[172:175],v[224:227],v[140:143],v[172:175]
ds_read_b128 v[224:227],v245 offset:6144
s_waitcnt lgkmcnt(6)
v_mfma_f32_16x16x32_bf16 v[168:171],v[204:207],v[120:123],v[168:171]
v_mfma_f32_16x16x32_bf16 v[172:175],v[204:207],v[144:147],v[172:175]
ds_read_b128 v[204:207],v244 offset:7168
s_add_u32 s89,s48,36864
s_add_u32 s96,s96,12288
s_cmp_eq_u32 s96,s89
s_cselect_b32 s96,s48,s96
s_waitcnt lgkmcnt(6)
v_mfma_f32_16x16x32_bf16 v[168:171],v[208:211],v[124:127],v[168:171]
s_waitcnt lgkmcnt(5)
v_dot2c_f32_bf16 v243,s55,v200
v_mfma_f32_16x16x32_bf16 v[172:175],v[208:211],v[148:151],v[172:175]
ds_read_b128 v[208:211],v245 offset:7168
s_cmp_eq_u32 s47,0
s_cselect_b32 s94,4,2
s_mul_i32 s94,s94,393216
s_waitcnt lgkmcnt(5)
v_mfma_f32_16x16x32_bf16 v[168:171],v[212:215],v[128:131],v[168:171]
v_dot2c_f32_bf16 v243,s55,v201
v_mfma_f32_16x16x32_bf16 v[172:175],v[212:215],v[152:155],v[172:175]
ds_read_b128 v[212:215],v244 offset:8192
s_xor_b32 s90,s33,1
s_mul_i32 s90,s90,18432
s_lshl_b32 s97,s84,10
s_add_u32 s97,s97,s51
s_add_u32 s97,s97,73728
s_add_u32 s97,s97,s90
s_mov_b32 s95,0
s_waitcnt lgkmcnt(5)
v_mfma_f32_16x16x32_bf16 v[168:171],v[216:219],v[132:135],v[168:171]
v_dot2c_f32_bf16 v243,s55,v202
v_mfma_f32_16x16x32_bf16 v[172:175],v[216:219],v[156:159],v[172:175]
ds_read_b128 v[216:219],v245 offset:8192
s_waitcnt lgkmcnt(5)
v_mfma_f32_16x16x32_bf16 v[176:179],v[220:223],v[112:115],v[160:163]
v_dot2c_f32_bf16 v243,s55,v203
v_mfma_f32_16x16x32_bf16 v[180:183],v[220:223],v[136:139],v[164:167]
ds_read_b64_tr_b16 v[220:221],v246
ds_read_b64_tr_b16 v[222:223],v246 offset:6144
s_waitcnt lgkmcnt(6)
v_mfma_f32_16x16x32_bf16 v[176:179],v[224:227],v[116:119],v[176:179]
v_mfma_f32_16x16x32_bf16 v[180:183],v[224:227],v[140:143],v[180:183]
ds_read_b64_tr_b16 v[224:225],v247
ds_read_b64_tr_b16 v[226:227],v247 offset:6144
v_exp_f32 v168,v168
s_mov_b32 s90,s98
s_add_u32 s98,s98,12288
s_cmp_eq_u32 s98,36864
s_cmov_b32 s98,0
s_sub_u32 s46,s98,s90
v_add_u32 v244,s46,v244
v_add_u32 v245,s46,v245
s_waitcnt lgkmcnt(7)
v_mfma_f32_16x16x32_bf16 v[176:179],v[204:207],v[120:123],v[176:179]
v_exp_f32 v169,v169
v_mfma_f32_16x16x32_bf16 v[180:183],v[204:207],v[144:147],v[180:183]
ds_read_b64_tr_b16 v[204:205],v248
ds_read_b64_tr_b16 v[206:207],v248 offset:6144
v_add_f32 v241,v241,v168
v_exp_f32 v170,v170
s_waitcnt lgkmcnt(8)
v_mfma_f32_16x16x32_bf16 v[176:179],v[208:211],v[124:127],v[176:179]
v_add_f32 v241,v241,v169
v_exp_f32 v171,v171
v_cvt_pk_bf16_f32 v192,v168,v169
v_mfma_f32_16x16x32_bf16 v[180:183],v[208:211],v[148:151],v[180:183]
ds_read_b64_tr_b16 v[208:209],v249
ds_read_b64_tr_b16 v[210:211],v249 offset:6144
v_add_f32 v241,v241,v170
v_exp_f32 v172,v172
s_waitcnt lgkmcnt(9)
v_mfma_f32_16x16x32_bf16 v[176:179],v[212:215],v[128:131],v[176:179]
v_add_f32 v241,v241,v171
v_exp_f32 v173,v173
v_cvt_pk_bf16_f32 v193,v170,v171
v_mfma_f32_16x16x32_bf16 v[180:183],v[212:215],v[152:155],v[180:183]
ds_read_b64_tr_b16 v[212:213],v246 offset:1024
ds_read_b64_tr_b16 v[214:215],v246 offset:7168
v_add_f32 v242,v242,v172
v_exp_f32 v174,v174
s_waitcnt lgkmcnt(10)
v_mfma_f32_16x16x32_bf16 v[176:179],v[216:219],v[132:135],v[176:179]
v_add_f32 v242,v242,v173
v_exp_f32 v175,v175
v_cvt_pk_bf16_f32 v196,v172,v173
v_mfma_f32_16x16x32_bf16 v[180:183],v[216:219],v[156:159],v[180:183]
ds_read_b64_tr_b16 v[216:217],v247 offset:1024
ds_read_b64_tr_b16 v[218:219],v247 offset:7168
v_add_f32 v242,v242,v174
s_waitcnt lgkmcnt(10)
v_mfma_f32_16x16x32_bf16 v[4:7],v[220:223],v[184:187],v[4:7]
v_add_f32 v242,v242,v175
v_cvt_pk_bf16_f32 v197,v174,v175
v_mfma_f32_16x16x32_bf16 v[52:55],v[220:223],v[188:191],v[52:55]
v_mfma_f32_16x16x32_bf16 v[100:103],v[220:223],v[200:203],v[100:103]
ds_read_b64_tr_b16 v[220:221],v248 offset:1024
)A"
R"A(ds_read_b64_tr_b16 v[222:223],v248 offset:7168
s_waitcnt lgkmcnt(10)
v_mfma_f32_16x16x32_bf16 v[8:11],v[224:227],v[184:187],v[8:11]
v_mfma_f32_16x16x32_bf16 v[56:59],v[224:227],v[188:191],v[56:59]
ds_read_b64_tr_b16 v[224:225],v249 offset:1024
ds_read_b64_tr_b16 v[226:227],v249 offset:7168
s_waitcnt lgkmcnt(10)
v_mfma_f32_16x16x32_bf16 v[12:15],v[204:207],v[184:187],v[12:15]
v_mfma_f32_16x16x32_bf16 v[60:63],v[204:207],v[188:191],v[60:63]
ds_read_b64_tr_b16 v[204:205],v246 offset:2048
ds_read_b64_tr_b16 v[206:207],v246 offset:8192
s_waitcnt lgkmcnt(10)
v_mfma_f32_16x16x32_bf16 v[16:19],v[208:211],v[184:187],v[16:19]
v_mfma_f32_16x16x32_bf16 v[64:67],v[208:211],v[188:191],v[64:67]
ds_read_b64_tr_b16 v[208:209],v247 offset:2048
ds_read_b64_tr_b16 v[210:211],v247 offset:8192
s_waitcnt lgkmcnt(10)
v_mfma_f32_16x16x32_bf16 v[20:23],v[212:215],v[184:187],v[20:23]
v_mfma_f32_16x16x32_bf16 v[68:71],v[212:215],v[188:191],v[68:71]
v_mfma_f32_16x16x32_bf16 v[104:107],v[212:215],v[200:203],v[104:107]
ds_read_b64_tr_b16 v[212:213],v248 offset:2048
ds_read_b64_tr_b16 v[214:215],v248 offset:8192
s_waitcnt lgkmcnt(10)
v_mfma_f32_16x16x32_bf16 v[24:27],v[216:219],v[184:187],v[24:27]
v_mfma_f32_16x16x32_bf16 v[72:75],v[216:219],v[188:191],v[72:75]
ds_read_b64_tr_b16 v[216:217],v249 offset:2048
ds_read_b64_tr_b16 v[218:219],v249 offset:8192
s_waitcnt lgkmcnt(10)
v_mfma_f32_16x16x32_bf16 v[28:31],v[220:223],v[184:187],v[28:31]
s_mov_b32 s90,s99
s_add_u32 s99,s99,12288
s_cmp_eq_u32 s99,36864
s_cmov_b32 s99,0
s_sub_u32 s46,s99,s90
v_add_u32 v246,s46,v246
v_add_u32 v247,s46,v247
v_add_u32 v248,s46,v248
v_add_u32 v249,s46,v249
v_mfma_f32_16x16x32_bf16 v[76:79],v[220:223],v[188:191],v[76:79]
s_waitcnt lgkmcnt(8)
v_mfma_f32_16x16x32_bf16 v[32:35],v[224:227],v[184:187],v[32:35]
v_mfma_f32_16x16x32_bf16 v[80:83],v[224:227],v[188:191],v[80:83]
s_waitcnt vmcnt(0) lgkmcnt(0)
s_barrier
ds_read_b64_tr_b16 v[220:221],v246
ds_read_b64_tr_b16 v[222:223],v246 offset:6144
ds_read_b64_tr_b16 v[224:225],v247
ds_read_b64_tr_b16 v[226:227],v247 offset:6144
v_mfma_f32_16x16x32_bf16 v[36:39],v[204:207],v[184:187],v[36:39]
v_exp_f32 v176,v176
s_mov_b32 m0,s96
v_mfma_f32_16x16x32_bf16 v[84:87],v[204:207],v[188:191],v[84:87]
v_exp_f32 v177,v177
s_cmp_lg_u32 s34,1
s_cbranch_scc1 LG17
buffer_load_dwordx4 v253,s[80:83],s94 offen lds
LG17:
v_mfma_f32_16x16x32_bf16 v[108:111],v[204:207],v[200:203],v[108:111]
ds_read_b64_tr_b16 v[204:205],v248
ds_read_b64_tr_b16 v[206:207],v248 offset:6144
v_add_f32 v241,v241,v176
v_exp_f32 v178,v178
s_add_u32 m0,s96,896
v_mfma_f32_16x16x32_bf16 v[40:43],v[208:211],v[184:187],v[40:43]
v_add_f32 v241,v241,v177
v_exp_f32 v179,v179
v_cvt_pk_bf16_f32 v194,v176,v177
s_cmp_lg_u32 s34,1
s_cbranch_scc1 LG18
buffer_load_dwordx4 v253,s[80:83],s94 offen offset:128 lds
LG18:
v_mfma_f32_16x16x32_bf16 v[88:91],v[208:211],v[188:191],v[88:91]
ds_read_b64_tr_b16 v[208:209],v249
ds_read_b64_tr_b16 v[210:211],v249 offset:6144
ds_read_b128 v[200:203],v250 offset:7168
v_add_f32 v241,v241,v178
v_exp_f32 v180,v180
s_add_u32 m0,s96,1792
v_mfma_f32_16x16x32_bf16 v[44:47],v[212:215],v[184:187],v[44:47]
v_add_f32 v241,v241,v179
v_exp_f32 v181,v181
v_cvt_pk_bf16_f32 v195,v178,v179
s_cmp_lg_u32 s34,1
s_cbranch_scc1 LG19
buffer_load_dwordx4 v253,s[80:83],s94 offen offset:256 lds
LG19:
v_mfma_f32_16x16x32_bf16 v[92:95],v[212:215],v[188:191],v[92:95]
ds_read_b64_tr_b16 v[212:213],v246 offset:1024
ds_read_b64_tr_b16 v[214:215],v246 offset:7168
v_add_f32 v242,v242,v180
v_exp_f32 v182,v182
s_mov_b32 m0,s97
s_mov_b32 s88,s95
v_mfma_f32_16x16x32_bf16 v[48:51],v[216:219],v[184:187],v[48:51]
v_add_f32 v242,v242,v181
v_exp_f32 v183,v183
v_cvt_pk_bf16_f32 v198,v180,v181
s_cmp_lg_u32 s34,1
s_cbranch_scc1 LG20
buffer_load_dwordx4 v254,s[76:79],s88 offen lds
LG20:
v_mfma_f32_16x16x32_bf16 v[96:99],v[216:219],v[188:191],v[96:99]
ds_read_b64_tr_b16 v[216:217],v247 offset:1024
ds_read_b64_tr_b16 v[218:219],v247 offset:7168
v_add_f32 v242,v242,v182
v_add_f32 v242,v242,v183
v_cvt_pk_bf16_f32 v199,v182,v183
s_add_u32 m0,s97,4096
s_add_u32 s88,s95,40960
s_waitcnt lgkmcnt(11)
v_mfma_f32_16x16x32_bf16 v[4:7],v[220:223],v[192:195],v[4:7]
s_cmp_lg_u32 s34,1
s_cbranch_scc1 LG21
buffer_load_dwordx4 v254,s[76:79],s88 offen lds
LG21:
v_mfma_f32_16x16x32_bf16 v[52:55],v[220:223],v[196:199],v[52:55]
s_add_u32 m0,s97,8192
s_add_u32 s88,s95,81920
s_waitcnt lgkmcnt(4)
v_mfma_f32_16x16x32_bf16 v[100:103],v[220:223],v[200:203],v[100:103]
ds_read_b64_tr_b16 v[220:221],v248 offset:1024
ds_read_b64_tr_b16 v[222:223],v248 offset:7168
s_cmp_lg_u32 s34,1
s_cbranch_scc1 LG22
buffer_load_dwordx4 v254,s[76:79],s88 offen lds
LG22:
v_mfma_f32_16x16x32_bf16 v[8:11],v[224:227],v[192:195],v[8:11]
s_add_u32 m0,s97,12288
s_add_u32 s88,s95,122880
v_mov_b32 v164,v241
s_nop 1
v_permlane32_swap_b32 v241,v164
v_add_f32 v241,v241,v164
v_mov_b32 v164,v241
s_nop 1
v_permlane16_swap_b32 v241,v164
v_add_f32 v241,v241,v164
v_rcp_f32 v0,v241
v_mfma_f32_16x16x32_bf16 v[56:59],v[224:227],v[196:199],v[56:59]
ds_read_b64_tr_b16 v[224:225],v249 offset:1024
ds_read_b64_tr_b16 v[226:227],v249 offset:7168
s_cmp_lg_u32 s34,1
s_cbranch_scc1 LG23
buffer_load_dwordx4 v254,s[76:79],s88 offen lds
LG23:
v_mov_b32 v165,v242
s_nop 1
v_permlane32_swap_b32 v242,v165
v_add_f32 v242,v242,v165
v_mov_b32 v165,v242
s_nop 1
v_permlane16_swap_b32 v242,v165
v_add_f32 v242,v242,v165
v_rcp_f32 v2,v242
v_mfma_f32_16x16x32_bf16 v[12:15],v[204:207],v[192:195],v[12:15]
s_add_u32 m0,s97,16384
s_add_u32 s88,s95,163840
v_mfma_f32_16x16x32_bf16 v[60:63],v[204:207],v[196:199],v[60:63]
ds_read_b64_tr_b16 v[204:205],v246 offset:2048
ds_read_b64_tr_b16 v[206:207],v246 offset:8192
s_cmp_lg_u32 s34,1
s_cbranch_scc1 LG24
s_cmp_ge_u32 s84,2
s_cbranch_scc1 LX25
buffer_load_dwordx4 v254,s[76:79],s88 offen lds
LX25:
LG24:
v_mfma_f32_16x16x32_bf16 v[16:19],v[208:211],v[192:195],v[16:19]
v_dot2c_f32_bf16 v243,s55,v200
v_mfma_f32_16x16x32_bf16 v[64:67],v[208:211],v[196:199],v[64:67]
ds_read_b64_tr_b16 v[208:209],v247 offset:2048
ds_read_b64_tr_b16 v[210:211],v247 offset:8192
s_waitcnt lgkmcnt(10)
v_mfma_f32_16x16x32_bf16 v[20:23],v[212:215],v[192:195],v[20:23]
v_dot2c_f32_bf16 v243,s55,v201
v_mfma_f32_16x16x32_bf16 v[68:71],v[212:215],v[196:199],v[68:71]
v_mfma_f32_16x16x32_bf16 v[104:107],v[212:215],v[200:203],v[104:107]
ds_read_b64_tr_b16 v[212:213],v248 offset:2048
ds_read_b64_tr_b16 v[214:215],v248 offset:8192
s_waitcnt lgkmcnt(10)
v_mfma_f32_16x16x32_bf16 v[24:27],v[216:219],v[192:195],v[24:27]
v_dot2c_f32_bf16 v243,s55,v202
v_mfma_f32_16x16x32_bf16 v[72:75],v[216:219],v[196:199],v[72:75]
ds_read_b64_tr_b16 v[216:217],v249 offset:2048
ds_read_b64_tr_b16 v[218:219],v249 offset:8192
s_waitcnt lgkmcnt(10)
v_mfma_f32_16x16x32_bf16 v[28:31],v[220:223],v[192:195],v[28:31]
v_dot2c_f32_bf16 v243,s55,v203
v_mfma_f32_16x16x32_bf16 v[76:79],v[220:223],v[196:199],v[76:79]
s_waitcnt lgkmcnt(8)
v_mfma_f32_16x16x32_bf16 v[32:35],v[224:227],v[192:195],v[32:35]
v_mfma_f32_16x16x32_bf16 v[80:83],v[224:227],v[196:199],v[80:83]
v_mov_b32 v166,v243
s_nop 1
v_permlane32_swap_b32 v243,v166
v_add_f32 v243,v243,v166
v_mov_b32 v166,v243
s_nop 1
v_permlane16_swap_b32 v243,v166
v_add_f32 v243,v243,v166
v_rcp_f32 v160,v243
s_waitcnt lgkmcnt(6)
v_mfma_f32_16x16x32_bf16 v[36:39],v[204:207],v[192:195],v[36:39]
s_mov_b32 s90,0x5d800000
v_cmp_ngt_f32 s[92:93],s90,v241
v_cmp_ngt_f32 vcc,s90,v242
s_nop 0
s_or_b64 s[92:93],s[92:93],vcc
v_cmp_ngt_f32 vcc,s90,v243
s_nop 0
s_or_b64 s[92:93],s[92:93],vcc
s_cselect_b32 s90,1,0
v_mov_b32 v164,s90
s_lshl_b32 s91,s47,4
s_add_u32 s91,s91,153600
v_mov_b32 v165,s91
ds_write_b32 v165,v164
v_mbcnt_lo_u32_b32 v167,-1,0
v_mbcnt_hi_u32_b32 v167,-1,v167
v_lshlrev_b32 v167,4,v167
s_cmp_eq_u32 s33,0
s_movk_i32 s91,18432
s_cselect_b32 s91,12288,s91
s_add_u32 s91,s91,s51
s_add_u32 s91,s91,73728
v_mbcnt_lo_u32_b32 v162,-1,0
v_mbcnt_hi_u32_b32 v162,-1,v162
v_lshlrev_b32 v162,3,v162
v_add_u32 v162,s91,v162
v_mfma_f32_16x16x32_bf16 v[84:87],v[204:207],v[196:199],v[84:87]
v_pk_mul_f32 v[4:5],v[4:5],v[0:1] op_sel_hi:[1,0]
v_pk_mul_f32 v[6:7],v[6:7],v[0:1] op_sel_hi:[1,0]
v_pk_mul_f32 v[8:9],v[8:9],v[0:1] op_sel_hi:[1,0]
v_pk_mul_f32 v[10:11],v[10:11],v[0:1] op_sel_hi:[1,0]
v_cvt_pk_bf16_f32 v4,v4,v5
v_cvt_pk_bf16_f32 v5,v6,v7
v_cvt_pk_bf16_f32 v6,v8,v9
v_cvt_pk_bf16_f32 v7,v10,v11
s_mov_b32 s91,0x0
buffer_store_dwordx4 v[4:7],v167,s[72:75],s91 offen sc1
v_mfma_f32_16x16x32_bf16 v[108:111],v[204:207],v[200:203],v[108:111]
v_pk_mul_f32 v[52:53],v[52:53],v[2:3] op_sel_hi:[1,0]
v_pk_mul_f32 v[54:55],v[54:55],v[2:3] op_sel_hi:[1,0]
v_pk_mul_f32 v[56:57],v[56:57],v[2:3] op_sel_hi:[1,0]
v_pk_mul_f32 v[58:59],v[58:59],v[2:3] op_sel_hi:[1,0]
v_cvt_pk_bf16_f32 v52,v52,v53
v_cvt_pk_bf16_f32 v53,v54,v55
v_cvt_pk_bf16_f32 v54,v56,v57
v_cvt_pk_bf16_f32 v55,v58,v59
s_mov_b32 s91,0x30000
buffer_store_dwordx4 v[52:55],v167,s[72:75],s91 offen sc1
s_waitcnt lgkmcnt(5)
v_mfma_f32_16x16x32_bf16 v[40:43],v[208:211],v[192:195],v[40:43]
v_pk_mul_f32 v[12:13],v[12:13],v[0:1] op_sel_hi:[1,0]
v_pk_mul_f32 v[14:15],v[14:15],v[0:1] op_sel_hi:[1,0]
v_pk_mul_f32 v[16:17],v[16:17],v[0:1] op_sel_hi:[1,0]
v_pk_mul_f32 v[18:19],v[18:19],v[0:1] op_sel_hi:[1,0]
v_cvt_pk_bf16_f32 v12,v12,v13
v_cvt_pk_bf16_f32 v13,v14,v15
v_cvt_pk_bf16_f32 v14,v16,v17
v_cvt_pk_bf16_f32 v15,v18,v19
s_mov_b32 s91,0x400
buffer_store_dwordx4 v[12:15],v167,s[72:75],s91 offen sc1
v_mfma_f32_16x16x32_bf16 v[88:91],v[208:211],v[196:199],v[88:91]
v_pk_mul_f32 v[60:61],v[60:61],v[2:3] op_sel_hi:[1,0]
v_pk_mul_f32 v[62:63],v[62:63],v[2:3] op_sel_hi:[1,0]
v_pk_mul_f32 v[64:65],v[64:65],v[2:3] op_sel_hi:[1,0]
v_pk_mul_f32 v[66:67],v[66:67],v[2:3] op_sel_hi:[1,0]
v_cvt_pk_bf16_f32 v60,v60,v61
v_cvt_pk_bf16_f32 v61,v62,v63
v_cvt_pk_bf16_f32 v62,v64,v65
v_cvt_pk_bf16_f32 v63,v66,v67
s_mov_b32 s91,0x30400
buffer_store_dwordx4 v[60:63],v167,s[72:75],s91 offen sc1
s_waitcnt lgkmcnt(3)
v_mfma_f32_16x16x32_bf16 v[44:47],v[212:215],v[192:195],v[44:47]
v_pk_mul_f32 v[100:101],v[100:101],v[160:161] op_sel_hi:[1,0]
v_pk_mul_f32 v[102:103],v[102:103],v[160:161] op_sel_hi:[1,0]
v_cvt_pk_bf16_f32 v100,v100,v101
v_cvt_pk_bf16_f32 v101,v102,v103
v_mfma_f32_16x16x32_bf16 v[92:95],v[212:215],v[196:199],v[92:95]
v_pk_mul_f32 v[20:21],v[20:21],v[0:1] op_sel_hi:[1,0]
v_pk_mul_f32 v[22:23],v[22:23],v[0:1] op_sel_hi:[1,0]
v_pk_mul_f32 v[24:25],v[24:25],v[0:1] op_sel_hi:[1,0]
v_pk_mul_f32 v[26:27],v[26:27],v[0:1] op_sel_hi:[1,0]
v_cvt_pk_bf16_f32 v20,v20,v21
v_cvt_pk_bf16_f32 v21,v22,v23
v_cvt_pk_bf16_f32 v22,v24,v25
v_cvt_pk_bf16_f32 v23,v26,v27
s_mov_b32 s91,0x800
buffer_store_dwordx4 v[20:23],v167,s[72:75],s91 offen sc1
s_waitcnt lgkmcnt(1)
v_mfma_f32_16x16x32_bf16 v[48:51],v[216:219],v[192:195],v[48:51]
v_pk_mul_f32 v[68:69],v[68:69],v[2:3] op_sel_hi:[1,0]
v_pk_mul_f32 v[70:71],v[70:71],v[2:3] op_sel_hi:[1,0]
v_pk_mul_f32 v[72:73],v[72:73],v[2:3] op_sel_hi:[1,0]
v_pk_mul_f32 v[74:75],v[74:75],v[2:3] op_sel_hi:[1,0]
v_cvt_pk_bf16_f32 v68,v68,v69
v_cvt_pk_bf16_f32 v69,v70,v71
v_cvt_pk_bf16_f32 v70,v72,v73
v_cvt_pk_bf16_f32 v71,v74,v75
s_mov_b32 s91,0x30800
buffer_store_dwordx4 v[68:71],v167,s[72:75],s91 offen sc1
v_mfma_f32_16x16x32_bf16 v[96:99],v[216:219],v[196:199],v[96:99]
v_pk_mul_f32 v[28:29],v[28:29],v[0:1] op_sel_hi:[1,0]
v_pk_mul_f32 v[30:31],v[30:31],v[0:1] op_sel_hi:[1,0]
v_pk_mul_f32 v[32:33],v[32:33],v[0:1] op_sel_hi:[1,0]
v_pk_mul_f32 v[34:35],v[34:35],v[0:1] op_sel_hi:[1,0]
v_cvt_pk_bf16_f32 v28,v28,v29
v_cvt_pk_bf16_f32 v29,v30,v31
v_cvt_pk_bf16_f32 v30,v32,v33
v_cvt_pk_bf16_f32 v31,v34,v35
s_mov_b32 s91,0xc00
buffer_store_dwordx4 v[28:31],v167,s[72:75],s91 offen sc1
v_pk_mul_f32 v[76:77],v[76:77],v[2:3] op_sel_hi:[1,0]
v_pk_mul_f32 v[78:79],v[78:79],v[2:3] op_sel_hi:[1,0]
v_pk_mul_f32 v[80:81],v[80:81],v[2:3] op_sel_hi:[1,0]
v_pk_mul_f32 v[82:83],v[82:83],v[2:3] op_sel_hi:[1,0]
v_cvt_pk_bf16_f32 v76,v76,v77
v_cvt_pk_bf16_f32 v77,v78,v79
v_cvt_pk_bf16_f32 v78,v80,v81
v_cvt_pk_bf16_f32 v79,v82,v83
s_mov_b32 s91,0x30c00
buffer_store_dwordx4 v[76:79],v167,s[72:75],s91 offen sc1
v_pk_mul_f32 v[104:105],v[104:105],v[160:161] op_sel_hi:[1,0]
v_pk_mul_f32 v[106:107],v[106:107],v[160:161] op_sel_hi:[1,0]
v_cvt_pk_bf16_f32 v104,v104,v105
v_cvt_pk_bf16_f32 v105,v106,v107
v_pk_mul_f32 v[36:37],v[36:37],v[0:1] op_sel_hi:[1,0]
v_pk_mul_f32 v[38:39],v[38:39],v[0:1] op_sel_hi:[1,0]
v_pk_mul_f32 v[40:41],v[40:41],v[0:1] op_sel_hi:[1,0]
v_pk_mul_f32 v[42:43],v[42:43],v[0:1] op_sel_hi:[1,0]
v_cvt_pk_bf16_f32 v36,v36,v37
v_cvt_pk_bf16_f32 v37,v38,v39
v_cvt_pk_bf16_f32 v38,v40,v41
v_cvt_pk_bf16_f32 v39,v42,v43
s_mov_b32 s91,0x1000
buffer_store_dwordx4 v[36:39],v167,s[72:75],s91 offen sc1
v_pk_mul_f32 v[84:85],v[84:85],v[2:3] op_sel_hi:[1,0]
v_pk_mul_f32 v[86:87],v[86:87],v[2:3] op_sel_hi:[1,0]
v_pk_mul_f32 v[88:89],v[88:89],v[2:3] op_sel_hi:[1,0]
v_pk_mul_f32 v[90:91],v[90:91],v[2:3] op_sel_hi:[1,0]
v_cvt_pk_bf16_f32 v84,v84,v85
v_cvt_pk_bf16_f32 v85,v86,v87
v_cvt_pk_bf16_f32 v86,v88,v89
v_cvt_pk_bf16_f32 v87,v90,v91
s_mov_b32 s91,0x31000
buffer_store_dwordx4 v[84:87],v167,s[72:75],s91 offen sc1
v_pk_mul_f32 v[44:45],v[44:45],v[0:1] op_sel_hi:[1,0]
v_pk_mul_f32 v[46:47],v[46:47],v[0:1] op_sel_hi:[1,0]
v_pk_mul_f32 v[48:49],v[48:49],v[0:1] op_sel_hi:[1,0]
v_pk_mul_f32 v[50:51],v[50:51],v[0:1] op_sel_hi:[1,0]
v_cvt_pk_bf16_f32 v44,v44,v45
v_cvt_pk_bf16_f32 v45,v46,v47
v_cvt_pk_bf16_f32 v46,v48,v49
v_cvt_pk_bf16_f32 v47,v50,v51
s_mov_b32 s91,0x1400
buffer_store_dwordx4 v[44:47],v167,s[72:75],s91 offen sc1
v_pk_mul_f32 v[92:93],v[92:93],v[2:3] op_sel_hi:[1,0]
v_pk_mul_f32 v[94:95],v[94:95],v[2:3] op_sel_hi:[1,0]
v_pk_mul_f32 v[96:97],v[96:97],v[2:3] op_sel_hi:[1,0]
v_pk_mul_f32 v[98:99],v[98:99],v[2:3] op_sel_hi:[1,0]
v_cvt_pk_bf16_f32 v92,v92,v93
v_cvt_pk_bf16_f32 v93,v94,v95
v_cvt_pk_bf16_f32 v94,v96,v97
v_cvt_pk_bf16_f32 v95,v98,v99
s_mov_b32 s91,0x31400
buffer_store_dwordx4 v[92:95],v167,s[72:75],s91 offen sc1
v_pk_mul_f32 v[108:109],v[108:109],v[160:161] op_sel_hi:[1,0]
v_pk_mul_f32 v[110:111],v[110:111],v[160:161] op_sel_hi:[1,0]
v_cvt_pk_bf16_f32 v108,v108,v109
v_cvt_pk_bf16_f32 v109,v110,v111
s_waitcnt lgkmcnt(0)
s_barrier
ds_read_b64 v[102:103],v162
ds_read_b64 v[106:107],v162 offset:512
ds_read_b64 v[110:111],v162 offset:1024
s_mov_b32 s91,0x180000
s_waitcnt lgkmcnt(2)
buffer_store_dwordx4 v[100:103],v167,s[72:75],s91 offen sc1
s_mov_b32 s91,0x180800
s_waitcnt lgkmcnt(1)
buffer_store_dwordx4 v[104:107],v167,s[72:75],s91 offen sc1
s_mov_b32 s91,0x181000
s_waitcnt lgkmcnt(0)
buffer_store_dwordx4 v[108:111],v167,s[72:75],s91 offen sc1
v_mov_b32 v1,0x25800
ds_read_b128 v[168:171],v1
ds_read_b128 v[172:175],v1 offset:16
s_waitcnt lgkmcnt(1)
v_or_b32 v0,v168,v169
v_or3_b32 v0,v0,v170,v171
s_waitcnt lgkmcnt(0)
v_or3_b32 v0,v0,v172,v173
v_or3_b32 v0,v0,v174,v175
s_nop 0
v_readfirstlane_b32 s90,v0
s_nop 0
s_andn2_b32 s90,s90,s44
s_cbranch_scc0 W0_EPI
)A"
R"A(s_mov_b32 s44,1
v_mov_b32 v0,0xff800000
v_mov_b32 v1,0xff800000
v_mov_b32 v2,0xff800000
s_mov_b32 s98,0
v_mbcnt_lo_u32_b32 v168,-1,0
v_mbcnt_hi_u32_b32 v168,-1,v168
v_and_b32 v169,15,v168
v_lshrrev_b32 v170,4,v168
v_lshrrev_b32 v168,3,v169
s_movk_i32 s88,3072
v_mul_u32_u24 v244,s88,v168
v_and_b32 v168,7,v169
v_lshl_add_u32 v244,v168,7,v244
v_lshrrev_b32 v168,1,v169
v_xor_b32 v168,v168,v170
v_lshl_add_u32 v244,v168,4,v244
v_xor_b32 v245,64,v244
s_add_u32 s88,s51,s98
v_add_u32 v244,s88,v244
v_add_u32 v245,s88,v245
s_mov_b32 s94,0
s_mov_b32 s96,s48
W0_MAX:
s_waitcnt vmcnt(0) lgkmcnt(0)
s_barrier
s_mov_b32 m0,s96
s_nop 0
buffer_load_dwordx4 v251,s[64:67],s94 offen nt lds
s_add_u32 m0,s96,896
s_nop 0
buffer_load_dwordx4 v251,s[64:67],s94 offen offset:128 nt lds
s_add_u32 m0,s96,1792
s_nop 0
buffer_load_dwordx4 v251,s[64:67],s94 offen offset:256 nt lds
s_waitcnt vmcnt(0)
s_barrier
ds_read_b128 v[204:207],v244
ds_read_b128 v[208:211],v245
ds_read_b128 v[212:215],v244 offset:1024
ds_read_b128 v[216:219],v245 offset:1024
ds_read_b128 v[220:223],v244 offset:2048
ds_read_b128 v[224:227],v245 offset:2048
ds_read_b128 v[228:231],v250
s_waitcnt lgkmcnt(6)
v_mfma_f32_16x16x32_bf16 v[168:171],v[204:207],v[112:115],0
v_mfma_f32_16x16x32_bf16 v[172:175],v[204:207],v[136:139],0
s_waitcnt lgkmcnt(0)
v_mfma_f32_16x16x32_bf16 v[236:239],v[204:207],v[228:231],0
ds_read_b128 v[232:235],v250 offset:1024
v_mfma_f32_16x16x32_bf16 v[168:171],v[208:211],v[116:119],v[168:171]
v_mfma_f32_16x16x32_bf16 v[172:175],v[208:211],v[140:143],v[172:175]
s_waitcnt lgkmcnt(0)
v_mfma_f32_16x16x32_bf16 v[236:239],v[208:211],v[232:235],v[236:239]
ds_read_b128 v[228:231],v250 offset:2048
v_mfma_f32_16x16x32_bf16 v[168:171],v[212:215],v[120:123],v[168:171]
v_mfma_f32_16x16x32_bf16 v[172:175],v[212:215],v[144:147],v[172:175]
s_waitcnt lgkmcnt(0)
v_mfma_f32_16x16x32_bf16 v[236:239],v[212:215],v[228:231],v[236:239]
ds_read_b128 v[232:235],v250 offset:3072
v_mfma_f32_16x16x32_bf16 v[168:171],v[216:219],v[124:127],v[168:171]
v_mfma_f32_16x16x32_bf16 v[172:175],v[216:219],v[148:151],v[172:175]
s_waitcnt lgkmcnt(0)
v_mfma_f32_16x16x32_bf16 v[236:239],v[216:219],v[232:235],v[236:239]
ds_read_b128 v[228:231],v250 offset:4096
v_mfma_f32_16x16x32_bf16 v[168:171],v[220:223],v[128:131],v[168:171]
v_mfma_f32_16x16x32_bf16 v[172:175],v[220:223],v[152:155],v[172:175]
s_waitcnt lgkmcnt(0)
v_mfma_f32_16x16x32_bf16 v[236:239],v[220:223],v[228:231],v[236:239]
ds_read_b128 v[232:235],v250 offset:5120
v_mfma_f32_16x16x32_bf16 v[168:171],v[224:227],v[132:135],v[168:171]
v_mfma_f32_16x16x32_bf16 v[172:175],v[224:227],v[156:159],v[172:175]
s_waitcnt lgkmcnt(0)
v_mfma_f32_16x16x32_bf16 v[236:239],v[224:227],v[232:235],v[236:239]
s_nop 4
v_max3_f32 v0,v0,v168,v169
v_max3_f32 v0,v0,v170,v171
v_max3_f32 v1,v1,v172,v173
v_max3_f32 v1,v1,v174,v175
v_max3_f32 v2,v2,v236,v237
v_max3_f32 v2,v2,v238,v239
ds_read_b128 v[204:207],v244 offset:6144
ds_read_b128 v[208:211],v245 offset:6144
ds_read_b128 v[212:215],v244 offset:7168
ds_read_b128 v[216:219],v245 offset:7168
ds_read_b128 v[220:223],v244 offset:8192
ds_read_b128 v[224:227],v245 offset:8192
ds_read_b128 v[228:231],v250
s_waitcnt lgkmcnt(6)
v_mfma_f32_16x16x32_bf16 v[168:171],v[204:207],v[112:115],0
v_mfma_f32_16x16x32_bf16 v[172:175],v[204:207],v[136:139],0
s_waitcnt lgkmcnt(0)
v_mfma_f32_16x16x32_bf16 v[236:239],v[204:207],v[228:231],0
ds_read_b128 v[232:235],v250 offset:1024
v_mfma_f32_16x16x32_bf16 v[168:171],v[208:211],v[116:119],v[168:171]
v_mfma_f32_16x16x32_bf16 v[172:175],v[208:211],v[140:143],v[172:175]
s_waitcnt lgkmcnt(0)
v_mfma_f32_16x16x32_bf16 v[236:239],v[208:211],v[232:235],v[236:239]
ds_read_b128 v[228:231],v250 offset:2048
v_mfma_f32_16x16x32_bf16 v[168:171],v[212:215],v[120:123],v[168:171]
v_mfma_f32_16x16x32_bf16 v[172:175],v[212:215],v[144:147],v[172:175]
s_waitcnt lgkmcnt(0)
v_mfma_f32_16x16x32_bf16 v[236:239],v[212:215],v[228:231],v[236:239]
ds_read_b128 v[232:235],v250 offset:3072
v_mfma_f32_16x16x32_bf16 v[168:171],v[216:219],v[124:127],v[168:171]
v_mfma_f32_16x16x32_bf16 v[172:175],v[216:219],v[148:151],v[172:175]
s_waitcnt lgkmcnt(0)
v_mfma_f32_16x16x32_bf16 v[236:239],v[216:219],v[232:235],v[236:239]
ds_read_b128 v[228:231],v250 offset:4096
v_mfma_f32_16x16x32_bf16 v[168:171],v[220:223],v[128:131],v[168:171]
v_mfma_f32_16x16x32_bf16 v[172:175],v[220:223],v[152:155],v[172:175]
s_waitcnt lgkmcnt(0)
v_mfma_f32_16x16x32_bf16 v[236:239],v[220:223],v[228:231],v[236:239]
ds_read_b128 v[232:235],v250 offset:5120
v_mfma_f32_16x16x32_bf16 v[168:171],v[224:227],v[132:135],v[168:171]
v_mfma_f32_16x16x32_bf16 v[172:175],v[224:227],v[156:159],v[172:175]
s_waitcnt lgkmcnt(0)
v_mfma_f32_16x16x32_bf16 v[236:239],v[224:227],v[232:235],v[236:239]
s_nop 4
v_max3_f32 v0,v0,v168,v169
v_max3_f32 v0,v0,v170,v171
v_max3_f32 v1,v1,v172,v173
v_max3_f32 v1,v1,v174,v175
v_max3_f32 v2,v2,v236,v237
v_max3_f32 v2,v2,v238,v239
s_add_u32 s94,s94,12288
s_cmp_lt_u32 s94,196608
s_cbranch_scc1 W0_MAX
v_mov_b32 v3,v0
s_nop 1
v_permlane32_swap_b32 v0,v3
v_max_f32 v0,v0,v3
v_mov_b32 v3,v0
s_nop 1
v_permlane16_swap_b32 v0,v3
v_max_f32 v0,v0,v3
v_mov_b32 v3,v1
s_nop 1
v_permlane32_swap_b32 v1,v3
v_max_f32 v1,v1,v3
v_mov_b32 v3,v1
s_nop 1
v_permlane16_swap_b32 v1,v3
v_max_f32 v1,v1,v3
v_mov_b32 v3,v2
s_nop 1
v_permlane32_swap_b32 v2,v3
v_max_f32 v2,v2,v3
v_mov_b32 v3,v2
s_nop 1
v_permlane16_swap_b32 v2,v3
v_max_f32 v2,v2,v3
v_xor_b32 v160,0x80000000,v0
v_xor_b32 v164,0x80000000,v1
v_xor_b32 v161,0x80000000,v0
v_xor_b32 v165,0x80000000,v1
v_xor_b32 v162,0x80000000,v0
v_xor_b32 v166,0x80000000,v1
v_xor_b32 v163,0x80000000,v0
v_xor_b32 v167,0x80000000,v1
v_xor_b32 v240,0x80000000,v2
s_waitcnt vmcnt(0) lgkmcnt(0)
s_barrier
s_mul_i32 s98,s47,12288
s_add_u32 s96,s48,s98
s_mul_i32 s88,s35,12288
s_add_u32 s97,s88,s49
s_mov_b32 s94,0
s_mov_b32 s95,0
s_mov_b32 m0,s96
s_nop 0
buffer_load_dwordx4 v251,s[64:67],s94 offen nt lds
s_add_u32 m0,s96,896
s_nop 0
buffer_load_dwordx4 v251,s[64:67],s94 offen offset:128 nt lds
s_add_u32 m0,s96,1792
s_nop 0
buffer_load_dwordx4 v251,s[64:67],s94 offen offset:256 nt lds
s_mov_b32 m0,s97
s_nop 0
buffer_load_dwordx4 v252,s[68:71],s95 offen nt lds
s_add_u32 m0,s97,896
s_nop 0
buffer_load_dwordx4 v252,s[68:71],s95 offen offset:128 nt lds
s_add_u32 m0,s97,1792
s_nop 0
buffer_load_dwordx4 v252,s[68:71],s95 offen offset:256 nt lds
s_add_u32 s94,s94,12288
s_add_u32 s89,s48,36864
s_add_u32 s96,s96,12288
s_cmp_eq_u32 s96,s89
s_cselect_b32 s96,s48,s96
s_add_u32 s95,s95,12288
s_add_u32 s89,s49,36864
s_add_u32 s97,s97,12288
s_cmp_eq_u32 s97,s89
s_cselect_b32 s97,s49,s97
s_mov_b32 m0,s96
s_nop 0
buffer_load_dwordx4 v251,s[64:67],s94 offen nt lds
s_add_u32 m0,s96,896
s_nop 0
buffer_load_dwordx4 v251,s[64:67],s94 offen offset:128 nt lds
s_add_u32 m0,s96,1792
s_nop 0
buffer_load_dwordx4 v251,s[64:67],s94 offen offset:256 nt lds
s_add_u32 s94,s94,12288
s_add_u32 s89,s48,36864
s_add_u32 s96,s96,12288
s_cmp_eq_u32 s96,s89
s_cselect_b32 s96,s48,s96
v_mbcnt_lo_u32_b32 v0,-1,0
v_mbcnt_hi_u32_b32 v0,-1,v0
v_and_b32 v1,15,v0
v_lshrrev_b32 v2,4,v0
v_lshrrev_b32 v0,3,v1
s_movk_i32 s88,3072
v_mul_u32_u24 v244,s88,v0
v_and_b32 v0,7,v1
v_lshl_add_u32 v244,v0,7,v244
v_lshrrev_b32 v0,1,v1
v_xor_b32 v0,v0,v2
v_lshl_add_u32 v244,v0,4,v244
v_xor_b32 v245,64,v244
s_add_u32 s88,s51,s98
v_add_u32 v244,s88,v244
v_add_u32 v245,s88,v245
v_mbcnt_lo_u32_b32 v0,-1,0
v_mbcnt_hi_u32_b32 v0,-1,v0
v_and_b32 v1,15,v0
v_lshrrev_b32 v2,4,v0
v_lshlrev_b32 v250,4,v0
v_add_u32 v250,s50,v250
s_movk_i32 s88,3072
v_lshrrev_b32 v3,1,v2
v_mul_u32_u24 v246,s88,v3
v_and_b32 v2,1,v2
v_lshrrev_b32 v1,2,v1
v_lshl_add_u32 v3,v2,2,v1
v_lshl_add_u32 v246,v3,7,v246
v_lshrrev_b32 v1,1,v1
v_lshl_or_b32 v1,v2,1,v1
v_xor_b32 v1,0,v1
v_lshl_add_u32 v246,v1,5,v246
v_and_b32 v0,3,v0
v_lshl_add_u32 v246,v0,3,v246
v_xor_b32 v247,32,v246
v_xor_b32 v248,64,v246
v_xor_b32 v249,0x60,v246
s_mul_i32 s99,s35,12288
s_add_u32 s88,s99,s51
s_add_u32 s88,s88,73728
v_add_u32 v246,s88,v246
v_add_u32 v247,s88,v247
v_add_u32 v248,s88,v248
v_add_u32 v249,s88,v249
s_waitcnt vmcnt(6) lgkmcnt(0)
s_barrier
s_branch W0_ATT
W0_EPI:
s_nop 3
v_mbcnt_lo_u32_b32 v0,-1,0
v_mbcnt_hi_u32_b32 v0,-1,v0
v_lshlrev_b32 v0,4,v0
s_branch LNEXT
W1_START:
v_mbcnt_lo_u32_b32 v0,-1,0
v_mbcnt_hi_u32_b32 v0,-1,v0
v_and_b32 v1,15,v0
v_lshrrev_b32 v2,4,v0
v_lshrrev_b32 v0,3,v1
s_movk_i32 s88,3072
v_mul_u32_u24 v244,s88,v0
v_and_b32 v0,7,v1
v_lshl_add_u32 v244,v0,7,v244
v_lshrrev_b32 v0,1,v1
v_xor_b32 v0,v0,v2
v_lshl_add_u32 v244,v0,4,v244
v_xor_b32 v245,64,v244
s_add_u32 s88,s51,s98
v_add_u32 v244,s88,v244
v_add_u32 v245,s88,v245
v_mbcnt_lo_u32_b32 v0,-1,0
v_mbcnt_hi_u32_b32 v0,-1,v0
v_and_b32 v1,15,v0
v_lshrrev_b32 v2,4,v0
v_lshlrev_b32 v250,4,v0
v_add_u32 v250,s50,v250
s_movk_i32 s88,3072
v_lshrrev_b32 v3,1,v2
v_mul_u32_u24 v246,s88,v3
v_and_b32 v2,1,v2
v_lshrrev_b32 v1,2,v1
v_lshl_add_u32 v3,v2,2,v1
v_lshl_add_u32 v246,v3,7,v246
v_lshrrev_b32 v1,1,v1
v_lshl_or_b32 v1,v2,1,v1
v_xor_b32 v1,1,v1
v_lshl_add_u32 v246,v1,5,v246
v_and_b32 v0,3,v0
v_lshl_add_u32 v246,v0,3,v246
v_xor_b32 v247,32,v246
v_xor_b32 v248,64,v246
v_xor_b32 v249,0x60,v246
s_mul_i32 s99,s35,12288
s_add_u32 s88,s99,s51
s_add_u32 s88,s88,73728
v_add_u32 v246,s88,v246
v_add_u32 v247,s88,v247
v_add_u32 v248,s88,v248
v_add_u32 v249,s88,v249
s_xor_b32 s90,s85,2
s_mul_i32 s88,s90,184320
s_add_u32 s76,s36,s88
s_addc_u32 s77,s37,0
s_and_b32 s77,s77,0xffff
s_mov_b32 s78,0x2d000
s_mov_b32 s79,0x20000
s_mul_i32 s88,s86,384
s_sub_u32 s89,0x780000,s88
s_add_u32 s80,s42,s88
s_addc_u32 s81,s43,0
s_and_b32 s81,s81,0xffff
s_mov_b32 s82,s89
s_mov_b32 s83,0x20000
s_mov_b32 s44,0
s_waitcnt vmcnt(3) lgkmcnt(0)
s_barrier
s_cmp_eq_u32 s35,0
s_cselect_b32 s90,12288,0
s_add_u32 s90,s90,s51
s_add_u32 s90,s90,73728
s_add_u32 s91,s47,1
s_mul_i32 s91,s91,12288
s_add_u32 s91,s91,s51
s_cmp_eq_u32 s47,0
s_cselect_b32 s92,24576,0
s_add_u32 s92,s92,s51
v_mbcnt_lo_u32_b32 v0,-1,0
v_mbcnt_hi_u32_b32 v0,-1,v0
v_lshlrev_b32 v0,4,v0
v_add_u32 v1,s90,v0
ds_read_b128 v[112:115],v1 offset:12288
ds_read_b128 v[116:119],v1 offset:13312
ds_read_b128 v[120:123],v1 offset:14336
ds_read_b128 v[124:127],v1 offset:15360
ds_read_b128 v[128:131],v1 offset:16384
ds_read_b128 v[132:135],v1 offset:17408
v_add_u32 v2,s90,v0
ds_read_b128 v[136:139],v2 offset:18432
ds_read_b128 v[140:143],v2 offset:19456
ds_read_b128 v[144:147],v2 offset:20480
ds_read_b128 v[148:151],v2 offset:21504
ds_read_b128 v[152:155],v2 offset:22528
ds_read_b128 v[156:159],v2 offset:23552
ds_read_b128 v[4:7],v244
ds_read_b128 v[8:11],v245
ds_read_b128 v[12:15],v244 offset:1024
ds_read_b128 v[16:19],v245 offset:1024
ds_read_b128 v[20:23],v244 offset:2048
ds_read_b128 v[24:27],v245 offset:2048
ds_read_b128 v[28:31],v250
s_waitcnt lgkmcnt(6)
v_mfma_f32_16x16x32_bf16 v[36:39],v[4:7],v[112:115],0
v_mfma_f32_16x16x32_bf16 v[40:43],v[4:7],v[136:139],0
s_waitcnt lgkmcnt(0)
v_mfma_f32_16x16x32_bf16 v[44:47],v[4:7],v[28:31],0
ds_read_b128 v[32:35],v250 offset:1024
v_mfma_f32_16x16x32_bf16 v[36:39],v[8:11],v[116:119],v[36:39]
v_mfma_f32_16x16x32_bf16 v[40:43],v[8:11],v[140:143],v[40:43]
s_waitcnt lgkmcnt(0)
v_mfma_f32_16x16x32_bf16 v[44:47],v[8:11],v[32:35],v[44:47]
ds_read_b128 v[28:31],v250 offset:2048
v_mfma_f32_16x16x32_bf16 v[36:39],v[12:15],v[120:123],v[36:39]
v_mfma_f32_16x16x32_bf16 v[40:43],v[12:15],v[144:147],v[40:43]
s_waitcnt lgkmcnt(0)
v_mfma_f32_16x16x32_bf16 v[44:47],v[12:15],v[28:31],v[44:47]
ds_read_b128 v[32:35],v250 offset:3072
v_mfma_f32_16x16x32_bf16 v[36:39],v[16:19],v[124:127],v[36:39]
v_mfma_f32_16x16x32_bf16 v[40:43],v[16:19],v[148:151],v[40:43]
s_waitcnt lgkmcnt(0)
v_mfma_f32_16x16x32_bf16 v[44:47],v[16:19],v[32:35],v[44:47]
ds_read_b128 v[28:31],v250 offset:4096
v_mfma_f32_16x16x32_bf16 v[36:39],v[20:23],v[128:131],v[36:39]
v_mfma_f32_16x16x32_bf16 v[40:43],v[20:23],v[152:155],v[40:43]
s_waitcnt lgkmcnt(0)
v_mfma_f32_16x16x32_bf16 v[44:47],v[20:23],v[28:31],v[44:47]
ds_read_b128 v[32:35],v250 offset:5120
v_mfma_f32_16x16x32_bf16 v[36:39],v[24:27],v[132:135],v[36:39]
v_mfma_f32_16x16x32_bf16 v[40:43],v[24:27],v[156:159],v[40:43]
s_waitcnt lgkmcnt(0)
v_mfma_f32_16x16x32_bf16 v[44:47],v[24:27],v[32:35],v[44:47]
s_nop 4
v_max_f32 v0,v36,v37
v_max3_f32 v0,v0,v38,v39
v_max_f32 v1,v40,v41
v_max3_f32 v1,v1,v42,v43
v_max_f32 v2,v44,v45
v_max3_f32 v2,v2,v46,v47
v_mov_b32 v3,v0
)A"
R"A(s_nop 1
v_permlane32_swap_b32 v0,v3
v_max_f32 v0,v0,v3
v_mov_b32 v3,v0
s_nop 1
v_permlane16_swap_b32 v0,v3
v_max_f32 v0,v0,v3
v_mov_b32 v3,v1
s_nop 1
v_permlane32_swap_b32 v1,v3
v_max_f32 v1,v1,v3
v_mov_b32 v3,v1
s_nop 1
v_permlane16_swap_b32 v1,v3
v_max_f32 v1,v1,v3
v_mov_b32 v3,v2
s_nop 1
v_permlane32_swap_b32 v2,v3
v_max_f32 v2,v2,v3
v_mov_b32 v3,v2
s_nop 1
v_permlane16_swap_b32 v2,v3
v_max_f32 v2,v2,v3
v_xor_b32 v160,0x80000000,v0
v_xor_b32 v164,0x80000000,v1
v_xor_b32 v161,0x80000000,v0
v_xor_b32 v165,0x80000000,v1
v_xor_b32 v162,0x80000000,v0
v_xor_b32 v166,0x80000000,v1
v_xor_b32 v163,0x80000000,v0
v_xor_b32 v167,0x80000000,v1
v_xor_b32 v240,0x80000000,v2
s_waitcnt lgkmcnt(0)
s_barrier
s_mov_b32 m0,s96
s_nop 0
buffer_load_dwordx4 v251,s[64:67],s94 offen nt lds
s_add_u32 m0,s96,896
s_nop 0
buffer_load_dwordx4 v251,s[64:67],s94 offen offset:128 nt lds
s_add_u32 m0,s96,1792
s_nop 0
buffer_load_dwordx4 v251,s[64:67],s94 offen offset:256 nt lds
s_add_u32 s94,s94,12288
s_add_u32 s89,s48,36864
s_add_u32 s96,s96,12288
s_cmp_eq_u32 s96,s89
s_cselect_b32 s96,s48,s96
W1_ATT:
v_mov_b32 v36,0
v_mov_b32 v37,0
v_mov_b32 v38,0
v_mov_b32 v39,0
v_mov_b32 v40,0
v_mov_b32 v41,0
v_mov_b32 v42,0
v_mov_b32 v43,0
v_mov_b32 v44,0
v_mov_b32 v45,0
v_mov_b32 v46,0
v_mov_b32 v47,0
v_mov_b32 v48,0
v_mov_b32 v49,0
v_mov_b32 v50,0
v_mov_b32 v51,0
v_mov_b32 v84,0
v_mov_b32 v85,0
v_mov_b32 v86,0
v_mov_b32 v87,0
v_mov_b32 v88,0
v_mov_b32 v89,0
v_mov_b32 v90,0
v_mov_b32 v91,0
v_mov_b32 v92,0
v_mov_b32 v93,0
v_mov_b32 v94,0
v_mov_b32 v95,0
v_mov_b32 v96,0
v_mov_b32 v97,0
v_mov_b32 v98,0
v_mov_b32 v99,0
v_mov_b32 v108,0
v_mov_b32 v109,0
v_mov_b32 v110,0
v_mov_b32 v111,0
v_mov_b32 v241,0
v_mov_b32 v242,0
v_mov_b32 v243,0
ds_read_b128 v[220:223],v244
ds_read_b128 v[224:227],v245
ds_read_b128 v[204:207],v244 offset:1024
ds_read_b128 v[208:211],v245 offset:1024
ds_read_b128 v[212:215],v244 offset:2048
ds_read_b128 v[216:219],v245 offset:2048
s_waitcnt lgkmcnt(5)
v_mfma_f32_16x16x32_bf16 v[168:171],v[220:223],v[112:115],v[160:163]
s_mov_b32 m0,s96
v_mfma_f32_16x16x32_bf16 v[172:175],v[220:223],v[136:139],v[164:167]
ds_read_b128 v[220:223],v244 offset:6144
buffer_load_dwordx4 v251,s[64:67],s94 offen nt lds
s_waitcnt lgkmcnt(5)
v_mfma_f32_16x16x32_bf16 v[168:171],v[224:227],v[116:119],v[168:171]
v_mfma_f32_16x16x32_bf16 v[172:175],v[224:227],v[140:143],v[172:175]
ds_read_b128 v[224:227],v245 offset:6144
s_waitcnt lgkmcnt(5)
v_mfma_f32_16x16x32_bf16 v[168:171],v[204:207],v[120:123],v[168:171]
s_add_u32 m0,s96,896
v_mfma_f32_16x16x32_bf16 v[172:175],v[204:207],v[144:147],v[172:175]
ds_read_b128 v[204:207],v244 offset:7168
ds_read_b128 v[228:231],v250
ds_read_b128 v[232:235],v250 offset:1024
buffer_load_dwordx4 v251,s[64:67],s94 offen offset:128 nt lds
s_waitcnt lgkmcnt(7)
v_mfma_f32_16x16x32_bf16 v[168:171],v[208:211],v[124:127],v[168:171]
v_mfma_f32_16x16x32_bf16 v[172:175],v[208:211],v[148:151],v[172:175]
ds_read_b128 v[208:211],v245 offset:7168
s_waitcnt lgkmcnt(7)
v_mfma_f32_16x16x32_bf16 v[168:171],v[212:215],v[128:131],v[168:171]
v_mov_b32 v236,v240
v_mov_b32 v237,v240
v_mov_b32 v238,v240
v_mov_b32 v239,v240
s_add_u32 m0,s96,1792
v_mfma_f32_16x16x32_bf16 v[172:175],v[212:215],v[152:155],v[172:175]
ds_read_b128 v[212:215],v244 offset:8192
buffer_load_dwordx4 v251,s[64:67],s94 offen offset:256 nt lds
s_waitcnt lgkmcnt(7)
v_mfma_f32_16x16x32_bf16 v[168:171],v[216:219],v[132:135],v[168:171]
v_mfma_f32_16x16x32_bf16 v[172:175],v[216:219],v[156:159],v[172:175]
ds_read_b128 v[216:219],v245 offset:8192
s_waitcnt lgkmcnt(7)
v_mfma_f32_16x16x32_bf16 v[176:179],v[220:223],v[112:115],v[160:163]
s_mov_b32 m0,s97
v_mfma_f32_16x16x32_bf16 v[180:183],v[220:223],v[136:139],v[164:167]
buffer_load_dwordx4 v252,s[68:71],s95 offen nt lds
s_waitcnt lgkmcnt(4)
v_mfma_f32_16x16x32_bf16 v[236:239],v[220:223],v[228:231],v[236:239]
ds_read_b128 v[228:231],v250 offset:2048
v_mfma_f32_16x16x32_bf16 v[176:179],v[224:227],v[116:119],v[176:179]
v_exp_f32 v168,v168
v_mfma_f32_16x16x32_bf16 v[180:183],v[224:227],v[140:143],v[180:183]
v_exp_f32 v169,v169
s_add_u32 m0,s97,896
s_waitcnt lgkmcnt(4)
v_mfma_f32_16x16x32_bf16 v[236:239],v[224:227],v[232:235],v[236:239]
ds_read_b128 v[232:235],v250 offset:3072
v_add_f32 v241,v241,v168
v_exp_f32 v170,v170
buffer_load_dwordx4 v252,s[68:71],s95 offen offset:128 nt lds
s_mov_b32 s90,s98
s_add_u32 s98,s98,12288
s_cmp_eq_u32 s98,36864
s_cmov_b32 s98,0
s_sub_u32 s46,s98,s90
v_add_u32 v244,s46,v244
v_add_u32 v245,s46,v245
v_mfma_f32_16x16x32_bf16 v[176:179],v[204:207],v[120:123],v[176:179]
v_add_f32 v241,v241,v169
v_exp_f32 v171,v171
v_cvt_pk_bf16_f32 v184,v168,v169
v_mfma_f32_16x16x32_bf16 v[180:183],v[204:207],v[144:147],v[180:183]
v_add_f32 v241,v241,v170
v_exp_f32 v172,v172
s_waitcnt lgkmcnt(1)
v_mfma_f32_16x16x32_bf16 v[236:239],v[204:207],v[228:231],v[236:239]
ds_read_b128 v[228:231],v250 offset:4096
v_add_f32 v241,v241,v171
v_exp_f32 v173,v173
v_cvt_pk_bf16_f32 v185,v170,v171
s_add_u32 m0,s97,1792
v_mfma_f32_16x16x32_bf16 v[176:179],v[208:211],v[124:127],v[176:179]
v_add_f32 v242,v242,v172
v_exp_f32 v174,v174
buffer_load_dwordx4 v252,s[68:71],s95 offen offset:256 nt lds
v_mfma_f32_16x16x32_bf16 v[180:183],v[208:211],v[148:151],v[180:183]
v_add_f32 v242,v242,v173
v_exp_f32 v175,v175
v_cvt_pk_bf16_f32 v188,v172,v173
s_waitcnt lgkmcnt(1)
v_mfma_f32_16x16x32_bf16 v[236:239],v[208:211],v[232:235],v[236:239]
ds_read_b128 v[232:235],v250 offset:5120
v_add_f32 v242,v242,v174
v_mfma_f32_16x16x32_bf16 v[176:179],v[212:215],v[128:131],v[176:179]
v_add_f32 v242,v242,v175
v_cvt_pk_bf16_f32 v189,v174,v175
v_mfma_f32_16x16x32_bf16 v[180:183],v[212:215],v[152:155],v[180:183]
s_waitcnt lgkmcnt(1)
v_mfma_f32_16x16x32_bf16 v[236:239],v[212:215],v[228:231],v[236:239]
s_add_u32 s94,s94,12288
s_add_u32 s89,s48,36864
s_add_u32 s96,s96,12288
s_cmp_eq_u32 s96,s89
s_cselect_b32 s96,s48,s96
v_mfma_f32_16x16x32_bf16 v[176:179],v[216:219],v[132:135],v[176:179]
v_mfma_f32_16x16x32_bf16 v[180:183],v[216:219],v[156:159],v[180:183]
s_add_u32 s95,s95,12288
s_add_u32 s89,s49,36864
s_add_u32 s97,s97,12288
s_cmp_eq_u32 s97,s89
s_cselect_b32 s97,s49,s97
s_waitcnt lgkmcnt(0)
v_mfma_f32_16x16x32_bf16 v[236:239],v[216:219],v[232:235],v[236:239]
s_nop 7
v_exp_f32 v236,v236
v_exp_f32 v237,v237
v_exp_f32 v238,v238
v_exp_f32 v239,v239
v_cvt_pk_bf16_f32 v0,v236,v237
v_cvt_pk_bf16_f32 v1,v238,v239
ds_write_b64 v250,v[0:1] offset:6152
s_waitcnt vmcnt(6) lgkmcnt(0)
s_barrier
ds_read_b128 v[220:223],v244
ds_read_b128 v[224:227],v245
ds_read_b128 v[204:207],v244 offset:1024
ds_read_b128 v[208:211],v245 offset:1024
ds_read_b128 v[212:215],v244 offset:2048
ds_read_b128 v[216:219],v245 offset:2048
ds_read_b128 v[200:203],v250 offset:6144
v_exp_f32 v176,v176
s_waitcnt lgkmcnt(6)
v_mfma_f32_16x16x32_bf16 v[168:171],v[220:223],v[112:115],v[160:163]
v_exp_f32 v177,v177
s_mov_b32 m0,s96
v_mfma_f32_16x16x32_bf16 v[172:175],v[220:223],v[136:139],v[164:167]
ds_read_b128 v[220:223],v244 offset:6144
v_add_f32 v241,v241,v176
v_exp_f32 v178,v178
buffer_load_dwordx4 v251,s[64:67],s94 offen nt lds
s_waitcnt lgkmcnt(6)
v_mfma_f32_16x16x32_bf16 v[168:171],v[224:227],v[116:119],v[168:171]
v_add_f32 v241,v241,v177
v_exp_f32 v179,v179
v_cvt_pk_bf16_f32 v186,v176,v177
v_mfma_f32_16x16x32_bf16 v[172:175],v[224:227],v[140:143],v[172:175]
ds_read_b128 v[224:227],v245 offset:6144
v_add_f32 v241,v241,v178
v_exp_f32 v180,v180
s_waitcnt lgkmcnt(6)
v_mfma_f32_16x16x32_bf16 v[168:171],v[204:207],v[120:123],v[168:171]
v_add_f32 v241,v241,v179
v_exp_f32 v181,v181
v_cvt_pk_bf16_f32 v187,v178,v179
s_add_u32 m0,s96,896
v_mfma_f32_16x16x32_bf16 v[172:175],v[204:207],v[144:147],v[172:175]
ds_read_b128 v[204:207],v244 offset:7168
v_add_f32 v242,v242,v180
v_exp_f32 v182,v182
buffer_load_dwordx4 v251,s[64:67],s94 offen offset:128 nt lds
s_waitcnt lgkmcnt(6)
v_mfma_f32_16x16x32_bf16 v[168:171],v[208:211],v[124:127],v[168:171]
s_waitcnt lgkmcnt(3)
v_dot2c_f32_bf16 v243,s55,v200
v_add_f32 v242,v242,v181
v_exp_f32 v183,v183
v_cvt_pk_bf16_f32 v190,v180,v181
v_mfma_f32_16x16x32_bf16 v[172:175],v[208:211],v[148:151],v[172:175]
ds_read_b128 v[208:211],v245 offset:7168
v_add_f32 v242,v242,v182
v_mfma_f32_16x16x32_bf16 v[168:171],v[212:215],v[128:131],v[168:171]
v_dot2c_f32_bf16 v243,s55,v201
v_add_f32 v242,v242,v183
v_cvt_pk_bf16_f32 v191,v182,v183
s_add_u32 m0,s96,1792
v_mfma_f32_16x16x32_bf16 v[172:175],v[212:215],v[152:155],v[172:175]
ds_read_b128 v[212:215],v244 offset:8192
buffer_load_dwordx4 v251,s[64:67],s94 offen offset:256 nt lds
v_mfma_f32_16x16x32_bf16 v[168:171],v[216:219],v[132:135],v[168:171]
v_dot2c_f32_bf16 v243,s55,v202
v_mfma_f32_16x16x32_bf16 v[172:175],v[216:219],v[156:159],v[172:175]
ds_read_b128 v[216:219],v245 offset:8192
s_waitcnt lgkmcnt(5)
v_mfma_f32_16x16x32_bf16 v[176:179],v[220:223],v[112:115],v[160:163]
v_dot2c_f32_bf16 v243,s55,v203
s_mov_b32 m0,s97
v_mfma_f32_16x16x32_bf16 v[180:183],v[220:223],v[136:139],v[164:167]
ds_read_b64_tr_b16 v[220:221],v246
ds_read_b64_tr_b16 v[222:223],v246 offset:6144
buffer_load_dwordx4 v252,s[68:71],s95 offen nt lds
s_waitcnt lgkmcnt(6)
v_mfma_f32_16x16x32_bf16 v[176:179],v[224:227],v[116:119],v[176:179]
v_mfma_f32_16x16x32_bf16 v[180:183],v[224:227],v[140:143],v[180:183]
ds_read_b64_tr_b16 v[224:225],v247
ds_read_b64_tr_b16 v[226:227],v247 offset:6144
v_exp_f32 v168,v168
s_mov_b32 s90,s98
s_add_u32 s98,s98,12288
s_cmp_eq_u32 s98,36864
s_cmov_b32 s98,0
s_sub_u32 s46,s98,s90
v_add_u32 v244,s46,v244
v_add_u32 v245,s46,v245
s_waitcnt lgkmcnt(7)
v_mfma_f32_16x16x32_bf16 v[176:179],v[204:207],v[120:123],v[176:179]
v_exp_f32 v169,v169
s_add_u32 m0,s97,896
v_mfma_f32_16x16x32_bf16 v[180:183],v[204:207],v[144:147],v[180:183]
ds_read_b64_tr_b16 v[204:205],v248
ds_read_b64_tr_b16 v[206:207],v248 offset:6144
v_add_f32 v241,v241,v168
v_exp_f32 v170,v170
buffer_load_dwordx4 v252,s[68:71],s95 offen offset:128 nt lds
s_waitcnt lgkmcnt(8)
v_mfma_f32_16x16x32_bf16 v[176:179],v[208:211],v[124:127],v[176:179]
v_add_f32 v241,v241,v169
v_exp_f32 v171,v171
v_cvt_pk_bf16_f32 v192,v168,v169
v_mfma_f32_16x16x32_bf16 v[180:183],v[208:211],v[148:151],v[180:183]
ds_read_b64_tr_b16 v[208:209],v249
ds_read_b64_tr_b16 v[210:211],v249 offset:6144
v_add_f32 v241,v241,v170
v_exp_f32 v172,v172
s_waitcnt lgkmcnt(9)
v_mfma_f32_16x16x32_bf16 v[176:179],v[212:215],v[128:131],v[176:179]
v_add_f32 v241,v241,v171
v_exp_f32 v173,v173
v_cvt_pk_bf16_f32 v193,v170,v171
s_add_u32 m0,s97,1792
v_mfma_f32_16x16x32_bf16 v[180:183],v[212:215],v[152:155],v[180:183]
ds_read_b64_tr_b16 v[212:213],v246 offset:1024
ds_read_b64_tr_b16 v[214:215],v246 offset:7168
v_add_f32 v242,v242,v172
v_exp_f32 v174,v174
buffer_load_dwordx4 v252,s[68:71],s95 offen offset:256 nt lds
s_waitcnt lgkmcnt(10)
v_mfma_f32_16x16x32_bf16 v[176:179],v[216:219],v[132:135],v[176:179]
v_add_f32 v242,v242,v173
v_exp_f32 v175,v175
v_cvt_pk_bf16_f32 v196,v172,v173
v_mfma_f32_16x16x32_bf16 v[180:183],v[216:219],v[156:159],v[180:183]
ds_read_b64_tr_b16 v[216:217],v247 offset:1024
ds_read_b64_tr_b16 v[218:219],v247 offset:7168
v_add_f32 v242,v242,v174
s_waitcnt lgkmcnt(10)
v_mfma_f32_16x16x32_bf16 v[4:7],v[220:223],v[184:187],0
v_add_f32 v242,v242,v175
v_cvt_pk_bf16_f32 v197,v174,v175
v_mfma_f32_16x16x32_bf16 v[52:55],v[220:223],v[188:191],0
v_mfma_f32_16x16x32_bf16 v[100:103],v[220:223],v[200:203],0
ds_read_b64_tr_b16 v[220:221],v248 offset:1024
ds_read_b64_tr_b16 v[222:223],v248 offset:7168
s_add_u32 s94,s94,12288
s_add_u32 s89,s48,36864
s_add_u32 s96,s96,12288
s_cmp_eq_u32 s96,s89
s_cselect_b32 s96,s48,s96
s_waitcnt lgkmcnt(10)
v_mfma_f32_16x16x32_bf16 v[8:11],v[224:227],v[184:187],0
v_mfma_f32_16x16x32_bf16 v[56:59],v[224:227],v[188:191],0
ds_read_b64_tr_b16 v[224:225],v249 offset:1024
ds_read_b64_tr_b16 v[226:227],v249 offset:7168
s_add_u32 s95,s95,12288
s_add_u32 s89,s49,36864
s_add_u32 s97,s97,12288
s_cmp_eq_u32 s97,s89
s_cselect_b32 s97,s49,s97
s_waitcnt lgkmcnt(10)
v_mfma_f32_16x16x32_bf16 v[12:15],v[204:207],v[184:187],0
v_mfma_f32_16x16x32_bf16 v[60:63],v[204:207],v[188:191],0
ds_read_b64_tr_b16 v[204:205],v246 offset:2048
ds_read_b64_tr_b16 v[206:207],v246 offset:8192
s_waitcnt lgkmcnt(10)
v_mfma_f32_16x16x32_bf16 v[16:19],v[208:211],v[184:187],0
v_mfma_f32_16x16x32_bf16 v[64:67],v[208:211],v[188:191],0
ds_read_b64_tr_b16 v[208:209],v247 offset:2048
ds_read_b64_tr_b16 v[210:211],v247 offset:8192
s_waitcnt lgkmcnt(10)
v_mfma_f32_16x16x32_bf16 v[20:23],v[212:215],v[184:187],0
v_mfma_f32_16x16x32_bf16 v[68:71],v[212:215],v[188:191],0
v_mfma_f32_16x16x32_bf16 v[104:107],v[212:215],v[200:203],0
ds_read_b64_tr_b16 v[212:213],v248 offset:2048
ds_read_b64_tr_b16 v[214:215],v248 offset:8192
s_waitcnt lgkmcnt(10)
v_mfma_f32_16x16x32_bf16 v[24:27],v[216:219],v[184:187],0
v_mfma_f32_16x16x32_bf16 v[72:75],v[216:219],v[188:191],0
ds_read_b64_tr_b16 v[216:217],v249 offset:2048
ds_read_b64_tr_b16 v[218:219],v249 offset:8192
s_waitcnt lgkmcnt(10)
v_mfma_f32_16x16x32_bf16 v[28:31],v[220:223],v[184:187],0
s_mov_b32 s90,s99
s_add_u32 s99,s99,12288
s_cmp_eq_u32 s99,36864
s_cmov_b32 s99,0
s_sub_u32 s46,s99,s90
v_add_u32 v246,s46,v246
v_add_u32 v247,s46,v247
v_add_u32 v248,s46,v248
v_add_u32 v249,s46,v249
v_mfma_f32_16x16x32_bf16 v[76:79],v[220:223],v[188:191],0
s_waitcnt lgkmcnt(8)
v_mfma_f32_16x16x32_bf16 v[32:35],v[224:227],v[184:187],0
v_mfma_f32_16x16x32_bf16 v[80:83],v[224:227],v[188:191],0
s_mov_b32 s45,6
W1_LOOP:
s_waitcnt vmcnt(6) lgkmcnt(0)
s_barrier
ds_read_b128 v[220:223],v244
ds_read_b128 v[224:227],v245
v_mfma_f32_16x16x32_bf16 v[36:39],v[204:207],v[184:187],v[36:39]
v_exp_f32 v176,v176
)A"
R"A(s_mov_b32 m0,s96
v_mfma_f32_16x16x32_bf16 v[84:87],v[204:207],v[188:191],v[84:87]
v_exp_f32 v177,v177
buffer_load_dwordx4 v251,s[64:67],s94 offen nt lds
v_mfma_f32_16x16x32_bf16 v[108:111],v[204:207],v[200:203],v[108:111]
ds_read_b128 v[204:207],v244 offset:1024
v_add_f32 v241,v241,v176
v_exp_f32 v178,v178
v_mfma_f32_16x16x32_bf16 v[40:43],v[208:211],v[184:187],v[40:43]
v_add_f32 v241,v241,v177
v_exp_f32 v179,v179
v_cvt_pk_bf16_f32 v194,v176,v177
v_mfma_f32_16x16x32_bf16 v[88:91],v[208:211],v[188:191],v[88:91]
ds_read_b128 v[208:211],v245 offset:1024
ds_read_b128 v[200:203],v250 offset:7168
v_add_f32 v241,v241,v178
v_exp_f32 v180,v180
s_add_u32 m0,s96,896
v_mfma_f32_16x16x32_bf16 v[44:47],v[212:215],v[184:187],v[44:47]
v_add_f32 v241,v241,v179
v_exp_f32 v181,v181
v_cvt_pk_bf16_f32 v195,v178,v179
buffer_load_dwordx4 v251,s[64:67],s94 offen offset:128 nt lds
v_mfma_f32_16x16x32_bf16 v[92:95],v[212:215],v[188:191],v[92:95]
ds_read_b128 v[212:215],v244 offset:2048
v_add_f32 v242,v242,v180
v_exp_f32 v182,v182
v_mfma_f32_16x16x32_bf16 v[48:51],v[216:219],v[184:187],v[48:51]
v_add_f32 v242,v242,v181
v_exp_f32 v183,v183
v_cvt_pk_bf16_f32 v198,v180,v181
v_mfma_f32_16x16x32_bf16 v[96:99],v[216:219],v[188:191],v[96:99]
ds_read_b128 v[216:219],v245 offset:2048
v_add_f32 v242,v242,v182
s_add_u32 m0,s96,1792
s_waitcnt lgkmcnt(6)
v_mfma_f32_16x16x32_bf16 v[168:171],v[220:223],v[112:115],v[160:163]
v_add_f32 v242,v242,v183
v_cvt_pk_bf16_f32 v199,v182,v183
buffer_load_dwordx4 v251,s[64:67],s94 offen offset:256 nt lds
v_mfma_f32_16x16x32_bf16 v[172:175],v[220:223],v[136:139],v[164:167]
ds_read_b128 v[220:223],v244 offset:6144
s_waitcnt lgkmcnt(6)
v_mfma_f32_16x16x32_bf16 v[168:171],v[224:227],v[116:119],v[168:171]
v_mfma_f32_16x16x32_bf16 v[172:175],v[224:227],v[140:143],v[172:175]
ds_read_b128 v[224:227],v245 offset:6144
s_mov_b32 m0,s97
s_waitcnt lgkmcnt(6)
v_mfma_f32_16x16x32_bf16 v[168:171],v[204:207],v[120:123],v[168:171]
buffer_load_dwordx4 v252,s[68:71],s95 offen nt lds
v_mfma_f32_16x16x32_bf16 v[172:175],v[204:207],v[144:147],v[172:175]
ds_read_b128 v[204:207],v244 offset:7168
ds_read_b128 v[228:231],v250
ds_read_b128 v[232:235],v250 offset:1024
s_waitcnt lgkmcnt(8)
v_mfma_f32_16x16x32_bf16 v[168:171],v[208:211],v[124:127],v[168:171]
s_waitcnt lgkmcnt(7)
v_dot2c_f32_bf16 v243,s55,v200
v_mfma_f32_16x16x32_bf16 v[172:175],v[208:211],v[148:151],v[172:175]
ds_read_b128 v[208:211],v245 offset:7168
s_add_u32 m0,s97,896
s_waitcnt lgkmcnt(7)
v_mfma_f32_16x16x32_bf16 v[168:171],v[212:215],v[128:131],v[168:171]
v_dot2c_f32_bf16 v243,s55,v201
v_mov_b32 v236,v240
v_mov_b32 v237,v240
v_mov_b32 v238,v240
v_mov_b32 v239,v240
buffer_load_dwordx4 v252,s[68:71],s95 offen offset:128 nt lds
v_mfma_f32_16x16x32_bf16 v[172:175],v[212:215],v[152:155],v[172:175]
ds_read_b128 v[212:215],v244 offset:8192
s_waitcnt lgkmcnt(7)
v_mfma_f32_16x16x32_bf16 v[168:171],v[216:219],v[132:135],v[168:171]
v_dot2c_f32_bf16 v243,s55,v202
v_mfma_f32_16x16x32_bf16 v[172:175],v[216:219],v[156:159],v[172:175]
ds_read_b128 v[216:219],v245 offset:8192
s_add_u32 m0,s97,1792
s_waitcnt lgkmcnt(7)
v_mfma_f32_16x16x32_bf16 v[176:179],v[220:223],v[112:115],v[160:163]
v_dot2c_f32_bf16 v243,s55,v203
buffer_load_dwordx4 v252,s[68:71],s95 offen offset:256 nt lds
v_mfma_f32_16x16x32_bf16 v[180:183],v[220:223],v[136:139],v[164:167]
s_waitcnt lgkmcnt(4)
v_mfma_f32_16x16x32_bf16 v[236:239],v[220:223],v[228:231],v[236:239]
ds_read_b64_tr_b16 v[220:221],v246
ds_read_b64_tr_b16 v[222:223],v246 offset:6144
ds_read_b128 v[228:231],v250 offset:2048
v_mfma_f32_16x16x32_bf16 v[176:179],v[224:227],v[116:119],v[176:179]
v_exp_f32 v168,v168
v_mfma_f32_16x16x32_bf16 v[180:183],v[224:227],v[140:143],v[180:183]
v_exp_f32 v169,v169
s_waitcnt lgkmcnt(6)
v_mfma_f32_16x16x32_bf16 v[236:239],v[224:227],v[232:235],v[236:239]
ds_read_b64_tr_b16 v[224:225],v247
ds_read_b64_tr_b16 v[226:227],v247 offset:6144
ds_read_b128 v[232:235],v250 offset:3072
v_add_f32 v241,v241,v168
v_exp_f32 v170,v170
s_add_u32 s94,s94,12288
s_add_u32 s89,s48,36864
s_add_u32 s96,s96,12288
s_cmp_eq_u32 s96,s89
s_cselect_b32 s96,s48,s96
s_mov_b32 s90,s98
s_add_u32 s98,s98,12288
s_cmp_eq_u32 s98,36864
s_cmov_b32 s98,0
s_sub_u32 s46,s98,s90
v_add_u32 v244,s46,v244
v_add_u32 v245,s46,v245
v_mfma_f32_16x16x32_bf16 v[176:179],v[204:207],v[120:123],v[176:179]
v_add_f32 v241,v241,v169
v_exp_f32 v171,v171
v_cvt_pk_bf16_f32 v184,v168,v169
v_mfma_f32_16x16x32_bf16 v[180:183],v[204:207],v[144:147],v[180:183]
v_add_f32 v241,v241,v170
v_exp_f32 v172,v172
s_add_u32 s95,s95,12288
s_add_u32 s89,s49,36864
s_add_u32 s97,s97,12288
s_cmp_eq_u32 s97,s89
s_cselect_b32 s97,s49,s97
s_waitcnt lgkmcnt(3)
v_mfma_f32_16x16x32_bf16 v[236:239],v[204:207],v[228:231],v[236:239]
ds_read_b64_tr_b16 v[204:205],v248
ds_read_b64_tr_b16 v[206:207],v248 offset:6144
ds_read_b128 v[228:231],v250 offset:4096
v_add_f32 v241,v241,v171
v_exp_f32 v173,v173
v_cvt_pk_bf16_f32 v185,v170,v171
v_mfma_f32_16x16x32_bf16 v[176:179],v[208:211],v[124:127],v[176:179]
v_add_f32 v242,v242,v172
v_exp_f32 v174,v174
s_sub_u32 s45,s45,1
v_mfma_f32_16x16x32_bf16 v[180:183],v[208:211],v[148:151],v[180:183]
v_add_f32 v242,v242,v173
v_exp_f32 v175,v175
v_cvt_pk_bf16_f32 v188,v172,v173
s_waitcnt lgkmcnt(3)
v_mfma_f32_16x16x32_bf16 v[236:239],v[208:211],v[232:235],v[236:239]
ds_read_b64_tr_b16 v[208:209],v249
ds_read_b64_tr_b16 v[210:211],v249 offset:6144
ds_read_b128 v[232:235],v250 offset:5120
v_add_f32 v242,v242,v174
v_mfma_f32_16x16x32_bf16 v[176:179],v[212:215],v[128:131],v[176:179]
v_add_f32 v242,v242,v175
v_cvt_pk_bf16_f32 v189,v174,v175
v_mfma_f32_16x16x32_bf16 v[180:183],v[212:215],v[152:155],v[180:183]
s_waitcnt lgkmcnt(3)
v_mfma_f32_16x16x32_bf16 v[236:239],v[212:215],v[228:231],v[236:239]
ds_read_b64_tr_b16 v[212:213],v246 offset:1024
ds_read_b64_tr_b16 v[214:215],v246 offset:7168
v_mfma_f32_16x16x32_bf16 v[176:179],v[216:219],v[132:135],v[176:179]
v_mfma_f32_16x16x32_bf16 v[180:183],v[216:219],v[156:159],v[180:183]
s_waitcnt lgkmcnt(2)
v_mfma_f32_16x16x32_bf16 v[236:239],v[216:219],v[232:235],v[236:239]
ds_read_b64_tr_b16 v[216:217],v247 offset:1024
ds_read_b64_tr_b16 v[218:219],v247 offset:7168
v_mfma_f32_16x16x32_bf16 v[4:7],v[220:223],v[192:195],v[4:7]
v_mfma_f32_16x16x32_bf16 v[52:55],v[220:223],v[196:199],v[52:55]
v_mfma_f32_16x16x32_bf16 v[100:103],v[220:223],v[200:203],v[100:103]
ds_read_b64_tr_b16 v[220:221],v248 offset:1024
ds_read_b64_tr_b16 v[222:223],v248 offset:7168
v_mfma_f32_16x16x32_bf16 v[8:11],v[224:227],v[192:195],v[8:11]
v_exp_f32 v236,v236
v_mfma_f32_16x16x32_bf16 v[56:59],v[224:227],v[196:199],v[56:59]
ds_read_b64_tr_b16 v[224:225],v249 offset:1024
ds_read_b64_tr_b16 v[226:227],v249 offset:7168
v_exp_f32 v237,v237
v_mfma_f32_16x16x32_bf16 v[12:15],v[204:207],v[192:195],v[12:15]
v_exp_f32 v238,v238
v_mfma_f32_16x16x32_bf16 v[60:63],v[204:207],v[196:199],v[60:63]
ds_read_b64_tr_b16 v[204:205],v246 offset:2048
ds_read_b64_tr_b16 v[206:207],v246 offset:8192
v_exp_f32 v239,v239
v_mfma_f32_16x16x32_bf16 v[16:19],v[208:211],v[192:195],v[16:19]
v_mfma_f32_16x16x32_bf16 v[64:67],v[208:211],v[196:199],v[64:67]
ds_read_b64_tr_b16 v[208:209],v247 offset:2048
ds_read_b64_tr_b16 v[210:211],v247 offset:8192
v_cvt_pk_bf16_f32 v0,v236,v237
v_cvt_pk_bf16_f32 v1,v238,v239
s_waitcnt lgkmcnt(10)
v_mfma_f32_16x16x32_bf16 v[20:23],v[212:215],v[192:195],v[20:23]
ds_write_b64 v250,v[0:1] offset:6152
v_mfma_f32_16x16x32_bf16 v[68:71],v[212:215],v[196:199],v[68:71]
v_mfma_f32_16x16x32_bf16 v[104:107],v[212:215],v[200:203],v[104:107]
ds_read_b64_tr_b16 v[212:213],v248 offset:2048
ds_read_b64_tr_b16 v[214:215],v248 offset:8192
s_waitcnt lgkmcnt(11)
v_mfma_f32_16x16x32_bf16 v[24:27],v[216:219],v[192:195],v[24:27]
v_mfma_f32_16x16x32_bf16 v[72:75],v[216:219],v[196:199],v[72:75]
ds_read_b64_tr_b16 v[216:217],v249 offset:2048
ds_read_b64_tr_b16 v[218:219],v249 offset:8192
s_waitcnt lgkmcnt(11)
v_mfma_f32_16x16x32_bf16 v[28:31],v[220:223],v[192:195],v[28:31]
s_mov_b32 s90,s99
s_add_u32 s99,s99,12288
s_cmp_eq_u32 s99,36864
s_cmov_b32 s99,0
s_sub_u32 s46,s99,s90
v_add_u32 v246,s46,v246
v_add_u32 v247,s46,v247
v_add_u32 v248,s46,v248
v_add_u32 v249,s46,v249
v_mfma_f32_16x16x32_bf16 v[76:79],v[220:223],v[196:199],v[76:79]
s_waitcnt lgkmcnt(9)
v_mfma_f32_16x16x32_bf16 v[32:35],v[224:227],v[192:195],v[32:35]
v_mfma_f32_16x16x32_bf16 v[80:83],v[224:227],v[196:199],v[80:83]
s_waitcnt vmcnt(6) lgkmcnt(0)
s_barrier
ds_read_b128 v[220:223],v244
ds_read_b128 v[224:227],v245
v_mfma_f32_16x16x32_bf16 v[36:39],v[204:207],v[192:195],v[36:39]
v_exp_f32 v176,v176
s_mov_b32 m0,s96
v_mfma_f32_16x16x32_bf16 v[84:87],v[204:207],v[196:199],v[84:87]
v_exp_f32 v177,v177
buffer_load_dwordx4 v251,s[64:67],s94 offen nt lds
v_mfma_f32_16x16x32_bf16 v[108:111],v[204:207],v[200:203],v[108:111]
ds_read_b128 v[204:207],v244 offset:1024
v_add_f32 v241,v241,v176
v_exp_f32 v178,v178
v_mfma_f32_16x16x32_bf16 v[40:43],v[208:211],v[192:195],v[40:43]
v_add_f32 v241,v241,v177
v_exp_f32 v179,v179
v_cvt_pk_bf16_f32 v186,v176,v177
v_mfma_f32_16x16x32_bf16 v[88:91],v[208:211],v[196:199],v[88:91]
ds_read_b128 v[208:211],v245 offset:1024
ds_read_b128 v[200:203],v250 offset:6144
v_add_f32 v241,v241,v178
v_exp_f32 v180,v180
s_add_u32 m0,s96,896
v_mfma_f32_16x16x32_bf16 v[44:47],v[212:215],v[192:195],v[44:47]
v_add_f32 v241,v241,v179
v_exp_f32 v181,v181
v_cvt_pk_bf16_f32 v187,v178,v179
buffer_load_dwordx4 v251,s[64:67],s94 offen offset:128 nt lds
v_mfma_f32_16x16x32_bf16 v[92:95],v[212:215],v[196:199],v[92:95]
ds_read_b128 v[212:215],v244 offset:2048
v_add_f32 v242,v242,v180
v_exp_f32 v182,v182
v_mfma_f32_16x16x32_bf16 v[48:51],v[216:219],v[192:195],v[48:51]
v_add_f32 v242,v242,v181
v_exp_f32 v183,v183
v_cvt_pk_bf16_f32 v190,v180,v181
v_mfma_f32_16x16x32_bf16 v[96:99],v[216:219],v[196:199],v[96:99]
ds_read_b128 v[216:219],v245 offset:2048
v_add_f32 v242,v242,v182
s_add_u32 m0,s96,1792
s_waitcnt lgkmcnt(6)
v_mfma_f32_16x16x32_bf16 v[168:171],v[220:223],v[112:115],v[160:163]
v_add_f32 v242,v242,v183
v_cvt_pk_bf16_f32 v191,v182,v183
buffer_load_dwordx4 v251,s[64:67],s94 offen offset:256 nt lds
v_mfma_f32_16x16x32_bf16 v[172:175],v[220:223],v[136:139],v[164:167]
ds_read_b128 v[220:223],v244 offset:6144
s_waitcnt lgkmcnt(6)
v_mfma_f32_16x16x32_bf16 v[168:171],v[224:227],v[116:119],v[168:171]
v_mfma_f32_16x16x32_bf16 v[172:175],v[224:227],v[140:143],v[172:175]
ds_read_b128 v[224:227],v245 offset:6144
s_mov_b32 m0,s97
s_waitcnt lgkmcnt(6)
v_mfma_f32_16x16x32_bf16 v[168:171],v[204:207],v[120:123],v[168:171]
buffer_load_dwordx4 v252,s[68:71],s95 offen nt lds
v_mfma_f32_16x16x32_bf16 v[172:175],v[204:207],v[144:147],v[172:175]
ds_read_b128 v[204:207],v244 offset:7168
s_waitcnt lgkmcnt(6)
v_mfma_f32_16x16x32_bf16 v[168:171],v[208:211],v[124:127],v[168:171]
s_waitcnt lgkmcnt(5)
v_dot2c_f32_bf16 v243,s55,v200
v_mfma_f32_16x16x32_bf16 v[172:175],v[208:211],v[148:151],v[172:175]
ds_read_b128 v[208:211],v245 offset:7168
s_add_u32 m0,s97,896
s_waitcnt lgkmcnt(5)
v_mfma_f32_16x16x32_bf16 v[168:171],v[212:215],v[128:131],v[168:171]
v_dot2c_f32_bf16 v243,s55,v201
buffer_load_dwordx4 v252,s[68:71],s95 offen offset:128 nt lds
v_mfma_f32_16x16x32_bf16 v[172:175],v[212:215],v[152:155],v[172:175]
ds_read_b128 v[212:215],v244 offset:8192
s_waitcnt lgkmcnt(5)
v_mfma_f32_16x16x32_bf16 v[168:171],v[216:219],v[132:135],v[168:171]
v_dot2c_f32_bf16 v243,s55,v202
v_mfma_f32_16x16x32_bf16 v[172:175],v[216:219],v[156:159],v[172:175]
ds_read_b128 v[216:219],v245 offset:8192
s_add_u32 m0,s97,1792
s_waitcnt lgkmcnt(5)
v_mfma_f32_16x16x32_bf16 v[176:179],v[220:223],v[112:115],v[160:163]
v_dot2c_f32_bf16 v243,s55,v203
buffer_load_dwordx4 v252,s[68:71],s95 offen offset:256 nt lds
v_mfma_f32_16x16x32_bf16 v[180:183],v[220:223],v[136:139],v[164:167]
ds_read_b64_tr_b16 v[220:221],v246
ds_read_b64_tr_b16 v[222:223],v246 offset:6144
s_waitcnt lgkmcnt(6)
v_mfma_f32_16x16x32_bf16 v[176:179],v[224:227],v[116:119],v[176:179]
v_mfma_f32_16x16x32_bf16 v[180:183],v[224:227],v[140:143],v[180:183]
ds_read_b64_tr_b16 v[224:225],v247
ds_read_b64_tr_b16 v[226:227],v247 offset:6144
v_exp_f32 v168,v168
s_mov_b32 s90,s98
s_add_u32 s98,s98,12288
s_cmp_eq_u32 s98,36864
s_cmov_b32 s98,0
s_sub_u32 s46,s98,s90
v_add_u32 v244,s46,v244
v_add_u32 v245,s46,v245
s_waitcnt lgkmcnt(7)
v_mfma_f32_16x16x32_bf16 v[176:179],v[204:207],v[120:123],v[176:179]
v_exp_f32 v169,v169
v_mfma_f32_16x16x32_bf16 v[180:183],v[204:207],v[144:147],v[180:183]
ds_read_b64_tr_b16 v[204:205],v248
ds_read_b64_tr_b16 v[206:207],v248 offset:6144
v_add_f32 v241,v241,v168
v_exp_f32 v170,v170
s_add_u32 s94,s94,12288
s_add_u32 s89,s48,36864
s_add_u32 s96,s96,12288
s_cmp_eq_u32 s96,s89
s_cselect_b32 s96,s48,s96
s_waitcnt lgkmcnt(8)
v_mfma_f32_16x16x32_bf16 v[176:179],v[208:211],v[124:127],v[176:179]
v_add_f32 v241,v241,v169
v_exp_f32 v171,v171
v_cvt_pk_bf16_f32 v192,v168,v169
v_mfma_f32_16x16x32_bf16 v[180:183],v[208:211],v[148:151],v[180:183]
ds_read_b64_tr_b16 v[208:209],v249
ds_read_b64_tr_b16 v[210:211],v249 offset:6144
v_add_f32 v241,v241,v170
v_exp_f32 v172,v172
s_add_u32 s95,s95,12288
s_add_u32 s89,s49,36864
s_add_u32 s97,s97,12288
s_cmp_eq_u32 s97,s89
s_cselect_b32 s97,s49,s97
s_waitcnt lgkmcnt(9)
v_mfma_f32_16x16x32_bf16 v[176:179],v[212:215],v[128:131],v[176:179]
v_add_f32 v241,v241,v171
v_exp_f32 v173,v173
v_cvt_pk_bf16_f32 v193,v170,v171
v_mfma_f32_16x16x32_bf16 v[180:183],v[212:215],v[152:155],v[180:183]
ds_read_b64_tr_b16 v[212:213],v246 offset:1024
ds_read_b64_tr_b16 v[214:215],v246 offset:7168
v_add_f32 v242,v242,v172
v_exp_f32 v174,v174
s_waitcnt lgkmcnt(10)
v_mfma_f32_16x16x32_bf16 v[176:179],v[216:219],v[132:135],v[176:179]
v_add_f32 v242,v242,v173
v_exp_f32 v175,v175
v_cvt_pk_bf16_f32 v196,v172,v173
v_mfma_f32_16x16x32_bf16 v[180:183],v[216:219],v[156:159],v[180:183]
ds_read_b64_tr_b16 v[216:217],v247 offset:1024
ds_read_b64_tr_b16 v[218:219],v247 offset:7168
v_add_f32 v242,v242,v174
s_waitcnt lgkmcnt(10)
v_mfma_f32_16x16x32_bf16 v[4:7],v[220:223],v[184:187],v[4:7]
v_add_f32 v242,v242,v175
v_cvt_pk_bf16_f32 v197,v174,v175
v_mfma_f32_16x16x32_bf16 v[52:55],v[220:223],v[188:191],v[52:55]
v_mfma_f32_16x16x32_bf16 v[100:103],v[220:223],v[200:203],v[100:103]
ds_read_b64_tr_b16 v[220:221],v248 offset:1024
ds_read_b64_tr_b16 v[222:223],v248 offset:7168
s_waitcnt lgkmcnt(10)
v_mfma_f32_16x16x32_bf16 v[8:11],v[224:227],v[184:187],v[8:11]
v_mfma_f32_16x16x32_bf16 v[56:59],v[224:227],v[188:191],v[56:59]
ds_read_b64_tr_b16 v[224:225],v249 offset:1024
ds_read_b64_tr_b16 v[226:227],v249 offset:7168
s_waitcnt lgkmcnt(10)
v_mfma_f32_16x16x32_bf16 v[12:15],v[204:207],v[184:187],v[12:15]
v_mfma_f32_16x16x32_bf16 v[60:63],v[204:207],v[188:191],v[60:63]
ds_read_b64_tr_b16 v[204:205],v246 offset:2048
ds_read_b64_tr_b16 v[206:207],v246 offset:8192
s_waitcnt lgkmcnt(10)
v_mfma_f32_16x16x32_bf16 v[16:19],v[208:211],v[184:187],v[16:19]
v_mfma_f32_16x16x32_bf16 v[64:67],v[208:211],v[188:191],v[64:67]
ds_read_b64_tr_b16 v[208:209],v247 offset:2048
ds_read_b64_tr_b16 v[210:211],v247 offset:8192
s_waitcnt lgkmcnt(10)
v_mfma_f32_16x16x32_bf16 v[20:23],v[212:215],v[184:187],v[20:23]
v_mfma_f32_16x16x32_bf16 v[68:71],v[212:215],v[188:191],v[68:71]
v_mfma_f32_16x16x32_bf16 v[104:107],v[212:215],v[200:203],v[104:107]
ds_read_b64_tr_b16 v[212:213],v248 offset:2048
ds_read_b64_tr_b16 v[214:215],v248 offset:8192
s_waitcnt lgkmcnt(10)
v_mfma_f32_16x16x32_bf16 v[24:27],v[216:219],v[184:187],v[24:27]
v_mfma_f32_16x16x32_bf16 v[72:75],v[216:219],v[188:191],v[72:75]
ds_read_b64_tr_b16 v[216:217],v249 offset:2048
ds_read_b64_tr_b16 v[218:219],v249 offset:8192
s_waitcnt lgkmcnt(10)
v_mfma_f32_16x16x32_bf16 v[28:31],v[220:223],v[184:187],v[28:31]
s_mov_b32 s90,s99
s_add_u32 s99,s99,12288
s_cmp_eq_u32 s99,36864
s_cmov_b32 s99,0
s_sub_u32 s46,s99,s90
v_add_u32 v246,s46,v246
v_add_u32 v247,s46,v247
v_add_u32 v248,s46,v248
v_add_u32 v249,s46,v249
v_mfma_f32_16x16x32_bf16 v[76:79],v[220:223],v[188:191],v[76:79]
s_waitcnt lgkmcnt(8)
v_mfma_f32_16x16x32_bf16 v[32:35],v[224:227],v[184:187],v[32:35]
)A"
R"A(v_mfma_f32_16x16x32_bf16 v[80:83],v[224:227],v[188:191],v[80:83]
s_cmp_lg_u32 s45,0
s_cbranch_scc1 W1_LOOP
s_cmp_eq_u32 s47,0
s_cselect_b32 s94,5,3
s_mul_i32 s94,s94,393216
s_waitcnt vmcnt(6) lgkmcnt(0)
s_barrier
ds_read_b128 v[220:223],v244
ds_read_b128 v[224:227],v245
v_mfma_f32_16x16x32_bf16 v[36:39],v[204:207],v[184:187],v[36:39]
v_exp_f32 v176,v176
s_mov_b32 m0,s96
v_mfma_f32_16x16x32_bf16 v[84:87],v[204:207],v[188:191],v[84:87]
v_exp_f32 v177,v177
s_cmp_lg_u32 s34,1
s_cbranch_scc1 LG26
buffer_load_dwordx4 v253,s[80:83],s94 offen lds
LG26:
v_mfma_f32_16x16x32_bf16 v[108:111],v[204:207],v[200:203],v[108:111]
ds_read_b128 v[204:207],v244 offset:1024
v_add_f32 v241,v241,v176
v_exp_f32 v178,v178
v_mfma_f32_16x16x32_bf16 v[40:43],v[208:211],v[184:187],v[40:43]
v_add_f32 v241,v241,v177
v_exp_f32 v179,v179
v_cvt_pk_bf16_f32 v194,v176,v177
v_mfma_f32_16x16x32_bf16 v[88:91],v[208:211],v[188:191],v[88:91]
ds_read_b128 v[208:211],v245 offset:1024
ds_read_b128 v[200:203],v250 offset:7168
v_add_f32 v241,v241,v178
v_exp_f32 v180,v180
s_add_u32 m0,s96,896
v_mfma_f32_16x16x32_bf16 v[44:47],v[212:215],v[184:187],v[44:47]
v_add_f32 v241,v241,v179
v_exp_f32 v181,v181
v_cvt_pk_bf16_f32 v195,v178,v179
s_cmp_lg_u32 s34,1
s_cbranch_scc1 LG27
buffer_load_dwordx4 v253,s[80:83],s94 offen offset:128 lds
LG27:
v_mfma_f32_16x16x32_bf16 v[92:95],v[212:215],v[188:191],v[92:95]
ds_read_b128 v[212:215],v244 offset:2048
v_add_f32 v242,v242,v180
v_exp_f32 v182,v182
v_mfma_f32_16x16x32_bf16 v[48:51],v[216:219],v[184:187],v[48:51]
v_add_f32 v242,v242,v181
v_exp_f32 v183,v183
v_cvt_pk_bf16_f32 v198,v180,v181
v_mfma_f32_16x16x32_bf16 v[96:99],v[216:219],v[188:191],v[96:99]
ds_read_b128 v[216:219],v245 offset:2048
v_add_f32 v242,v242,v182
s_add_u32 m0,s96,1792
s_waitcnt lgkmcnt(6)
v_mfma_f32_16x16x32_bf16 v[168:171],v[220:223],v[112:115],v[160:163]
v_add_f32 v242,v242,v183
v_cvt_pk_bf16_f32 v199,v182,v183
s_cmp_lg_u32 s34,1
s_cbranch_scc1 LG28
buffer_load_dwordx4 v253,s[80:83],s94 offen offset:256 lds
LG28:
v_mfma_f32_16x16x32_bf16 v[172:175],v[220:223],v[136:139],v[164:167]
ds_read_b128 v[220:223],v244 offset:6144
s_waitcnt lgkmcnt(6)
v_mfma_f32_16x16x32_bf16 v[168:171],v[224:227],v[116:119],v[168:171]
v_mfma_f32_16x16x32_bf16 v[172:175],v[224:227],v[140:143],v[172:175]
ds_read_b128 v[224:227],v245 offset:6144
s_mov_b32 m0,s97
s_waitcnt lgkmcnt(6)
v_mfma_f32_16x16x32_bf16 v[168:171],v[204:207],v[120:123],v[168:171]
buffer_load_dwordx4 v252,s[68:71],s95 offen nt lds
v_mfma_f32_16x16x32_bf16 v[172:175],v[204:207],v[144:147],v[172:175]
ds_read_b128 v[204:207],v244 offset:7168
ds_read_b128 v[228:231],v250
ds_read_b128 v[232:235],v250 offset:1024
s_waitcnt lgkmcnt(8)
v_mfma_f32_16x16x32_bf16 v[168:171],v[208:211],v[124:127],v[168:171]
s_waitcnt lgkmcnt(7)
v_dot2c_f32_bf16 v243,s55,v200
v_mfma_f32_16x16x32_bf16 v[172:175],v[208:211],v[148:151],v[172:175]
ds_read_b128 v[208:211],v245 offset:7168
s_add_u32 m0,s97,896
s_waitcnt lgkmcnt(7)
v_mfma_f32_16x16x32_bf16 v[168:171],v[212:215],v[128:131],v[168:171]
v_dot2c_f32_bf16 v243,s55,v201
v_mov_b32 v236,v240
v_mov_b32 v237,v240
v_mov_b32 v238,v240
v_mov_b32 v239,v240
buffer_load_dwordx4 v252,s[68:71],s95 offen offset:128 nt lds
v_mfma_f32_16x16x32_bf16 v[172:175],v[212:215],v[152:155],v[172:175]
ds_read_b128 v[212:215],v244 offset:8192
s_waitcnt lgkmcnt(7)
v_mfma_f32_16x16x32_bf16 v[168:171],v[216:219],v[132:135],v[168:171]
v_dot2c_f32_bf16 v243,s55,v202
v_mfma_f32_16x16x32_bf16 v[172:175],v[216:219],v[156:159],v[172:175]
ds_read_b128 v[216:219],v245 offset:8192
s_add_u32 m0,s97,1792
s_waitcnt lgkmcnt(7)
v_mfma_f32_16x16x32_bf16 v[176:179],v[220:223],v[112:115],v[160:163]
v_dot2c_f32_bf16 v243,s55,v203
buffer_load_dwordx4 v252,s[68:71],s95 offen offset:256 nt lds
v_mfma_f32_16x16x32_bf16 v[180:183],v[220:223],v[136:139],v[164:167]
s_waitcnt lgkmcnt(4)
v_mfma_f32_16x16x32_bf16 v[236:239],v[220:223],v[228:231],v[236:239]
ds_read_b64_tr_b16 v[220:221],v246
ds_read_b64_tr_b16 v[222:223],v246 offset:6144
ds_read_b128 v[228:231],v250 offset:2048
v_mfma_f32_16x16x32_bf16 v[176:179],v[224:227],v[116:119],v[176:179]
v_exp_f32 v168,v168
v_mfma_f32_16x16x32_bf16 v[180:183],v[224:227],v[140:143],v[180:183]
v_exp_f32 v169,v169
s_waitcnt lgkmcnt(6)
v_mfma_f32_16x16x32_bf16 v[236:239],v[224:227],v[232:235],v[236:239]
ds_read_b64_tr_b16 v[224:225],v247
ds_read_b64_tr_b16 v[226:227],v247 offset:6144
ds_read_b128 v[232:235],v250 offset:3072
v_add_f32 v241,v241,v168
v_exp_f32 v170,v170
s_add_u32 s89,s48,36864
s_add_u32 s96,s96,12288
s_cmp_eq_u32 s96,s89
s_cselect_b32 s96,s48,s96
s_mov_b32 s90,s98
s_add_u32 s98,s98,12288
s_cmp_eq_u32 s98,36864
s_cmov_b32 s98,0
s_sub_u32 s46,s98,s90
v_add_u32 v244,s46,v244
v_add_u32 v245,s46,v245
v_mfma_f32_16x16x32_bf16 v[176:179],v[204:207],v[120:123],v[176:179]
v_add_f32 v241,v241,v169
v_exp_f32 v171,v171
v_cvt_pk_bf16_f32 v184,v168,v169
v_mfma_f32_16x16x32_bf16 v[180:183],v[204:207],v[144:147],v[180:183]
v_add_f32 v241,v241,v170
v_exp_f32 v172,v172
s_cmp_eq_u32 s47,0
s_cselect_b32 s94,0,1
s_mul_i32 s94,s94,393216
s_waitcnt lgkmcnt(3)
v_mfma_f32_16x16x32_bf16 v[236:239],v[204:207],v[228:231],v[236:239]
ds_read_b64_tr_b16 v[204:205],v248
ds_read_b64_tr_b16 v[206:207],v248 offset:6144
ds_read_b128 v[228:231],v250 offset:4096
v_add_f32 v241,v241,v171
v_exp_f32 v173,v173
v_cvt_pk_bf16_f32 v185,v170,v171
v_mfma_f32_16x16x32_bf16 v[176:179],v[208:211],v[124:127],v[176:179]
v_add_f32 v242,v242,v172
v_exp_f32 v174,v174
v_mfma_f32_16x16x32_bf16 v[180:183],v[208:211],v[148:151],v[180:183]
v_add_f32 v242,v242,v173
v_exp_f32 v175,v175
v_cvt_pk_bf16_f32 v188,v172,v173
s_waitcnt lgkmcnt(3)
v_mfma_f32_16x16x32_bf16 v[236:239],v[208:211],v[232:235],v[236:239]
ds_read_b64_tr_b16 v[208:209],v249
ds_read_b64_tr_b16 v[210:211],v249 offset:6144
ds_read_b128 v[232:235],v250 offset:5120
v_add_f32 v242,v242,v174
v_mfma_f32_16x16x32_bf16 v[176:179],v[212:215],v[128:131],v[176:179]
v_add_f32 v242,v242,v175
v_cvt_pk_bf16_f32 v189,v174,v175
v_mfma_f32_16x16x32_bf16 v[180:183],v[212:215],v[152:155],v[180:183]
s_waitcnt lgkmcnt(3)
v_mfma_f32_16x16x32_bf16 v[236:239],v[212:215],v[228:231],v[236:239]
ds_read_b64_tr_b16 v[212:213],v246 offset:1024
ds_read_b64_tr_b16 v[214:215],v246 offset:7168
v_mfma_f32_16x16x32_bf16 v[176:179],v[216:219],v[132:135],v[176:179]
v_mfma_f32_16x16x32_bf16 v[180:183],v[216:219],v[156:159],v[180:183]
s_waitcnt lgkmcnt(2)
v_mfma_f32_16x16x32_bf16 v[236:239],v[216:219],v[232:235],v[236:239]
ds_read_b64_tr_b16 v[216:217],v247 offset:1024
ds_read_b64_tr_b16 v[218:219],v247 offset:7168
v_mfma_f32_16x16x32_bf16 v[4:7],v[220:223],v[192:195],v[4:7]
v_mfma_f32_16x16x32_bf16 v[52:55],v[220:223],v[196:199],v[52:55]
v_mfma_f32_16x16x32_bf16 v[100:103],v[220:223],v[200:203],v[100:103]
ds_read_b64_tr_b16 v[220:221],v248 offset:1024
ds_read_b64_tr_b16 v[222:223],v248 offset:7168
v_mfma_f32_16x16x32_bf16 v[8:11],v[224:227],v[192:195],v[8:11]
v_exp_f32 v236,v236
v_mfma_f32_16x16x32_bf16 v[56:59],v[224:227],v[196:199],v[56:59]
ds_read_b64_tr_b16 v[224:225],v249 offset:1024
ds_read_b64_tr_b16 v[226:227],v249 offset:7168
v_exp_f32 v237,v237
v_mfma_f32_16x16x32_bf16 v[12:15],v[204:207],v[192:195],v[12:15]
v_exp_f32 v238,v238
v_mfma_f32_16x16x32_bf16 v[60:63],v[204:207],v[196:199],v[60:63]
ds_read_b64_tr_b16 v[204:205],v246 offset:2048
ds_read_b64_tr_b16 v[206:207],v246 offset:8192
v_exp_f32 v239,v239
v_mfma_f32_16x16x32_bf16 v[16:19],v[208:211],v[192:195],v[16:19]
v_mfma_f32_16x16x32_bf16 v[64:67],v[208:211],v[196:199],v[64:67]
ds_read_b64_tr_b16 v[208:209],v247 offset:2048
ds_read_b64_tr_b16 v[210:211],v247 offset:8192
v_cvt_pk_bf16_f32 v0,v236,v237
v_cvt_pk_bf16_f32 v1,v238,v239
s_waitcnt lgkmcnt(10)
v_mfma_f32_16x16x32_bf16 v[20:23],v[212:215],v[192:195],v[20:23]
ds_write_b64 v250,v[0:1] offset:6152
v_mfma_f32_16x16x32_bf16 v[68:71],v[212:215],v[196:199],v[68:71]
v_mfma_f32_16x16x32_bf16 v[104:107],v[212:215],v[200:203],v[104:107]
ds_read_b64_tr_b16 v[212:213],v248 offset:2048
ds_read_b64_tr_b16 v[214:215],v248 offset:8192
s_waitcnt lgkmcnt(11)
v_mfma_f32_16x16x32_bf16 v[24:27],v[216:219],v[192:195],v[24:27]
v_mfma_f32_16x16x32_bf16 v[72:75],v[216:219],v[196:199],v[72:75]
ds_read_b64_tr_b16 v[216:217],v249 offset:2048
ds_read_b64_tr_b16 v[218:219],v249 offset:8192
s_waitcnt lgkmcnt(11)
v_mfma_f32_16x16x32_bf16 v[28:31],v[220:223],v[192:195],v[28:31]
s_mov_b32 s90,s99
s_add_u32 s99,s99,12288
s_cmp_eq_u32 s99,36864
s_cmov_b32 s99,0
s_sub_u32 s46,s99,s90
v_add_u32 v246,s46,v246
v_add_u32 v247,s46,v247
v_add_u32 v248,s46,v248
v_add_u32 v249,s46,v249
v_mfma_f32_16x16x32_bf16 v[76:79],v[220:223],v[196:199],v[76:79]
s_waitcnt lgkmcnt(9)
v_mfma_f32_16x16x32_bf16 v[32:35],v[224:227],v[192:195],v[32:35]
v_mfma_f32_16x16x32_bf16 v[80:83],v[224:227],v[196:199],v[80:83]
s_waitcnt vmcnt(3) lgkmcnt(0)
s_barrier
ds_read_b128 v[220:223],v244
ds_read_b128 v[224:227],v245
v_mfma_f32_16x16x32_bf16 v[36:39],v[204:207],v[192:195],v[36:39]
v_exp_f32 v176,v176
s_mov_b32 m0,s96
v_mfma_f32_16x16x32_bf16 v[84:87],v[204:207],v[196:199],v[84:87]
v_exp_f32 v177,v177
s_cmp_lg_u32 s34,1
s_cbranch_scc1 LG29
buffer_load_dwordx4 v253,s[80:83],s94 offen lds
LG29:
v_mfma_f32_16x16x32_bf16 v[108:111],v[204:207],v[200:203],v[108:111]
ds_read_b128 v[204:207],v244 offset:1024
v_add_f32 v241,v241,v176
v_exp_f32 v178,v178
v_mfma_f32_16x16x32_bf16 v[40:43],v[208:211],v[192:195],v[40:43]
v_add_f32 v241,v241,v177
v_exp_f32 v179,v179
v_cvt_pk_bf16_f32 v186,v176,v177
v_mfma_f32_16x16x32_bf16 v[88:91],v[208:211],v[196:199],v[88:91]
ds_read_b128 v[208:211],v245 offset:1024
ds_read_b128 v[200:203],v250 offset:6144
v_add_f32 v241,v241,v178
v_exp_f32 v180,v180
s_add_u32 m0,s96,896
v_mfma_f32_16x16x32_bf16 v[44:47],v[212:215],v[192:195],v[44:47]
v_add_f32 v241,v241,v179
v_exp_f32 v181,v181
v_cvt_pk_bf16_f32 v187,v178,v179
s_cmp_lg_u32 s34,1
s_cbranch_scc1 LG30
buffer_load_dwordx4 v253,s[80:83],s94 offen offset:128 lds
LG30:
v_mfma_f32_16x16x32_bf16 v[92:95],v[212:215],v[196:199],v[92:95]
ds_read_b128 v[212:215],v244 offset:2048
v_add_f32 v242,v242,v180
v_exp_f32 v182,v182
v_mfma_f32_16x16x32_bf16 v[48:51],v[216:219],v[192:195],v[48:51]
v_add_f32 v242,v242,v181
v_exp_f32 v183,v183
v_cvt_pk_bf16_f32 v190,v180,v181
v_mfma_f32_16x16x32_bf16 v[96:99],v[216:219],v[196:199],v[96:99]
ds_read_b128 v[216:219],v245 offset:2048
v_add_f32 v242,v242,v182
s_add_u32 m0,s96,1792
s_waitcnt lgkmcnt(6)
v_mfma_f32_16x16x32_bf16 v[168:171],v[220:223],v[112:115],v[160:163]
v_add_f32 v242,v242,v183
v_cvt_pk_bf16_f32 v191,v182,v183
s_cmp_lg_u32 s34,1
s_cbranch_scc1 LG31
buffer_load_dwordx4 v253,s[80:83],s94 offen offset:256 lds
LG31:
v_mfma_f32_16x16x32_bf16 v[172:175],v[220:223],v[136:139],v[164:167]
ds_read_b128 v[220:223],v244 offset:6144
s_waitcnt lgkmcnt(6)
v_mfma_f32_16x16x32_bf16 v[168:171],v[224:227],v[116:119],v[168:171]
v_mfma_f32_16x16x32_bf16 v[172:175],v[224:227],v[140:143],v[172:175]
ds_read_b128 v[224:227],v245 offset:6144
s_waitcnt lgkmcnt(6)
v_mfma_f32_16x16x32_bf16 v[168:171],v[204:207],v[120:123],v[168:171]
v_mfma_f32_16x16x32_bf16 v[172:175],v[204:207],v[144:147],v[172:175]
ds_read_b128 v[204:207],v244 offset:7168
s_add_u32 s89,s48,36864
s_add_u32 s96,s96,12288
s_cmp_eq_u32 s96,s89
s_cselect_b32 s96,s48,s96
s_waitcnt lgkmcnt(6)
v_mfma_f32_16x16x32_bf16 v[168:171],v[208:211],v[124:127],v[168:171]
s_waitcnt lgkmcnt(5)
v_dot2c_f32_bf16 v243,s55,v200
v_mfma_f32_16x16x32_bf16 v[172:175],v[208:211],v[148:151],v[172:175]
ds_read_b128 v[208:211],v245 offset:7168
s_cmp_eq_u32 s47,0
s_cselect_b32 s94,4,2
s_mul_i32 s94,s94,393216
s_waitcnt lgkmcnt(5)
v_mfma_f32_16x16x32_bf16 v[168:171],v[212:215],v[128:131],v[168:171]
v_dot2c_f32_bf16 v243,s55,v201
v_mfma_f32_16x16x32_bf16 v[172:175],v[212:215],v[152:155],v[172:175]
ds_read_b128 v[212:215],v244 offset:8192
s_xor_b32 s90,s33,1
s_mul_i32 s90,s90,18432
s_lshl_b32 s97,s84,10
s_add_u32 s97,s97,s51
s_add_u32 s97,s97,73728
s_add_u32 s97,s97,s90
s_mov_b32 s95,0
s_waitcnt lgkmcnt(5)
v_mfma_f32_16x16x32_bf16 v[168:171],v[216:219],v[132:135],v[168:171]
v_dot2c_f32_bf16 v243,s55,v202
v_mfma_f32_16x16x32_bf16 v[172:175],v[216:219],v[156:159],v[172:175]
ds_read_b128 v[216:219],v245 offset:8192
s_waitcnt lgkmcnt(5)
v_mfma_f32_16x16x32_bf16 v[176:179],v[220:223],v[112:115],v[160:163]
v_dot2c_f32_bf16 v243,s55,v203
v_mfma_f32_16x16x32_bf16 v[180:183],v[220:223],v[136:139],v[164:167]
ds_read_b64_tr_b16 v[220:221],v246
ds_read_b64_tr_b16 v[222:223],v246 offset:6144
s_waitcnt lgkmcnt(6)
v_mfma_f32_16x16x32_bf16 v[176:179],v[224:227],v[116:119],v[176:179]
v_mfma_f32_16x16x32_bf16 v[180:183],v[224:227],v[140:143],v[180:183]
ds_read_b64_tr_b16 v[224:225],v247
ds_read_b64_tr_b16 v[226:227],v247 offset:6144
v_exp_f32 v168,v168
s_mov_b32 s90,s98
s_add_u32 s98,s98,12288
s_cmp_eq_u32 s98,36864
s_cmov_b32 s98,0
s_sub_u32 s46,s98,s90
v_add_u32 v244,s46,v244
v_add_u32 v245,s46,v245
s_waitcnt lgkmcnt(7)
v_mfma_f32_16x16x32_bf16 v[176:179],v[204:207],v[120:123],v[176:179]
v_exp_f32 v169,v169
v_mfma_f32_16x16x32_bf16 v[180:183],v[204:207],v[144:147],v[180:183]
ds_read_b64_tr_b16 v[204:205],v248
ds_read_b64_tr_b16 v[206:207],v248 offset:6144
v_add_f32 v241,v241,v168
v_exp_f32 v170,v170
s_waitcnt lgkmcnt(8)
v_mfma_f32_16x16x32_bf16 v[176:179],v[208:211],v[124:127],v[176:179]
v_add_f32 v241,v241,v169
v_exp_f32 v171,v171
v_cvt_pk_bf16_f32 v192,v168,v169
v_mfma_f32_16x16x32_bf16 v[180:183],v[208:211],v[148:151],v[180:183]
ds_read_b64_tr_b16 v[208:209],v249
ds_read_b64_tr_b16 v[210:211],v249 offset:6144
v_add_f32 v241,v241,v170
v_exp_f32 v172,v172
s_waitcnt lgkmcnt(9)
v_mfma_f32_16x16x32_bf16 v[176:179],v[212:215],v[128:131],v[176:179]
v_add_f32 v241,v241,v171
v_exp_f32 v173,v173
v_cvt_pk_bf16_f32 v193,v170,v171
v_mfma_f32_16x16x32_bf16 v[180:183],v[212:215],v[152:155],v[180:183]
ds_read_b64_tr_b16 v[212:213],v246 offset:1024
ds_read_b64_tr_b16 v[214:215],v246 offset:7168
v_add_f32 v242,v242,v172
v_exp_f32 v174,v174
s_waitcnt lgkmcnt(10)
v_mfma_f32_16x16x32_bf16 v[176:179],v[216:219],v[132:135],v[176:179]
v_add_f32 v242,v242,v173
v_exp_f32 v175,v175
v_cvt_pk_bf16_f32 v196,v172,v173
v_mfma_f32_16x16x32_bf16 v[180:183],v[216:219],v[156:159],v[180:183]
ds_read_b64_tr_b16 v[216:217],v247 offset:1024
ds_read_b64_tr_b16 v[218:219],v247 offset:7168
v_add_f32 v242,v242,v174
s_waitcnt lgkmcnt(10)
v_mfma_f32_16x16x32_bf16 v[4:7],v[220:223],v[184:187],v[4:7]
v_add_f32 v242,v242,v175
v_cvt_pk_bf16_f32 v197,v174,v175
v_mfma_f32_16x16x32_bf16 v[52:55],v[220:223],v[188:191],v[52:55]
v_mfma_f32_16x16x32_bf16 v[100:103],v[220:223],v[200:203],v[100:103]
ds_read_b64_tr_b16 v[220:221],v248 offset:1024
ds_read_b64_tr_b16 v[222:223],v248 offset:7168
s_waitcnt lgkmcnt(10)
v_mfma_f32_16x16x32_bf16 v[8:11],v[224:227],v[184:187],v[8:11]
v_mfma_f32_16x16x32_bf16 v[56:59],v[224:227],v[188:191],v[56:59]
ds_read_b64_tr_b16 v[224:225],v249 offset:1024
ds_read_b64_tr_b16 v[226:227],v249 offset:7168
s_waitcnt lgkmcnt(10)
v_mfma_f32_16x16x32_bf16 v[12:15],v[204:207],v[184:187],v[12:15]
v_mfma_f32_16x16x32_bf16 v[60:63],v[204:207],v[188:191],v[60:63]
ds_read_b64_tr_b16 v[204:205],v246 offset:2048
ds_read_b64_tr_b16 v[206:207],v246 offset:8192
s_waitcnt lgkmcnt(10)
v_mfma_f32_16x16x32_bf16 v[16:19],v[208:211],v[184:187],v[16:19]
v_mfma_f32_16x16x32_bf16 v[64:67],v[208:211],v[188:191],v[64:67]
ds_read_b64_tr_b16 v[208:209],v247 offset:2048
ds_read_b64_tr_b16 v[210:211],v247 offset:8192
s_waitcnt lgkmcnt(10)
)A"
R"A(v_mfma_f32_16x16x32_bf16 v[20:23],v[212:215],v[184:187],v[20:23]
v_mfma_f32_16x16x32_bf16 v[68:71],v[212:215],v[188:191],v[68:71]
v_mfma_f32_16x16x32_bf16 v[104:107],v[212:215],v[200:203],v[104:107]
ds_read_b64_tr_b16 v[212:213],v248 offset:2048
ds_read_b64_tr_b16 v[214:215],v248 offset:8192
s_waitcnt lgkmcnt(10)
v_mfma_f32_16x16x32_bf16 v[24:27],v[216:219],v[184:187],v[24:27]
v_mfma_f32_16x16x32_bf16 v[72:75],v[216:219],v[188:191],v[72:75]
ds_read_b64_tr_b16 v[216:217],v249 offset:2048
ds_read_b64_tr_b16 v[218:219],v249 offset:8192
s_waitcnt lgkmcnt(10)
v_mfma_f32_16x16x32_bf16 v[28:31],v[220:223],v[184:187],v[28:31]
s_mov_b32 s90,s99
s_add_u32 s99,s99,12288
s_cmp_eq_u32 s99,36864
s_cmov_b32 s99,0
s_sub_u32 s46,s99,s90
v_add_u32 v246,s46,v246
v_add_u32 v247,s46,v247
v_add_u32 v248,s46,v248
v_add_u32 v249,s46,v249
v_mfma_f32_16x16x32_bf16 v[76:79],v[220:223],v[188:191],v[76:79]
s_waitcnt lgkmcnt(8)
v_mfma_f32_16x16x32_bf16 v[32:35],v[224:227],v[184:187],v[32:35]
v_mfma_f32_16x16x32_bf16 v[80:83],v[224:227],v[188:191],v[80:83]
s_waitcnt vmcnt(0) lgkmcnt(0)
s_barrier
ds_read_b64_tr_b16 v[220:221],v246
ds_read_b64_tr_b16 v[222:223],v246 offset:6144
ds_read_b64_tr_b16 v[224:225],v247
ds_read_b64_tr_b16 v[226:227],v247 offset:6144
v_mfma_f32_16x16x32_bf16 v[36:39],v[204:207],v[184:187],v[36:39]
v_exp_f32 v176,v176
s_mov_b32 m0,s96
v_mfma_f32_16x16x32_bf16 v[84:87],v[204:207],v[188:191],v[84:87]
v_exp_f32 v177,v177
s_cmp_lg_u32 s34,1
s_cbranch_scc1 LG32
buffer_load_dwordx4 v253,s[80:83],s94 offen lds
LG32:
v_mfma_f32_16x16x32_bf16 v[108:111],v[204:207],v[200:203],v[108:111]
ds_read_b64_tr_b16 v[204:205],v248
ds_read_b64_tr_b16 v[206:207],v248 offset:6144
v_add_f32 v241,v241,v176
v_exp_f32 v178,v178
s_add_u32 m0,s96,896
v_mfma_f32_16x16x32_bf16 v[40:43],v[208:211],v[184:187],v[40:43]
v_add_f32 v241,v241,v177
v_exp_f32 v179,v179
v_cvt_pk_bf16_f32 v194,v176,v177
s_cmp_lg_u32 s34,1
s_cbranch_scc1 LG33
buffer_load_dwordx4 v253,s[80:83],s94 offen offset:128 lds
LG33:
v_mfma_f32_16x16x32_bf16 v[88:91],v[208:211],v[188:191],v[88:91]
ds_read_b64_tr_b16 v[208:209],v249
ds_read_b64_tr_b16 v[210:211],v249 offset:6144
ds_read_b128 v[200:203],v250 offset:7168
v_add_f32 v241,v241,v178
v_exp_f32 v180,v180
s_add_u32 m0,s96,1792
v_mfma_f32_16x16x32_bf16 v[44:47],v[212:215],v[184:187],v[44:47]
v_add_f32 v241,v241,v179
v_exp_f32 v181,v181
v_cvt_pk_bf16_f32 v195,v178,v179
s_cmp_lg_u32 s34,1
s_cbranch_scc1 LG34
buffer_load_dwordx4 v253,s[80:83],s94 offen offset:256 lds
LG34:
v_mfma_f32_16x16x32_bf16 v[92:95],v[212:215],v[188:191],v[92:95]
ds_read_b64_tr_b16 v[212:213],v246 offset:1024
ds_read_b64_tr_b16 v[214:215],v246 offset:7168
v_add_f32 v242,v242,v180
v_exp_f32 v182,v182
s_mov_b32 m0,s97
s_mov_b32 s88,s95
v_mfma_f32_16x16x32_bf16 v[48:51],v[216:219],v[184:187],v[48:51]
v_add_f32 v242,v242,v181
v_exp_f32 v183,v183
v_cvt_pk_bf16_f32 v198,v180,v181
s_cmp_lg_u32 s34,1
s_cbranch_scc1 LG35
buffer_load_dwordx4 v254,s[76:79],s88 offen lds
LG35:
v_mfma_f32_16x16x32_bf16 v[96:99],v[216:219],v[188:191],v[96:99]
ds_read_b64_tr_b16 v[216:217],v247 offset:1024
ds_read_b64_tr_b16 v[218:219],v247 offset:7168
v_add_f32 v242,v242,v182
v_add_f32 v242,v242,v183
v_cvt_pk_bf16_f32 v199,v182,v183
s_add_u32 m0,s97,4096
s_add_u32 s88,s95,40960
s_waitcnt lgkmcnt(11)
v_mfma_f32_16x16x32_bf16 v[4:7],v[220:223],v[192:195],v[4:7]
s_cmp_lg_u32 s34,1
s_cbranch_scc1 LG36
buffer_load_dwordx4 v254,s[76:79],s88 offen lds
LG36:
v_mfma_f32_16x16x32_bf16 v[52:55],v[220:223],v[196:199],v[52:55]
s_add_u32 m0,s97,8192
s_add_u32 s88,s95,81920
s_waitcnt lgkmcnt(4)
v_mfma_f32_16x16x32_bf16 v[100:103],v[220:223],v[200:203],v[100:103]
ds_read_b64_tr_b16 v[220:221],v248 offset:1024
ds_read_b64_tr_b16 v[222:223],v248 offset:7168
s_cmp_lg_u32 s34,1
s_cbranch_scc1 LG37
buffer_load_dwordx4 v254,s[76:79],s88 offen lds
LG37:
v_mfma_f32_16x16x32_bf16 v[8:11],v[224:227],v[192:195],v[8:11]
s_add_u32 m0,s97,12288
s_add_u32 s88,s95,122880
v_mov_b32 v164,v241
s_nop 1
v_permlane32_swap_b32 v241,v164
v_add_f32 v241,v241,v164
v_mov_b32 v164,v241
s_nop 1
v_permlane16_swap_b32 v241,v164
v_add_f32 v241,v241,v164
v_rcp_f32 v0,v241
v_mfma_f32_16x16x32_bf16 v[56:59],v[224:227],v[196:199],v[56:59]
ds_read_b64_tr_b16 v[224:225],v249 offset:1024
ds_read_b64_tr_b16 v[226:227],v249 offset:7168
s_cmp_lg_u32 s34,1
s_cbranch_scc1 LG38
buffer_load_dwordx4 v254,s[76:79],s88 offen lds
LG38:
v_mov_b32 v165,v242
s_nop 1
v_permlane32_swap_b32 v242,v165
v_add_f32 v242,v242,v165
v_mov_b32 v165,v242
s_nop 1
v_permlane16_swap_b32 v242,v165
v_add_f32 v242,v242,v165
v_rcp_f32 v2,v242
v_mfma_f32_16x16x32_bf16 v[12:15],v[204:207],v[192:195],v[12:15]
s_add_u32 m0,s97,16384
s_add_u32 s88,s95,163840
v_mfma_f32_16x16x32_bf16 v[60:63],v[204:207],v[196:199],v[60:63]
ds_read_b64_tr_b16 v[204:205],v246 offset:2048
ds_read_b64_tr_b16 v[206:207],v246 offset:8192
s_cmp_lg_u32 s34,1
s_cbranch_scc1 LG39
s_cmp_ge_u32 s84,2
s_cbranch_scc1 LX40
buffer_load_dwordx4 v254,s[76:79],s88 offen lds
LX40:
LG39:
v_mfma_f32_16x16x32_bf16 v[16:19],v[208:211],v[192:195],v[16:19]
v_dot2c_f32_bf16 v243,s55,v200
v_mfma_f32_16x16x32_bf16 v[64:67],v[208:211],v[196:199],v[64:67]
ds_read_b64_tr_b16 v[208:209],v247 offset:2048
ds_read_b64_tr_b16 v[210:211],v247 offset:8192
s_waitcnt lgkmcnt(10)
v_mfma_f32_16x16x32_bf16 v[20:23],v[212:215],v[192:195],v[20:23]
v_dot2c_f32_bf16 v243,s55,v201
v_mfma_f32_16x16x32_bf16 v[68:71],v[212:215],v[196:199],v[68:71]
v_mfma_f32_16x16x32_bf16 v[104:107],v[212:215],v[200:203],v[104:107]
ds_read_b64_tr_b16 v[212:213],v248 offset:2048
ds_read_b64_tr_b16 v[214:215],v248 offset:8192
s_waitcnt lgkmcnt(10)
v_mfma_f32_16x16x32_bf16 v[24:27],v[216:219],v[192:195],v[24:27]
v_dot2c_f32_bf16 v243,s55,v202
v_mfma_f32_16x16x32_bf16 v[72:75],v[216:219],v[196:199],v[72:75]
ds_read_b64_tr_b16 v[216:217],v249 offset:2048
ds_read_b64_tr_b16 v[218:219],v249 offset:8192
s_waitcnt lgkmcnt(10)
v_mfma_f32_16x16x32_bf16 v[28:31],v[220:223],v[192:195],v[28:31]
v_dot2c_f32_bf16 v243,s55,v203
v_mfma_f32_16x16x32_bf16 v[76:79],v[220:223],v[196:199],v[76:79]
s_waitcnt lgkmcnt(8)
v_mfma_f32_16x16x32_bf16 v[32:35],v[224:227],v[192:195],v[32:35]
v_mfma_f32_16x16x32_bf16 v[80:83],v[224:227],v[196:199],v[80:83]
v_mov_b32 v166,v243
s_nop 1
v_permlane32_swap_b32 v243,v166
v_add_f32 v243,v243,v166
v_mov_b32 v166,v243
s_nop 1
v_permlane16_swap_b32 v243,v166
v_add_f32 v243,v243,v166
v_rcp_f32 v160,v243
s_waitcnt lgkmcnt(6)
v_mfma_f32_16x16x32_bf16 v[36:39],v[204:207],v[192:195],v[36:39]
s_mov_b32 s90,0x5d800000
v_cmp_ngt_f32 s[92:93],s90,v241
v_cmp_ngt_f32 vcc,s90,v242
s_nop 0
s_or_b64 s[92:93],s[92:93],vcc
v_cmp_ngt_f32 vcc,s90,v243
s_nop 0
s_or_b64 s[92:93],s[92:93],vcc
s_cselect_b32 s90,1,0
v_mov_b32 v164,s90
s_lshl_b32 s91,s47,4
s_add_u32 s91,s91,153604
v_mov_b32 v165,s91
ds_write_b32 v165,v164
v_mbcnt_lo_u32_b32 v167,-1,0
v_mbcnt_hi_u32_b32 v167,-1,v167
v_lshlrev_b32 v167,4,v167
s_cmp_eq_u32 s33,0
s_movk_i32 s91,18432
s_cselect_b32 s91,12288,s91
s_add_u32 s91,s91,s51
s_add_u32 s91,s91,73728
v_mbcnt_lo_u32_b32 v162,-1,0
v_mbcnt_hi_u32_b32 v162,-1,v162
v_lshlrev_b32 v162,3,v162
v_add_u32 v162,s91,v162
v_mfma_f32_16x16x32_bf16 v[84:87],v[204:207],v[196:199],v[84:87]
v_pk_mul_f32 v[8:9],v[8:9],v[0:1] op_sel_hi:[1,0]
v_pk_mul_f32 v[10:11],v[10:11],v[0:1] op_sel_hi:[1,0]
v_pk_mul_f32 v[4:5],v[4:5],v[0:1] op_sel_hi:[1,0]
v_pk_mul_f32 v[6:7],v[6:7],v[0:1] op_sel_hi:[1,0]
v_cvt_pk_bf16_f32 v8,v8,v9
v_cvt_pk_bf16_f32 v9,v10,v11
v_cvt_pk_bf16_f32 v10,v4,v5
v_cvt_pk_bf16_f32 v11,v6,v7
s_mov_b32 s91,0x60000
buffer_store_dwordx4 v[8:11],v167,s[72:75],s91 offen sc1
v_mfma_f32_16x16x32_bf16 v[108:111],v[204:207],v[200:203],v[108:111]
v_pk_mul_f32 v[56:57],v[56:57],v[2:3] op_sel_hi:[1,0]
v_pk_mul_f32 v[58:59],v[58:59],v[2:3] op_sel_hi:[1,0]
v_pk_mul_f32 v[52:53],v[52:53],v[2:3] op_sel_hi:[1,0]
v_pk_mul_f32 v[54:55],v[54:55],v[2:3] op_sel_hi:[1,0]
v_cvt_pk_bf16_f32 v56,v56,v57
v_cvt_pk_bf16_f32 v57,v58,v59
v_cvt_pk_bf16_f32 v58,v52,v53
v_cvt_pk_bf16_f32 v59,v54,v55
s_mov_b32 s91,0x90000
buffer_store_dwordx4 v[56:59],v167,s[72:75],s91 offen sc1
s_waitcnt lgkmcnt(5)
v_mfma_f32_16x16x32_bf16 v[40:43],v[208:211],v[192:195],v[40:43]
v_pk_mul_f32 v[16:17],v[16:17],v[0:1] op_sel_hi:[1,0]
v_pk_mul_f32 v[18:19],v[18:19],v[0:1] op_sel_hi:[1,0]
v_pk_mul_f32 v[12:13],v[12:13],v[0:1] op_sel_hi:[1,0]
v_pk_mul_f32 v[14:15],v[14:15],v[0:1] op_sel_hi:[1,0]
v_cvt_pk_bf16_f32 v16,v16,v17
v_cvt_pk_bf16_f32 v17,v18,v19
v_cvt_pk_bf16_f32 v18,v12,v13
v_cvt_pk_bf16_f32 v19,v14,v15
s_mov_b32 s91,0x60400
buffer_store_dwordx4 v[16:19],v167,s[72:75],s91 offen sc1
v_mfma_f32_16x16x32_bf16 v[88:91],v[208:211],v[196:199],v[88:91]
v_pk_mul_f32 v[64:65],v[64:65],v[2:3] op_sel_hi:[1,0]
v_pk_mul_f32 v[66:67],v[66:67],v[2:3] op_sel_hi:[1,0]
v_pk_mul_f32 v[60:61],v[60:61],v[2:3] op_sel_hi:[1,0]
v_pk_mul_f32 v[62:63],v[62:63],v[2:3] op_sel_hi:[1,0]
v_cvt_pk_bf16_f32 v64,v64,v65
v_cvt_pk_bf16_f32 v65,v66,v67
v_cvt_pk_bf16_f32 v66,v60,v61
v_cvt_pk_bf16_f32 v67,v62,v63
s_mov_b32 s91,0x90400
buffer_store_dwordx4 v[64:67],v167,s[72:75],s91 offen sc1
s_waitcnt lgkmcnt(3)
v_mfma_f32_16x16x32_bf16 v[44:47],v[212:215],v[192:195],v[44:47]
v_pk_mul_f32 v[100:101],v[100:101],v[160:161] op_sel_hi:[1,0]
v_pk_mul_f32 v[102:103],v[102:103],v[160:161] op_sel_hi:[1,0]
v_cvt_pk_bf16_f32 v100,v100,v101
v_cvt_pk_bf16_f32 v101,v102,v103
ds_write_b64 v162,v[100:101]
v_mfma_f32_16x16x32_bf16 v[92:95],v[212:215],v[196:199],v[92:95]
v_pk_mul_f32 v[24:25],v[24:25],v[0:1] op_sel_hi:[1,0]
v_pk_mul_f32 v[26:27],v[26:27],v[0:1] op_sel_hi:[1,0]
v_pk_mul_f32 v[20:21],v[20:21],v[0:1] op_sel_hi:[1,0]
v_pk_mul_f32 v[22:23],v[22:23],v[0:1] op_sel_hi:[1,0]
v_cvt_pk_bf16_f32 v24,v24,v25
v_cvt_pk_bf16_f32 v25,v26,v27
v_cvt_pk_bf16_f32 v26,v20,v21
v_cvt_pk_bf16_f32 v27,v22,v23
s_mov_b32 s91,0x60800
buffer_store_dwordx4 v[24:27],v167,s[72:75],s91 offen sc1
s_waitcnt lgkmcnt(2)
v_mfma_f32_16x16x32_bf16 v[48:51],v[216:219],v[192:195],v[48:51]
v_pk_mul_f32 v[72:73],v[72:73],v[2:3] op_sel_hi:[1,0]
v_pk_mul_f32 v[74:75],v[74:75],v[2:3] op_sel_hi:[1,0]
v_pk_mul_f32 v[68:69],v[68:69],v[2:3] op_sel_hi:[1,0]
v_pk_mul_f32 v[70:71],v[70:71],v[2:3] op_sel_hi:[1,0]
v_cvt_pk_bf16_f32 v72,v72,v73
v_cvt_pk_bf16_f32 v73,v74,v75
v_cvt_pk_bf16_f32 v74,v68,v69
v_cvt_pk_bf16_f32 v75,v70,v71
s_mov_b32 s91,0x90800
buffer_store_dwordx4 v[72:75],v167,s[72:75],s91 offen sc1
v_mfma_f32_16x16x32_bf16 v[96:99],v[216:219],v[196:199],v[96:99]
v_pk_mul_f32 v[32:33],v[32:33],v[0:1] op_sel_hi:[1,0]
v_pk_mul_f32 v[34:35],v[34:35],v[0:1] op_sel_hi:[1,0]
v_pk_mul_f32 v[28:29],v[28:29],v[0:1] op_sel_hi:[1,0]
v_pk_mul_f32 v[30:31],v[30:31],v[0:1] op_sel_hi:[1,0]
v_cvt_pk_bf16_f32 v32,v32,v33
v_cvt_pk_bf16_f32 v33,v34,v35
v_cvt_pk_bf16_f32 v34,v28,v29
v_cvt_pk_bf16_f32 v35,v30,v31
s_mov_b32 s91,0x60c00
buffer_store_dwordx4 v[32:35],v167,s[72:75],s91 offen sc1
v_pk_mul_f32 v[80:81],v[80:81],v[2:3] op_sel_hi:[1,0]
v_pk_mul_f32 v[82:83],v[82:83],v[2:3] op_sel_hi:[1,0]
v_pk_mul_f32 v[76:77],v[76:77],v[2:3] op_sel_hi:[1,0]
v_pk_mul_f32 v[78:79],v[78:79],v[2:3] op_sel_hi:[1,0]
v_cvt_pk_bf16_f32 v80,v80,v81
v_cvt_pk_bf16_f32 v81,v82,v83
v_cvt_pk_bf16_f32 v82,v76,v77
v_cvt_pk_bf16_f32 v83,v78,v79
s_mov_b32 s91,0x90c00
buffer_store_dwordx4 v[80:83],v167,s[72:75],s91 offen sc1
v_pk_mul_f32 v[104:105],v[104:105],v[160:161] op_sel_hi:[1,0]
v_pk_mul_f32 v[106:107],v[106:107],v[160:161] op_sel_hi:[1,0]
v_cvt_pk_bf16_f32 v104,v104,v105
v_cvt_pk_bf16_f32 v105,v106,v107
ds_write_b64 v162,v[104:105] offset:512
v_pk_mul_f32 v[40:41],v[40:41],v[0:1] op_sel_hi:[1,0]
v_pk_mul_f32 v[42:43],v[42:43],v[0:1] op_sel_hi:[1,0]
v_pk_mul_f32 v[36:37],v[36:37],v[0:1] op_sel_hi:[1,0]
v_pk_mul_f32 v[38:39],v[38:39],v[0:1] op_sel_hi:[1,0]
v_cvt_pk_bf16_f32 v40,v40,v41
v_cvt_pk_bf16_f32 v41,v42,v43
v_cvt_pk_bf16_f32 v42,v36,v37
v_cvt_pk_bf16_f32 v43,v38,v39
s_mov_b32 s91,0x61000
buffer_store_dwordx4 v[40:43],v167,s[72:75],s91 offen sc1
v_pk_mul_f32 v[88:89],v[88:89],v[2:3] op_sel_hi:[1,0]
v_pk_mul_f32 v[90:91],v[90:91],v[2:3] op_sel_hi:[1,0]
v_pk_mul_f32 v[84:85],v[84:85],v[2:3] op_sel_hi:[1,0]
v_pk_mul_f32 v[86:87],v[86:87],v[2:3] op_sel_hi:[1,0]
v_cvt_pk_bf16_f32 v88,v88,v89
v_cvt_pk_bf16_f32 v89,v90,v91
v_cvt_pk_bf16_f32 v90,v84,v85
v_cvt_pk_bf16_f32 v91,v86,v87
s_mov_b32 s91,0x91000
buffer_store_dwordx4 v[88:91],v167,s[72:75],s91 offen sc1
v_pk_mul_f32 v[48:49],v[48:49],v[0:1] op_sel_hi:[1,0]
v_pk_mul_f32 v[50:51],v[50:51],v[0:1] op_sel_hi:[1,0]
v_pk_mul_f32 v[44:45],v[44:45],v[0:1] op_sel_hi:[1,0]
v_pk_mul_f32 v[46:47],v[46:47],v[0:1] op_sel_hi:[1,0]
v_cvt_pk_bf16_f32 v48,v48,v49
v_cvt_pk_bf16_f32 v49,v50,v51
v_cvt_pk_bf16_f32 v50,v44,v45
v_cvt_pk_bf16_f32 v51,v46,v47
s_mov_b32 s91,0x61400
buffer_store_dwordx4 v[48:51],v167,s[72:75],s91 offen sc1
v_pk_mul_f32 v[96:97],v[96:97],v[2:3] op_sel_hi:[1,0]
v_pk_mul_f32 v[98:99],v[98:99],v[2:3] op_sel_hi:[1,0]
v_pk_mul_f32 v[92:93],v[92:93],v[2:3] op_sel_hi:[1,0]
v_pk_mul_f32 v[94:95],v[94:95],v[2:3] op_sel_hi:[1,0]
v_cvt_pk_bf16_f32 v96,v96,v97
v_cvt_pk_bf16_f32 v97,v98,v99
v_cvt_pk_bf16_f32 v98,v92,v93
v_cvt_pk_bf16_f32 v99,v94,v95
s_mov_b32 s91,0x91400
buffer_store_dwordx4 v[96:99],v167,s[72:75],s91 offen sc1
v_pk_mul_f32 v[108:109],v[108:109],v[160:161] op_sel_hi:[1,0]
v_pk_mul_f32 v[110:111],v[110:111],v[160:161] op_sel_hi:[1,0]
v_cvt_pk_bf16_f32 v108,v108,v109
v_cvt_pk_bf16_f32 v109,v110,v111
ds_write_b64 v162,v[108:109] offset:1024
s_waitcnt lgkmcnt(0)
s_barrier
v_mov_b32 v1,0x25800
ds_read_b128 v[168:171],v1
ds_read_b128 v[172:175],v1 offset:16
s_waitcnt lgkmcnt(1)
v_or_b32 v0,v168,v169
v_or3_b32 v0,v0,v170,v171
s_waitcnt lgkmcnt(0)
v_or3_b32 v0,v0,v172,v173
v_or3_b32 v0,v0,v174,v175
s_nop 0
v_readfirstlane_b32 s90,v0
s_nop 0
s_andn2_b32 s90,s90,s44
s_cbranch_scc0 W1_EPI
s_mov_b32 s44,1
v_mov_b32 v0,0xff800000
v_mov_b32 v1,0xff800000
v_mov_b32 v2,0xff800000
s_mov_b32 s98,0
v_mbcnt_lo_u32_b32 v168,-1,0
v_mbcnt_hi_u32_b32 v168,-1,v168
v_and_b32 v169,15,v168
v_lshrrev_b32 v170,4,v168
v_lshrrev_b32 v168,3,v169
s_movk_i32 s88,3072
v_mul_u32_u24 v244,s88,v168
v_and_b32 v168,7,v169
v_lshl_add_u32 v244,v168,7,v244
v_lshrrev_b32 v168,1,v169
v_xor_b32 v168,v168,v170
v_lshl_add_u32 v244,v168,4,v244
v_xor_b32 v245,64,v244
s_add_u32 s88,s51,s98
v_add_u32 v244,s88,v244
v_add_u32 v245,s88,v245
s_mov_b32 s94,0
s_mov_b32 s96,s48
W1_MAX:
s_waitcnt vmcnt(0) lgkmcnt(0)
s_barrier
)A"
R"A(s_mov_b32 m0,s96
s_nop 0
buffer_load_dwordx4 v251,s[64:67],s94 offen nt lds
s_add_u32 m0,s96,896
s_nop 0
buffer_load_dwordx4 v251,s[64:67],s94 offen offset:128 nt lds
s_add_u32 m0,s96,1792
s_nop 0
buffer_load_dwordx4 v251,s[64:67],s94 offen offset:256 nt lds
s_waitcnt vmcnt(0)
s_barrier
ds_read_b128 v[204:207],v244
ds_read_b128 v[208:211],v245
ds_read_b128 v[212:215],v244 offset:1024
ds_read_b128 v[216:219],v245 offset:1024
ds_read_b128 v[220:223],v244 offset:2048
ds_read_b128 v[224:227],v245 offset:2048
ds_read_b128 v[228:231],v250
s_waitcnt lgkmcnt(6)
v_mfma_f32_16x16x32_bf16 v[168:171],v[204:207],v[112:115],0
v_mfma_f32_16x16x32_bf16 v[172:175],v[204:207],v[136:139],0
s_waitcnt lgkmcnt(0)
v_mfma_f32_16x16x32_bf16 v[236:239],v[204:207],v[228:231],0
ds_read_b128 v[232:235],v250 offset:1024
v_mfma_f32_16x16x32_bf16 v[168:171],v[208:211],v[116:119],v[168:171]
v_mfma_f32_16x16x32_bf16 v[172:175],v[208:211],v[140:143],v[172:175]
s_waitcnt lgkmcnt(0)
v_mfma_f32_16x16x32_bf16 v[236:239],v[208:211],v[232:235],v[236:239]
ds_read_b128 v[228:231],v250 offset:2048
v_mfma_f32_16x16x32_bf16 v[168:171],v[212:215],v[120:123],v[168:171]
v_mfma_f32_16x16x32_bf16 v[172:175],v[212:215],v[144:147],v[172:175]
s_waitcnt lgkmcnt(0)
v_mfma_f32_16x16x32_bf16 v[236:239],v[212:215],v[228:231],v[236:239]
ds_read_b128 v[232:235],v250 offset:3072
v_mfma_f32_16x16x32_bf16 v[168:171],v[216:219],v[124:127],v[168:171]
v_mfma_f32_16x16x32_bf16 v[172:175],v[216:219],v[148:151],v[172:175]
s_waitcnt lgkmcnt(0)
v_mfma_f32_16x16x32_bf16 v[236:239],v[216:219],v[232:235],v[236:239]
ds_read_b128 v[228:231],v250 offset:4096
v_mfma_f32_16x16x32_bf16 v[168:171],v[220:223],v[128:131],v[168:171]
v_mfma_f32_16x16x32_bf16 v[172:175],v[220:223],v[152:155],v[172:175]
s_waitcnt lgkmcnt(0)
v_mfma_f32_16x16x32_bf16 v[236:239],v[220:223],v[228:231],v[236:239]
ds_read_b128 v[232:235],v250 offset:5120
v_mfma_f32_16x16x32_bf16 v[168:171],v[224:227],v[132:135],v[168:171]
v_mfma_f32_16x16x32_bf16 v[172:175],v[224:227],v[156:159],v[172:175]
s_waitcnt lgkmcnt(0)
v_mfma_f32_16x16x32_bf16 v[236:239],v[224:227],v[232:235],v[236:239]
s_nop 4
v_max3_f32 v0,v0,v168,v169
v_max3_f32 v0,v0,v170,v171
v_max3_f32 v1,v1,v172,v173
v_max3_f32 v1,v1,v174,v175
v_max3_f32 v2,v2,v236,v237
v_max3_f32 v2,v2,v238,v239
ds_read_b128 v[204:207],v244 offset:6144
ds_read_b128 v[208:211],v245 offset:6144
ds_read_b128 v[212:215],v244 offset:7168
ds_read_b128 v[216:219],v245 offset:7168
ds_read_b128 v[220:223],v244 offset:8192
ds_read_b128 v[224:227],v245 offset:8192
ds_read_b128 v[228:231],v250
s_waitcnt lgkmcnt(6)
v_mfma_f32_16x16x32_bf16 v[168:171],v[204:207],v[112:115],0
v_mfma_f32_16x16x32_bf16 v[172:175],v[204:207],v[136:139],0
s_waitcnt lgkmcnt(0)
v_mfma_f32_16x16x32_bf16 v[236:239],v[204:207],v[228:231],0
ds_read_b128 v[232:235],v250 offset:1024
v_mfma_f32_16x16x32_bf16 v[168:171],v[208:211],v[116:119],v[168:171]
v_mfma_f32_16x16x32_bf16 v[172:175],v[208:211],v[140:143],v[172:175]
s_waitcnt lgkmcnt(0)
v_mfma_f32_16x16x32_bf16 v[236:239],v[208:211],v[232:235],v[236:239]
ds_read_b128 v[228:231],v250 offset:2048
v_mfma_f32_16x16x32_bf16 v[168:171],v[212:215],v[120:123],v[168:171]
v_mfma_f32_16x16x32_bf16 v[172:175],v[212:215],v[144:147],v[172:175]
s_waitcnt lgkmcnt(0)
v_mfma_f32_16x16x32_bf16 v[236:239],v[212:215],v[228:231],v[236:239]
ds_read_b128 v[232:235],v250 offset:3072
v_mfma_f32_16x16x32_bf16 v[168:171],v[216:219],v[124:127],v[168:171]
v_mfma_f32_16x16x32_bf16 v[172:175],v[216:219],v[148:151],v[172:175]
s_waitcnt lgkmcnt(0)
v_mfma_f32_16x16x32_bf16 v[236:239],v[216:219],v[232:235],v[236:239]
ds_read_b128 v[228:231],v250 offset:4096
v_mfma_f32_16x16x32_bf16 v[168:171],v[220:223],v[128:131],v[168:171]
v_mfma_f32_16x16x32_bf16 v[172:175],v[220:223],v[152:155],v[172:175]
s_waitcnt lgkmcnt(0)
v_mfma_f32_16x16x32_bf16 v[236:239],v[220:223],v[228:231],v[236:239]
ds_read_b128 v[232:235],v250 offset:5120
v_mfma_f32_16x16x32_bf16 v[168:171],v[224:227],v[132:135],v[168:171]
v_mfma_f32_16x16x32_bf16 v[172:175],v[224:227],v[156:159],v[172:175]
s_waitcnt lgkmcnt(0)
v_mfma_f32_16x16x32_bf16 v[236:239],v[224:227],v[232:235],v[236:239]
s_nop 4
v_max3_f32 v0,v0,v168,v169
v_max3_f32 v0,v0,v170,v171
v_max3_f32 v1,v1,v172,v173
v_max3_f32 v1,v1,v174,v175
v_max3_f32 v2,v2,v236,v237
v_max3_f32 v2,v2,v238,v239
s_add_u32 s94,s94,12288
s_cmp_lt_u32 s94,196608
s_cbranch_scc1 W1_MAX
v_mov_b32 v3,v0
s_nop 1
v_permlane32_swap_b32 v0,v3
v_max_f32 v0,v0,v3
v_mov_b32 v3,v0
s_nop 1
v_permlane16_swap_b32 v0,v3
v_max_f32 v0,v0,v3
v_mov_b32 v3,v1
s_nop 1
v_permlane32_swap_b32 v1,v3
v_max_f32 v1,v1,v3
v_mov_b32 v3,v1
s_nop 1
v_permlane16_swap_b32 v1,v3
v_max_f32 v1,v1,v3
v_mov_b32 v3,v2
s_nop 1
v_permlane32_swap_b32 v2,v3
v_max_f32 v2,v2,v3
v_mov_b32 v3,v2
s_nop 1
v_permlane16_swap_b32 v2,v3
v_max_f32 v2,v2,v3
v_xor_b32 v160,0x80000000,v0
v_xor_b32 v164,0x80000000,v1
v_xor_b32 v161,0x80000000,v0
v_xor_b32 v165,0x80000000,v1
v_xor_b32 v162,0x80000000,v0
v_xor_b32 v166,0x80000000,v1
v_xor_b32 v163,0x80000000,v0
v_xor_b32 v167,0x80000000,v1
v_xor_b32 v240,0x80000000,v2
s_waitcnt vmcnt(0) lgkmcnt(0)
s_barrier
s_mul_i32 s98,s47,12288
s_add_u32 s96,s48,s98
s_mul_i32 s88,s35,12288
s_add_u32 s97,s88,s49
s_mov_b32 s94,0
s_mov_b32 s95,0
s_mov_b32 m0,s96
s_nop 0
buffer_load_dwordx4 v251,s[64:67],s94 offen nt lds
s_add_u32 m0,s96,896
s_nop 0
buffer_load_dwordx4 v251,s[64:67],s94 offen offset:128 nt lds
s_add_u32 m0,s96,1792
s_nop 0
buffer_load_dwordx4 v251,s[64:67],s94 offen offset:256 nt lds
s_mov_b32 m0,s97
s_nop 0
buffer_load_dwordx4 v252,s[68:71],s95 offen nt lds
s_add_u32 m0,s97,896
s_nop 0
buffer_load_dwordx4 v252,s[68:71],s95 offen offset:128 nt lds
s_add_u32 m0,s97,1792
s_nop 0
buffer_load_dwordx4 v252,s[68:71],s95 offen offset:256 nt lds
s_add_u32 s94,s94,12288
s_add_u32 s89,s48,36864
s_add_u32 s96,s96,12288
s_cmp_eq_u32 s96,s89
s_cselect_b32 s96,s48,s96
s_add_u32 s95,s95,12288
s_add_u32 s89,s49,36864
s_add_u32 s97,s97,12288
s_cmp_eq_u32 s97,s89
s_cselect_b32 s97,s49,s97
s_mov_b32 m0,s96
s_nop 0
buffer_load_dwordx4 v251,s[64:67],s94 offen nt lds
s_add_u32 m0,s96,896
s_nop 0
buffer_load_dwordx4 v251,s[64:67],s94 offen offset:128 nt lds
s_add_u32 m0,s96,1792
s_nop 0
buffer_load_dwordx4 v251,s[64:67],s94 offen offset:256 nt lds
s_add_u32 s94,s94,12288
s_add_u32 s89,s48,36864
s_add_u32 s96,s96,12288
s_cmp_eq_u32 s96,s89
s_cselect_b32 s96,s48,s96
v_mbcnt_lo_u32_b32 v0,-1,0
v_mbcnt_hi_u32_b32 v0,-1,v0
v_and_b32 v1,15,v0
v_lshrrev_b32 v2,4,v0
v_lshrrev_b32 v0,3,v1
s_movk_i32 s88,3072
v_mul_u32_u24 v244,s88,v0
v_and_b32 v0,7,v1
v_lshl_add_u32 v244,v0,7,v244
v_lshrrev_b32 v0,1,v1
v_xor_b32 v0,v0,v2
v_lshl_add_u32 v244,v0,4,v244
v_xor_b32 v245,64,v244
s_add_u32 s88,s51,s98
v_add_u32 v244,s88,v244
v_add_u32 v245,s88,v245
v_mbcnt_lo_u32_b32 v0,-1,0
v_mbcnt_hi_u32_b32 v0,-1,v0
v_and_b32 v1,15,v0
v_lshrrev_b32 v2,4,v0
v_lshlrev_b32 v250,4,v0
v_add_u32 v250,s50,v250
s_movk_i32 s88,3072
v_lshrrev_b32 v3,1,v2
v_mul_u32_u24 v246,s88,v3
v_and_b32 v2,1,v2
v_lshrrev_b32 v1,2,v1
v_lshl_add_u32 v3,v2,2,v1
v_lshl_add_u32 v246,v3,7,v246
v_lshrrev_b32 v1,1,v1
v_lshl_or_b32 v1,v2,1,v1
v_xor_b32 v1,1,v1
v_lshl_add_u32 v246,v1,5,v246
v_and_b32 v0,3,v0
v_lshl_add_u32 v246,v0,3,v246
v_xor_b32 v247,32,v246
v_xor_b32 v248,64,v246
v_xor_b32 v249,0x60,v246
s_mul_i32 s99,s35,12288
s_add_u32 s88,s99,s51
s_add_u32 s88,s88,73728
v_add_u32 v246,s88,v246
v_add_u32 v247,s88,v247
v_add_u32 v248,s88,v248
v_add_u32 v249,s88,v249
s_waitcnt vmcnt(6) lgkmcnt(0)
s_barrier
s_branch W1_ATT
W1_EPI:
s_nop 3
v_mbcnt_lo_u32_b32 v0,-1,0
v_mbcnt_hi_u32_b32 v0,-1,v0
v_lshlrev_b32 v0,4,v0
s_branch LNEXT
W2_START:
v_mbcnt_lo_u32_b32 v0,-1,0
v_mbcnt_hi_u32_b32 v0,-1,v0
v_and_b32 v1,15,v0
v_lshrrev_b32 v2,4,v0
v_lshrrev_b32 v0,3,v1
s_movk_i32 s88,3072
v_mul_u32_u24 v244,s88,v0
v_and_b32 v0,7,v1
v_lshl_add_u32 v244,v0,7,v244
v_lshrrev_b32 v0,1,v1
v_xor_b32 v0,v0,v2
v_lshl_add_u32 v244,v0,4,v244
v_xor_b32 v245,64,v244
s_add_u32 s88,s51,s98
v_add_u32 v244,s88,v244
v_add_u32 v245,s88,v245
v_mbcnt_lo_u32_b32 v0,-1,0
v_mbcnt_hi_u32_b32 v0,-1,v0
v_and_b32 v1,15,v0
v_lshrrev_b32 v2,4,v0
v_lshlrev_b32 v250,4,v0
v_add_u32 v250,s50,v250
s_movk_i32 s88,3072
v_lshrrev_b32 v3,1,v2
v_mul_u32_u24 v246,s88,v3
v_and_b32 v2,1,v2
v_lshrrev_b32 v1,2,v1
v_lshl_add_u32 v3,v2,2,v1
v_lshl_add_u32 v246,v3,7,v246
v_lshrrev_b32 v1,1,v1
v_lshl_or_b32 v1,v2,1,v1
v_xor_b32 v1,2,v1
v_lshl_add_u32 v246,v1,5,v246
v_and_b32 v0,3,v0
v_lshl_add_u32 v246,v0,3,v246
v_xor_b32 v247,32,v246
v_xor_b32 v248,64,v246
v_xor_b32 v249,0x60,v246
s_mul_i32 s99,s35,12288
s_add_u32 s88,s99,s51
s_add_u32 s88,s88,73728
v_add_u32 v246,s88,v246
v_add_u32 v247,s88,v247
v_add_u32 v248,s88,v248
v_add_u32 v249,s88,v249
s_xor_b32 s90,s85,2
s_mul_i32 s88,s90,184320
s_add_u32 s76,s36,s88
s_addc_u32 s77,s37,0
s_and_b32 s77,s77,0xffff
s_mov_b32 s78,0x2d000
s_mov_b32 s79,0x20000
s_mul_i32 s88,s86,384
s_sub_u32 s89,0x780000,s88
s_add_u32 s80,s42,s88
s_addc_u32 s81,s43,0
s_and_b32 s81,s81,0xffff
s_mov_b32 s82,s89
s_mov_b32 s83,0x20000
s_mov_b32 s44,0
s_waitcnt vmcnt(3) lgkmcnt(0)
s_barrier
s_cmp_eq_u32 s35,0
s_cselect_b32 s90,12288,0
s_add_u32 s90,s90,s51
s_add_u32 s90,s90,73728
s_add_u32 s91,s47,1
s_mul_i32 s91,s91,12288
s_add_u32 s91,s91,s51
s_cmp_eq_u32 s47,0
s_cselect_b32 s92,24576,0
s_add_u32 s92,s92,s51
v_mbcnt_lo_u32_b32 v0,-1,0
v_mbcnt_hi_u32_b32 v0,-1,v0
v_lshlrev_b32 v0,4,v0
v_add_u32 v1,s91,v0
ds_read_b128 v[112:115],v1
ds_read_b128 v[116:119],v1 offset:1024
ds_read_b128 v[120:123],v1 offset:2048
ds_read_b128 v[124:127],v1 offset:3072
ds_read_b128 v[128:131],v1 offset:4096
ds_read_b128 v[132:135],v1 offset:5120
v_add_u32 v2,s91,v0
ds_read_b128 v[136:139],v2 offset:6144
ds_read_b128 v[140:143],v2 offset:7168
ds_read_b128 v[144:147],v2 offset:8192
ds_read_b128 v[148:151],v2 offset:9216
ds_read_b128 v[152:155],v2 offset:10240
ds_read_b128 v[156:159],v2 offset:11264
ds_read_b128 v[4:7],v244
ds_read_b128 v[8:11],v245
ds_read_b128 v[12:15],v244 offset:1024
ds_read_b128 v[16:19],v245 offset:1024
ds_read_b128 v[20:23],v244 offset:2048
ds_read_b128 v[24:27],v245 offset:2048
ds_read_b128 v[28:31],v250
s_waitcnt lgkmcnt(6)
v_mfma_f32_16x16x32_bf16 v[36:39],v[4:7],v[112:115],0
v_mfma_f32_16x16x32_bf16 v[40:43],v[4:7],v[136:139],0
s_waitcnt lgkmcnt(0)
v_mfma_f32_16x16x32_bf16 v[44:47],v[4:7],v[28:31],0
ds_read_b128 v[32:35],v250 offset:1024
v_mfma_f32_16x16x32_bf16 v[36:39],v[8:11],v[116:119],v[36:39]
v_mfma_f32_16x16x32_bf16 v[40:43],v[8:11],v[140:143],v[40:43]
s_waitcnt lgkmcnt(0)
v_mfma_f32_16x16x32_bf16 v[44:47],v[8:11],v[32:35],v[44:47]
ds_read_b128 v[28:31],v250 offset:2048
v_mfma_f32_16x16x32_bf16 v[36:39],v[12:15],v[120:123],v[36:39]
v_mfma_f32_16x16x32_bf16 v[40:43],v[12:15],v[144:147],v[40:43]
s_waitcnt lgkmcnt(0)
v_mfma_f32_16x16x32_bf16 v[44:47],v[12:15],v[28:31],v[44:47]
ds_read_b128 v[32:35],v250 offset:3072
v_mfma_f32_16x16x32_bf16 v[36:39],v[16:19],v[124:127],v[36:39]
v_mfma_f32_16x16x32_bf16 v[40:43],v[16:19],v[148:151],v[40:43]
s_waitcnt lgkmcnt(0)
v_mfma_f32_16x16x32_bf16 v[44:47],v[16:19],v[32:35],v[44:47]
ds_read_b128 v[28:31],v250 offset:4096
v_mfma_f32_16x16x32_bf16 v[36:39],v[20:23],v[128:131],v[36:39]
v_mfma_f32_16x16x32_bf16 v[40:43],v[20:23],v[152:155],v[40:43]
s_waitcnt lgkmcnt(0)
v_mfma_f32_16x16x32_bf16 v[44:47],v[20:23],v[28:31],v[44:47]
ds_read_b128 v[32:35],v250 offset:5120
v_mfma_f32_16x16x32_bf16 v[36:39],v[24:27],v[132:135],v[36:39]
v_mfma_f32_16x16x32_bf16 v[40:43],v[24:27],v[156:159],v[40:43]
s_waitcnt lgkmcnt(0)
v_mfma_f32_16x16x32_bf16 v[44:47],v[24:27],v[32:35],v[44:47]
s_nop 4
v_max_f32 v0,v36,v37
v_max3_f32 v0,v0,v38,v39
v_max_f32 v1,v40,v41
v_max3_f32 v1,v1,v42,v43
v_max_f32 v2,v44,v45
v_max3_f32 v2,v2,v46,v47
v_mov_b32 v3,v0
s_nop 1
v_permlane32_swap_b32 v0,v3
v_max_f32 v0,v0,v3
v_mov_b32 v3,v0
s_nop 1
v_permlane16_swap_b32 v0,v3
v_max_f32 v0,v0,v3
v_mov_b32 v3,v1
s_nop 1
v_permlane32_swap_b32 v1,v3
v_max_f32 v1,v1,v3
v_mov_b32 v3,v1
s_nop 1
v_permlane16_swap_b32 v1,v3
v_max_f32 v1,v1,v3
v_mov_b32 v3,v2
s_nop 1
v_permlane32_swap_b32 v2,v3
v_max_f32 v2,v2,v3
v_mov_b32 v3,v2
s_nop 1
v_permlane16_swap_b32 v2,v3
v_max_f32 v2,v2,v3
v_xor_b32 v160,0x80000000,v0
v_xor_b32 v164,0x80000000,v1
v_xor_b32 v161,0x80000000,v0
)A"
R"A(v_xor_b32 v165,0x80000000,v1
v_xor_b32 v162,0x80000000,v0
v_xor_b32 v166,0x80000000,v1
v_xor_b32 v163,0x80000000,v0
v_xor_b32 v167,0x80000000,v1
v_xor_b32 v240,0x80000000,v2
s_waitcnt lgkmcnt(0)
s_barrier
s_mov_b32 m0,s96
s_nop 0
buffer_load_dwordx4 v251,s[64:67],s94 offen nt lds
s_add_u32 m0,s96,896
s_nop 0
buffer_load_dwordx4 v251,s[64:67],s94 offen offset:128 nt lds
s_add_u32 m0,s96,1792
s_nop 0
buffer_load_dwordx4 v251,s[64:67],s94 offen offset:256 nt lds
s_add_u32 s94,s94,12288
s_add_u32 s89,s48,36864
s_add_u32 s96,s96,12288
s_cmp_eq_u32 s96,s89
s_cselect_b32 s96,s48,s96
W2_ATT:
v_mov_b32 v36,0
v_mov_b32 v37,0
v_mov_b32 v38,0
v_mov_b32 v39,0
v_mov_b32 v40,0
v_mov_b32 v41,0
v_mov_b32 v42,0
v_mov_b32 v43,0
v_mov_b32 v44,0
v_mov_b32 v45,0
v_mov_b32 v46,0
v_mov_b32 v47,0
v_mov_b32 v48,0
v_mov_b32 v49,0
v_mov_b32 v50,0
v_mov_b32 v51,0
v_mov_b32 v84,0
v_mov_b32 v85,0
v_mov_b32 v86,0
v_mov_b32 v87,0
v_mov_b32 v88,0
v_mov_b32 v89,0
v_mov_b32 v90,0
v_mov_b32 v91,0
v_mov_b32 v92,0
v_mov_b32 v93,0
v_mov_b32 v94,0
v_mov_b32 v95,0
v_mov_b32 v96,0
v_mov_b32 v97,0
v_mov_b32 v98,0
v_mov_b32 v99,0
v_mov_b32 v108,0
v_mov_b32 v109,0
v_mov_b32 v110,0
v_mov_b32 v111,0
v_mov_b32 v241,0
v_mov_b32 v242,0
v_mov_b32 v243,0
ds_read_b128 v[220:223],v244
ds_read_b128 v[224:227],v245
ds_read_b128 v[204:207],v244 offset:1024
ds_read_b128 v[208:211],v245 offset:1024
ds_read_b128 v[212:215],v244 offset:2048
ds_read_b128 v[216:219],v245 offset:2048
s_waitcnt lgkmcnt(5)
v_mfma_f32_16x16x32_bf16 v[168:171],v[220:223],v[112:115],v[160:163]
s_mov_b32 m0,s96
v_mfma_f32_16x16x32_bf16 v[172:175],v[220:223],v[136:139],v[164:167]
ds_read_b128 v[220:223],v244 offset:6144
buffer_load_dwordx4 v251,s[64:67],s94 offen nt lds
s_waitcnt lgkmcnt(5)
v_mfma_f32_16x16x32_bf16 v[168:171],v[224:227],v[116:119],v[168:171]
v_mfma_f32_16x16x32_bf16 v[172:175],v[224:227],v[140:143],v[172:175]
ds_read_b128 v[224:227],v245 offset:6144
s_waitcnt lgkmcnt(5)
v_mfma_f32_16x16x32_bf16 v[168:171],v[204:207],v[120:123],v[168:171]
s_add_u32 m0,s96,896
v_mfma_f32_16x16x32_bf16 v[172:175],v[204:207],v[144:147],v[172:175]
ds_read_b128 v[204:207],v244 offset:7168
buffer_load_dwordx4 v251,s[64:67],s94 offen offset:128 nt lds
s_waitcnt lgkmcnt(5)
v_mfma_f32_16x16x32_bf16 v[168:171],v[208:211],v[124:127],v[168:171]
v_mfma_f32_16x16x32_bf16 v[172:175],v[208:211],v[148:151],v[172:175]
ds_read_b128 v[208:211],v245 offset:7168
s_waitcnt lgkmcnt(5)
v_mfma_f32_16x16x32_bf16 v[168:171],v[212:215],v[128:131],v[168:171]
s_add_u32 m0,s96,1792
v_mfma_f32_16x16x32_bf16 v[172:175],v[212:215],v[152:155],v[172:175]
ds_read_b128 v[212:215],v244 offset:8192
buffer_load_dwordx4 v251,s[64:67],s94 offen offset:256 nt lds
s_waitcnt lgkmcnt(5)
v_mfma_f32_16x16x32_bf16 v[168:171],v[216:219],v[132:135],v[168:171]
v_mfma_f32_16x16x32_bf16 v[172:175],v[216:219],v[156:159],v[172:175]
ds_read_b128 v[216:219],v245 offset:8192
s_waitcnt lgkmcnt(5)
v_mfma_f32_16x16x32_bf16 v[176:179],v[220:223],v[112:115],v[160:163]
s_mov_b32 m0,s97
v_mfma_f32_16x16x32_bf16 v[180:183],v[220:223],v[136:139],v[164:167]
buffer_load_dwordx4 v252,s[68:71],s95 offen nt lds
s_waitcnt lgkmcnt(4)
v_mfma_f32_16x16x32_bf16 v[176:179],v[224:227],v[116:119],v[176:179]
v_mfma_f32_16x16x32_bf16 v[180:183],v[224:227],v[140:143],v[180:183]
v_exp_f32 v168,v168
s_mov_b32 s90,s98
s_add_u32 s98,s98,12288
s_cmp_eq_u32 s98,36864
s_cmov_b32 s98,0
s_sub_u32 s46,s98,s90
v_add_u32 v244,s46,v244
v_add_u32 v245,s46,v245
s_waitcnt lgkmcnt(3)
v_mfma_f32_16x16x32_bf16 v[176:179],v[204:207],v[120:123],v[176:179]
v_exp_f32 v169,v169
s_add_u32 m0,s97,896
v_mfma_f32_16x16x32_bf16 v[180:183],v[204:207],v[144:147],v[180:183]
v_add_f32 v241,v241,v168
v_exp_f32 v170,v170
buffer_load_dwordx4 v252,s[68:71],s95 offen offset:128 nt lds
s_waitcnt lgkmcnt(2)
v_mfma_f32_16x16x32_bf16 v[176:179],v[208:211],v[124:127],v[176:179]
v_add_f32 v241,v241,v169
v_exp_f32 v171,v171
v_cvt_pk_bf16_f32 v184,v168,v169
v_mfma_f32_16x16x32_bf16 v[180:183],v[208:211],v[148:151],v[180:183]
v_add_f32 v241,v241,v170
v_exp_f32 v172,v172
s_waitcnt lgkmcnt(1)
v_mfma_f32_16x16x32_bf16 v[176:179],v[212:215],v[128:131],v[176:179]
v_add_f32 v241,v241,v171
v_exp_f32 v173,v173
v_cvt_pk_bf16_f32 v185,v170,v171
s_add_u32 m0,s97,1792
v_mfma_f32_16x16x32_bf16 v[180:183],v[212:215],v[152:155],v[180:183]
v_add_f32 v242,v242,v172
v_exp_f32 v174,v174
buffer_load_dwordx4 v252,s[68:71],s95 offen offset:256 nt lds
s_waitcnt lgkmcnt(0)
v_mfma_f32_16x16x32_bf16 v[176:179],v[216:219],v[132:135],v[176:179]
v_add_f32 v242,v242,v173
v_exp_f32 v175,v175
v_cvt_pk_bf16_f32 v188,v172,v173
v_mfma_f32_16x16x32_bf16 v[180:183],v[216:219],v[156:159],v[180:183]
v_add_f32 v242,v242,v174
v_add_f32 v242,v242,v175
v_cvt_pk_bf16_f32 v189,v174,v175
s_add_u32 s94,s94,12288
s_add_u32 s89,s48,36864
s_add_u32 s96,s96,12288
s_cmp_eq_u32 s96,s89
s_cselect_b32 s96,s48,s96
s_add_u32 s95,s95,12288
s_add_u32 s89,s49,36864
s_add_u32 s97,s97,12288
s_cmp_eq_u32 s97,s89
s_cselect_b32 s97,s49,s97
s_waitcnt vmcnt(6) lgkmcnt(0)
s_barrier
ds_read_b128 v[220:223],v244
ds_read_b128 v[224:227],v245
ds_read_b128 v[204:207],v244 offset:1024
ds_read_b128 v[208:211],v245 offset:1024
ds_read_b128 v[212:215],v244 offset:2048
ds_read_b128 v[216:219],v245 offset:2048
ds_read_b128 v[200:203],v250 offset:6144
v_mov_b32 v236,v240
v_mov_b32 v237,v240
v_mov_b32 v238,v240
v_mov_b32 v239,v240
ds_read_b128 v[228:231],v250
ds_read_b128 v[232:235],v250 offset:1024
v_exp_f32 v176,v176
s_waitcnt lgkmcnt(8)
v_mfma_f32_16x16x32_bf16 v[168:171],v[220:223],v[112:115],v[160:163]
v_exp_f32 v177,v177
s_mov_b32 m0,s96
v_mfma_f32_16x16x32_bf16 v[172:175],v[220:223],v[136:139],v[164:167]
v_add_f32 v241,v241,v176
v_exp_f32 v178,v178
buffer_load_dwordx4 v251,s[64:67],s94 offen nt lds
s_waitcnt lgkmcnt(1)
v_mfma_f32_16x16x32_bf16 v[236:239],v[220:223],v[228:231],v[236:239]
ds_read_b128 v[220:223],v244 offset:6144
ds_read_b128 v[228:231],v250 offset:2048
v_add_f32 v241,v241,v177
v_exp_f32 v179,v179
v_cvt_pk_bf16_f32 v186,v176,v177
v_mfma_f32_16x16x32_bf16 v[168:171],v[224:227],v[116:119],v[168:171]
v_add_f32 v241,v241,v178
v_exp_f32 v180,v180
v_mfma_f32_16x16x32_bf16 v[172:175],v[224:227],v[140:143],v[172:175]
v_add_f32 v241,v241,v179
v_exp_f32 v181,v181
v_cvt_pk_bf16_f32 v187,v178,v179
s_add_u32 m0,s96,896
s_waitcnt lgkmcnt(2)
v_mfma_f32_16x16x32_bf16 v[236:239],v[224:227],v[232:235],v[236:239]
ds_read_b128 v[224:227],v245 offset:6144
ds_read_b128 v[232:235],v250 offset:3072
v_add_f32 v242,v242,v180
v_exp_f32 v182,v182
buffer_load_dwordx4 v251,s[64:67],s94 offen offset:128 nt lds
v_mfma_f32_16x16x32_bf16 v[168:171],v[204:207],v[120:123],v[168:171]
v_add_f32 v242,v242,v181
v_exp_f32 v183,v183
v_cvt_pk_bf16_f32 v190,v180,v181
v_mfma_f32_16x16x32_bf16 v[172:175],v[204:207],v[144:147],v[172:175]
v_add_f32 v242,v242,v182
s_waitcnt lgkmcnt(2)
v_mfma_f32_16x16x32_bf16 v[236:239],v[204:207],v[228:231],v[236:239]
ds_read_b128 v[204:207],v244 offset:7168
ds_read_b128 v[228:231],v250 offset:4096
v_add_f32 v242,v242,v183
v_cvt_pk_bf16_f32 v191,v182,v183
s_add_u32 m0,s96,1792
v_mfma_f32_16x16x32_bf16 v[168:171],v[208:211],v[124:127],v[168:171]
v_dot2c_f32_bf16 v243,s55,v200
buffer_load_dwordx4 v251,s[64:67],s94 offen offset:256 nt lds
v_mfma_f32_16x16x32_bf16 v[172:175],v[208:211],v[148:151],v[172:175]
s_waitcnt lgkmcnt(2)
v_mfma_f32_16x16x32_bf16 v[236:239],v[208:211],v[232:235],v[236:239]
ds_read_b128 v[208:211],v245 offset:7168
ds_read_b128 v[232:235],v250 offset:5120
v_mfma_f32_16x16x32_bf16 v[168:171],v[212:215],v[128:131],v[168:171]
v_dot2c_f32_bf16 v243,s55,v201
s_mov_b32 m0,s97
v_mfma_f32_16x16x32_bf16 v[172:175],v[212:215],v[152:155],v[172:175]
buffer_load_dwordx4 v252,s[68:71],s95 offen nt lds
s_waitcnt lgkmcnt(2)
v_mfma_f32_16x16x32_bf16 v[236:239],v[212:215],v[228:231],v[236:239]
ds_read_b128 v[212:215],v244 offset:8192
v_mfma_f32_16x16x32_bf16 v[168:171],v[216:219],v[132:135],v[168:171]
v_dot2c_f32_bf16 v243,s55,v202
v_mfma_f32_16x16x32_bf16 v[172:175],v[216:219],v[156:159],v[172:175]
s_add_u32 m0,s97,896
s_waitcnt lgkmcnt(1)
v_mfma_f32_16x16x32_bf16 v[236:239],v[216:219],v[232:235],v[236:239]
ds_read_b128 v[216:219],v245 offset:8192
buffer_load_dwordx4 v252,s[68:71],s95 offen offset:128 nt lds
v_mfma_f32_16x16x32_bf16 v[176:179],v[220:223],v[112:115],v[160:163]
v_dot2c_f32_bf16 v243,s55,v203
v_mfma_f32_16x16x32_bf16 v[180:183],v[220:223],v[136:139],v[164:167]
ds_read_b64_tr_b16 v[220:221],v246
ds_read_b64_tr_b16 v[222:223],v246 offset:6144
v_mfma_f32_16x16x32_bf16 v[176:179],v[224:227],v[116:119],v[176:179]
s_add_u32 m0,s97,1792
v_mfma_f32_16x16x32_bf16 v[180:183],v[224:227],v[140:143],v[180:183]
ds_read_b64_tr_b16 v[224:225],v247
ds_read_b64_tr_b16 v[226:227],v247 offset:6144
v_exp_f32 v236,v236
v_exp_f32 v168,v168
buffer_load_dwordx4 v252,s[68:71],s95 offen offset:256 nt lds
s_mov_b32 s90,s98
s_add_u32 s98,s98,12288
s_cmp_eq_u32 s98,36864
s_cmov_b32 s98,0
s_sub_u32 s46,s98,s90
v_add_u32 v244,s46,v244
v_add_u32 v245,s46,v245
v_mfma_f32_16x16x32_bf16 v[176:179],v[204:207],v[120:123],v[176:179]
v_exp_f32 v237,v237
v_exp_f32 v169,v169
v_mfma_f32_16x16x32_bf16 v[180:183],v[204:207],v[144:147],v[180:183]
ds_read_b64_tr_b16 v[204:205],v248
ds_read_b64_tr_b16 v[206:207],v248 offset:6144
v_exp_f32 v238,v238
v_add_f32 v241,v241,v168
v_exp_f32 v170,v170
v_mfma_f32_16x16x32_bf16 v[176:179],v[208:211],v[124:127],v[176:179]
v_exp_f32 v239,v239
v_add_f32 v241,v241,v169
v_exp_f32 v171,v171
v_cvt_pk_bf16_f32 v192,v168,v169
v_mfma_f32_16x16x32_bf16 v[180:183],v[208:211],v[148:151],v[180:183]
ds_read_b64_tr_b16 v[208:209],v249
ds_read_b64_tr_b16 v[210:211],v249 offset:6144
v_add_f32 v241,v241,v170
v_exp_f32 v172,v172
s_waitcnt lgkmcnt(9)
v_mfma_f32_16x16x32_bf16 v[176:179],v[212:215],v[128:131],v[176:179]
v_cvt_pk_bf16_f32 v0,v236,v237
v_cvt_pk_bf16_f32 v1,v238,v239
v_add_f32 v241,v241,v171
v_exp_f32 v173,v173
v_cvt_pk_bf16_f32 v193,v170,v171
s_add_u32 s94,s94,12288
s_add_u32 s89,s48,36864
s_add_u32 s96,s96,12288
s_cmp_eq_u32 s96,s89
s_cselect_b32 s96,s48,s96
v_mfma_f32_16x16x32_bf16 v[180:183],v[212:215],v[152:155],v[180:183]
ds_read_b64_tr_b16 v[212:213],v246 offset:1024
ds_read_b64_tr_b16 v[214:215],v246 offset:7168
ds_write_b64 v250,v[0:1] offset:7168
v_add_f32 v242,v242,v172
v_exp_f32 v174,v174
s_waitcnt lgkmcnt(11)
v_mfma_f32_16x16x32_bf16 v[176:179],v[216:219],v[132:135],v[176:179]
v_add_f32 v242,v242,v173
v_exp_f32 v175,v175
v_cvt_pk_bf16_f32 v196,v172,v173
s_add_u32 s95,s95,12288
s_add_u32 s89,s49,36864
s_add_u32 s97,s97,12288
s_cmp_eq_u32 s97,s89
s_cselect_b32 s97,s49,s97
v_mfma_f32_16x16x32_bf16 v[180:183],v[216:219],v[156:159],v[180:183]
ds_read_b64_tr_b16 v[216:217],v247 offset:1024
ds_read_b64_tr_b16 v[218:219],v247 offset:7168
v_add_f32 v242,v242,v174
s_waitcnt lgkmcnt(11)
v_mfma_f32_16x16x32_bf16 v[4:7],v[220:223],v[184:187],0
v_add_f32 v242,v242,v175
v_cvt_pk_bf16_f32 v197,v174,v175
v_mfma_f32_16x16x32_bf16 v[52:55],v[220:223],v[188:191],0
v_mfma_f32_16x16x32_bf16 v[100:103],v[220:223],v[200:203],0
ds_read_b64_tr_b16 v[220:221],v248 offset:1024
ds_read_b64_tr_b16 v[222:223],v248 offset:7168
s_waitcnt lgkmcnt(11)
v_mfma_f32_16x16x32_bf16 v[8:11],v[224:227],v[184:187],0
v_mfma_f32_16x16x32_bf16 v[56:59],v[224:227],v[188:191],0
ds_read_b64_tr_b16 v[224:225],v249 offset:1024
ds_read_b64_tr_b16 v[226:227],v249 offset:7168
s_waitcnt lgkmcnt(11)
v_mfma_f32_16x16x32_bf16 v[12:15],v[204:207],v[184:187],0
v_mfma_f32_16x16x32_bf16 v[60:63],v[204:207],v[188:191],0
ds_read_b64_tr_b16 v[204:205],v246 offset:2048
ds_read_b64_tr_b16 v[206:207],v246 offset:8192
s_waitcnt lgkmcnt(11)
v_mfma_f32_16x16x32_bf16 v[16:19],v[208:211],v[184:187],0
v_mfma_f32_16x16x32_bf16 v[64:67],v[208:211],v[188:191],0
ds_read_b64_tr_b16 v[208:209],v247 offset:2048
ds_read_b64_tr_b16 v[210:211],v247 offset:8192
s_waitcnt lgkmcnt(11)
v_mfma_f32_16x16x32_bf16 v[20:23],v[212:215],v[184:187],0
v_mfma_f32_16x16x32_bf16 v[68:71],v[212:215],v[188:191],0
v_mfma_f32_16x16x32_bf16 v[104:107],v[212:215],v[200:203],0
ds_read_b64_tr_b16 v[212:213],v248 offset:2048
ds_read_b64_tr_b16 v[214:215],v248 offset:8192
s_waitcnt lgkmcnt(10)
v_mfma_f32_16x16x32_bf16 v[24:27],v[216:219],v[184:187],0
v_mfma_f32_16x16x32_bf16 v[72:75],v[216:219],v[188:191],0
ds_read_b64_tr_b16 v[216:217],v249 offset:2048
ds_read_b64_tr_b16 v[218:219],v249 offset:8192
s_waitcnt lgkmcnt(10)
v_mfma_f32_16x16x32_bf16 v[28:31],v[220:223],v[184:187],0
s_mov_b32 s90,s99
s_add_u32 s99,s99,12288
s_cmp_eq_u32 s99,36864
s_cmov_b32 s99,0
s_sub_u32 s46,s99,s90
v_add_u32 v246,s46,v246
v_add_u32 v247,s46,v247
v_add_u32 v248,s46,v248
v_add_u32 v249,s46,v249
v_mfma_f32_16x16x32_bf16 v[76:79],v[220:223],v[188:191],0
s_waitcnt lgkmcnt(8)
v_mfma_f32_16x16x32_bf16 v[32:35],v[224:227],v[184:187],0
v_mfma_f32_16x16x32_bf16 v[80:83],v[224:227],v[188:191],0
s_mov_b32 s45,6
W2_LOOP:
s_waitcnt vmcnt(6) lgkmcnt(0)
s_barrier
ds_read_b128 v[220:223],v244
ds_read_b128 v[224:227],v245
v_mfma_f32_16x16x32_bf16 v[36:39],v[204:207],v[184:187],v[36:39]
v_exp_f32 v176,v176
s_mov_b32 m0,s96
v_mfma_f32_16x16x32_bf16 v[84:87],v[204:207],v[188:191],v[84:87]
v_exp_f32 v177,v177
buffer_load_dwordx4 v251,s[64:67],s94 offen nt lds
v_mfma_f32_16x16x32_bf16 v[108:111],v[204:207],v[200:203],v[108:111]
ds_read_b128 v[204:207],v244 offset:1024
v_add_f32 v241,v241,v176
v_exp_f32 v178,v178
v_mfma_f32_16x16x32_bf16 v[40:43],v[208:211],v[184:187],v[40:43]
v_add_f32 v241,v241,v177
v_exp_f32 v179,v179
v_cvt_pk_bf16_f32 v194,v176,v177
v_mfma_f32_16x16x32_bf16 v[88:91],v[208:211],v[188:191],v[88:91]
ds_read_b128 v[208:211],v245 offset:1024
ds_read_b128 v[200:203],v250 offset:7168
v_add_f32 v241,v241,v178
v_exp_f32 v180,v180
s_add_u32 m0,s96,896
v_mfma_f32_16x16x32_bf16 v[44:47],v[212:215],v[184:187],v[44:47]
v_add_f32 v241,v241,v179
v_exp_f32 v181,v181
v_cvt_pk_bf16_f32 v195,v178,v179
buffer_load_dwordx4 v251,s[64:67],s94 offen offset:128 nt lds
v_mfma_f32_16x16x32_bf16 v[92:95],v[212:215],v[188:191],v[92:95]
ds_read_b128 v[212:215],v244 offset:2048
v_add_f32 v242,v242,v180
v_exp_f32 v182,v182
v_mfma_f32_16x16x32_bf16 v[48:51],v[216:219],v[184:187],v[48:51]
v_add_f32 v242,v242,v181
v_exp_f32 v183,v183
)A"
R"A(v_cvt_pk_bf16_f32 v198,v180,v181
v_mfma_f32_16x16x32_bf16 v[96:99],v[216:219],v[188:191],v[96:99]
ds_read_b128 v[216:219],v245 offset:2048
v_add_f32 v242,v242,v182
s_add_u32 m0,s96,1792
s_waitcnt lgkmcnt(6)
v_mfma_f32_16x16x32_bf16 v[168:171],v[220:223],v[112:115],v[160:163]
v_add_f32 v242,v242,v183
v_cvt_pk_bf16_f32 v199,v182,v183
buffer_load_dwordx4 v251,s[64:67],s94 offen offset:256 nt lds
v_mfma_f32_16x16x32_bf16 v[172:175],v[220:223],v[136:139],v[164:167]
ds_read_b128 v[220:223],v244 offset:6144
s_waitcnt lgkmcnt(6)
v_mfma_f32_16x16x32_bf16 v[168:171],v[224:227],v[116:119],v[168:171]
v_mfma_f32_16x16x32_bf16 v[172:175],v[224:227],v[140:143],v[172:175]
ds_read_b128 v[224:227],v245 offset:6144
s_mov_b32 m0,s97
s_waitcnt lgkmcnt(6)
v_mfma_f32_16x16x32_bf16 v[168:171],v[204:207],v[120:123],v[168:171]
buffer_load_dwordx4 v252,s[68:71],s95 offen nt lds
v_mfma_f32_16x16x32_bf16 v[172:175],v[204:207],v[144:147],v[172:175]
ds_read_b128 v[204:207],v244 offset:7168
s_waitcnt lgkmcnt(6)
v_mfma_f32_16x16x32_bf16 v[168:171],v[208:211],v[124:127],v[168:171]
s_waitcnt lgkmcnt(5)
v_dot2c_f32_bf16 v243,s55,v200
v_mfma_f32_16x16x32_bf16 v[172:175],v[208:211],v[148:151],v[172:175]
ds_read_b128 v[208:211],v245 offset:7168
s_add_u32 m0,s97,896
s_waitcnt lgkmcnt(5)
v_mfma_f32_16x16x32_bf16 v[168:171],v[212:215],v[128:131],v[168:171]
v_dot2c_f32_bf16 v243,s55,v201
buffer_load_dwordx4 v252,s[68:71],s95 offen offset:128 nt lds
v_mfma_f32_16x16x32_bf16 v[172:175],v[212:215],v[152:155],v[172:175]
ds_read_b128 v[212:215],v244 offset:8192
s_waitcnt lgkmcnt(5)
v_mfma_f32_16x16x32_bf16 v[168:171],v[216:219],v[132:135],v[168:171]
v_dot2c_f32_bf16 v243,s55,v202
v_mfma_f32_16x16x32_bf16 v[172:175],v[216:219],v[156:159],v[172:175]
ds_read_b128 v[216:219],v245 offset:8192
s_add_u32 m0,s97,1792
s_waitcnt lgkmcnt(5)
v_mfma_f32_16x16x32_bf16 v[176:179],v[220:223],v[112:115],v[160:163]
v_dot2c_f32_bf16 v243,s55,v203
buffer_load_dwordx4 v252,s[68:71],s95 offen offset:256 nt lds
v_mfma_f32_16x16x32_bf16 v[180:183],v[220:223],v[136:139],v[164:167]
ds_read_b64_tr_b16 v[220:221],v246
ds_read_b64_tr_b16 v[222:223],v246 offset:6144
s_waitcnt lgkmcnt(6)
v_mfma_f32_16x16x32_bf16 v[176:179],v[224:227],v[116:119],v[176:179]
v_mfma_f32_16x16x32_bf16 v[180:183],v[224:227],v[140:143],v[180:183]
ds_read_b64_tr_b16 v[224:225],v247
ds_read_b64_tr_b16 v[226:227],v247 offset:6144
v_exp_f32 v168,v168
s_mov_b32 s90,s98
s_add_u32 s98,s98,12288
s_cmp_eq_u32 s98,36864
s_cmov_b32 s98,0
s_sub_u32 s46,s98,s90
v_add_u32 v244,s46,v244
v_add_u32 v245,s46,v245
s_waitcnt lgkmcnt(7)
v_mfma_f32_16x16x32_bf16 v[176:179],v[204:207],v[120:123],v[176:179]
v_exp_f32 v169,v169
v_mfma_f32_16x16x32_bf16 v[180:183],v[204:207],v[144:147],v[180:183]
ds_read_b64_tr_b16 v[204:205],v248
ds_read_b64_tr_b16 v[206:207],v248 offset:6144
v_add_f32 v241,v241,v168
v_exp_f32 v170,v170
s_add_u32 s94,s94,12288
s_add_u32 s89,s48,36864
s_add_u32 s96,s96,12288
s_cmp_eq_u32 s96,s89
s_cselect_b32 s96,s48,s96
s_waitcnt lgkmcnt(8)
v_mfma_f32_16x16x32_bf16 v[176:179],v[208:211],v[124:127],v[176:179]
v_add_f32 v241,v241,v169
v_exp_f32 v171,v171
v_cvt_pk_bf16_f32 v184,v168,v169
v_mfma_f32_16x16x32_bf16 v[180:183],v[208:211],v[148:151],v[180:183]
ds_read_b64_tr_b16 v[208:209],v249
ds_read_b64_tr_b16 v[210:211],v249 offset:6144
v_add_f32 v241,v241,v170
v_exp_f32 v172,v172
s_add_u32 s95,s95,12288
s_add_u32 s89,s49,36864
s_add_u32 s97,s97,12288
s_cmp_eq_u32 s97,s89
s_cselect_b32 s97,s49,s97
s_waitcnt lgkmcnt(9)
v_mfma_f32_16x16x32_bf16 v[176:179],v[212:215],v[128:131],v[176:179]
v_add_f32 v241,v241,v171
v_exp_f32 v173,v173
v_cvt_pk_bf16_f32 v185,v170,v171
v_mfma_f32_16x16x32_bf16 v[180:183],v[212:215],v[152:155],v[180:183]
ds_read_b64_tr_b16 v[212:213],v246 offset:1024
ds_read_b64_tr_b16 v[214:215],v246 offset:7168
v_add_f32 v242,v242,v172
v_exp_f32 v174,v174
s_sub_u32 s45,s45,1
s_waitcnt lgkmcnt(10)
v_mfma_f32_16x16x32_bf16 v[176:179],v[216:219],v[132:135],v[176:179]
v_add_f32 v242,v242,v173
v_exp_f32 v175,v175
v_cvt_pk_bf16_f32 v188,v172,v173
v_mfma_f32_16x16x32_bf16 v[180:183],v[216:219],v[156:159],v[180:183]
ds_read_b64_tr_b16 v[216:217],v247 offset:1024
ds_read_b64_tr_b16 v[218:219],v247 offset:7168
v_add_f32 v242,v242,v174
s_waitcnt lgkmcnt(10)
v_mfma_f32_16x16x32_bf16 v[4:7],v[220:223],v[192:195],v[4:7]
v_add_f32 v242,v242,v175
v_cvt_pk_bf16_f32 v189,v174,v175
v_mfma_f32_16x16x32_bf16 v[52:55],v[220:223],v[196:199],v[52:55]
v_mfma_f32_16x16x32_bf16 v[100:103],v[220:223],v[200:203],v[100:103]
ds_read_b64_tr_b16 v[220:221],v248 offset:1024
ds_read_b64_tr_b16 v[222:223],v248 offset:7168
s_waitcnt lgkmcnt(10)
v_mfma_f32_16x16x32_bf16 v[8:11],v[224:227],v[192:195],v[8:11]
v_mfma_f32_16x16x32_bf16 v[56:59],v[224:227],v[196:199],v[56:59]
ds_read_b64_tr_b16 v[224:225],v249 offset:1024
ds_read_b64_tr_b16 v[226:227],v249 offset:7168
s_waitcnt lgkmcnt(10)
v_mfma_f32_16x16x32_bf16 v[12:15],v[204:207],v[192:195],v[12:15]
v_mfma_f32_16x16x32_bf16 v[60:63],v[204:207],v[196:199],v[60:63]
ds_read_b64_tr_b16 v[204:205],v246 offset:2048
ds_read_b64_tr_b16 v[206:207],v246 offset:8192
s_waitcnt lgkmcnt(10)
v_mfma_f32_16x16x32_bf16 v[16:19],v[208:211],v[192:195],v[16:19]
v_mfma_f32_16x16x32_bf16 v[64:67],v[208:211],v[196:199],v[64:67]
ds_read_b64_tr_b16 v[208:209],v247 offset:2048
ds_read_b64_tr_b16 v[210:211],v247 offset:8192
s_waitcnt lgkmcnt(10)
v_mfma_f32_16x16x32_bf16 v[20:23],v[212:215],v[192:195],v[20:23]
v_mfma_f32_16x16x32_bf16 v[68:71],v[212:215],v[196:199],v[68:71]
v_mfma_f32_16x16x32_bf16 v[104:107],v[212:215],v[200:203],v[104:107]
ds_read_b64_tr_b16 v[212:213],v248 offset:2048
ds_read_b64_tr_b16 v[214:215],v248 offset:8192
s_waitcnt lgkmcnt(10)
v_mfma_f32_16x16x32_bf16 v[24:27],v[216:219],v[192:195],v[24:27]
v_mfma_f32_16x16x32_bf16 v[72:75],v[216:219],v[196:199],v[72:75]
ds_read_b64_tr_b16 v[216:217],v249 offset:2048
ds_read_b64_tr_b16 v[218:219],v249 offset:8192
s_waitcnt lgkmcnt(10)
v_mfma_f32_16x16x32_bf16 v[28:31],v[220:223],v[192:195],v[28:31]
s_mov_b32 s90,s99
s_add_u32 s99,s99,12288
s_cmp_eq_u32 s99,36864
s_cmov_b32 s99,0
s_sub_u32 s46,s99,s90
v_add_u32 v246,s46,v246
v_add_u32 v247,s46,v247
v_add_u32 v248,s46,v248
v_add_u32 v249,s46,v249
v_mfma_f32_16x16x32_bf16 v[76:79],v[220:223],v[196:199],v[76:79]
s_waitcnt lgkmcnt(8)
v_mfma_f32_16x16x32_bf16 v[32:35],v[224:227],v[192:195],v[32:35]
v_mfma_f32_16x16x32_bf16 v[80:83],v[224:227],v[196:199],v[80:83]
s_waitcnt vmcnt(6) lgkmcnt(0)
s_barrier
ds_read_b128 v[220:223],v244
ds_read_b128 v[224:227],v245
v_mfma_f32_16x16x32_bf16 v[36:39],v[204:207],v[192:195],v[36:39]
v_exp_f32 v176,v176
s_mov_b32 m0,s96
v_mfma_f32_16x16x32_bf16 v[84:87],v[204:207],v[196:199],v[84:87]
v_exp_f32 v177,v177
buffer_load_dwordx4 v251,s[64:67],s94 offen nt lds
v_mfma_f32_16x16x32_bf16 v[108:111],v[204:207],v[200:203],v[108:111]
ds_read_b128 v[204:207],v244 offset:1024
ds_read_b128 v[228:231],v250
ds_read_b128 v[232:235],v250 offset:1024
v_add_f32 v241,v241,v176
v_exp_f32 v178,v178
v_mfma_f32_16x16x32_bf16 v[40:43],v[208:211],v[192:195],v[40:43]
v_add_f32 v241,v241,v177
v_exp_f32 v179,v179
v_cvt_pk_bf16_f32 v186,v176,v177
v_mfma_f32_16x16x32_bf16 v[88:91],v[208:211],v[196:199],v[88:91]
ds_read_b128 v[208:211],v245 offset:1024
ds_read_b128 v[200:203],v250 offset:6144
v_add_f32 v241,v241,v178
v_exp_f32 v180,v180
s_add_u32 m0,s96,896
v_mfma_f32_16x16x32_bf16 v[44:47],v[212:215],v[192:195],v[44:47]
v_mov_b32 v236,v240
v_mov_b32 v237,v240
v_mov_b32 v238,v240
v_mov_b32 v239,v240
v_add_f32 v241,v241,v179
v_exp_f32 v181,v181
v_cvt_pk_bf16_f32 v187,v178,v179
buffer_load_dwordx4 v251,s[64:67],s94 offen offset:128 nt lds
v_mfma_f32_16x16x32_bf16 v[92:95],v[212:215],v[196:199],v[92:95]
ds_read_b128 v[212:215],v244 offset:2048
v_add_f32 v242,v242,v180
v_exp_f32 v182,v182
v_mfma_f32_16x16x32_bf16 v[48:51],v[216:219],v[192:195],v[48:51]
v_add_f32 v242,v242,v181
v_exp_f32 v183,v183
v_cvt_pk_bf16_f32 v190,v180,v181
v_mfma_f32_16x16x32_bf16 v[96:99],v[216:219],v[196:199],v[96:99]
ds_read_b128 v[216:219],v245 offset:2048
v_add_f32 v242,v242,v182
s_add_u32 m0,s96,1792
s_waitcnt lgkmcnt(8)
v_mfma_f32_16x16x32_bf16 v[168:171],v[220:223],v[112:115],v[160:163]
v_add_f32 v242,v242,v183
v_cvt_pk_bf16_f32 v191,v182,v183
buffer_load_dwordx4 v251,s[64:67],s94 offen offset:256 nt lds
v_mfma_f32_16x16x32_bf16 v[172:175],v[220:223],v[136:139],v[164:167]
s_waitcnt lgkmcnt(5)
v_mfma_f32_16x16x32_bf16 v[236:239],v[220:223],v[228:231],v[236:239]
ds_read_b128 v[220:223],v244 offset:6144
ds_read_b128 v[228:231],v250 offset:2048
v_mfma_f32_16x16x32_bf16 v[168:171],v[224:227],v[116:119],v[168:171]
s_mov_b32 m0,s97
v_mfma_f32_16x16x32_bf16 v[172:175],v[224:227],v[140:143],v[172:175]
buffer_load_dwordx4 v252,s[68:71],s95 offen nt lds
s_waitcnt lgkmcnt(6)
v_mfma_f32_16x16x32_bf16 v[236:239],v[224:227],v[232:235],v[236:239]
ds_read_b128 v[224:227],v245 offset:6144
ds_read_b128 v[232:235],v250 offset:3072
v_mfma_f32_16x16x32_bf16 v[168:171],v[204:207],v[120:123],v[168:171]
v_mfma_f32_16x16x32_bf16 v[172:175],v[204:207],v[144:147],v[172:175]
s_add_u32 m0,s97,896
s_waitcnt lgkmcnt(2)
v_mfma_f32_16x16x32_bf16 v[236:239],v[204:207],v[228:231],v[236:239]
ds_read_b128 v[204:207],v244 offset:7168
ds_read_b128 v[228:231],v250 offset:4096
buffer_load_dwordx4 v252,s[68:71],s95 offen offset:128 nt lds
v_mfma_f32_16x16x32_bf16 v[168:171],v[208:211],v[124:127],v[168:171]
v_dot2c_f32_bf16 v243,s55,v200
v_mfma_f32_16x16x32_bf16 v[172:175],v[208:211],v[148:151],v[172:175]
s_waitcnt lgkmcnt(2)
v_mfma_f32_16x16x32_bf16 v[236:239],v[208:211],v[232:235],v[236:239]
ds_read_b128 v[208:211],v245 offset:7168
ds_read_b128 v[232:235],v250 offset:5120
s_add_u32 m0,s97,1792
v_mfma_f32_16x16x32_bf16 v[168:171],v[212:215],v[128:131],v[168:171]
v_dot2c_f32_bf16 v243,s55,v201
buffer_load_dwordx4 v252,s[68:71],s95 offen offset:256 nt lds
v_mfma_f32_16x16x32_bf16 v[172:175],v[212:215],v[152:155],v[172:175]
s_waitcnt lgkmcnt(2)
v_mfma_f32_16x16x32_bf16 v[236:239],v[212:215],v[228:231],v[236:239]
ds_read_b128 v[212:215],v244 offset:8192
v_mfma_f32_16x16x32_bf16 v[168:171],v[216:219],v[132:135],v[168:171]
v_dot2c_f32_bf16 v243,s55,v202
v_mfma_f32_16x16x32_bf16 v[172:175],v[216:219],v[156:159],v[172:175]
s_waitcnt lgkmcnt(1)
v_mfma_f32_16x16x32_bf16 v[236:239],v[216:219],v[232:235],v[236:239]
ds_read_b128 v[216:219],v245 offset:8192
s_add_u32 s94,s94,12288
s_add_u32 s89,s48,36864
s_add_u32 s96,s96,12288
s_cmp_eq_u32 s96,s89
s_cselect_b32 s96,s48,s96
v_mfma_f32_16x16x32_bf16 v[176:179],v[220:223],v[112:115],v[160:163]
v_dot2c_f32_bf16 v243,s55,v203
v_mfma_f32_16x16x32_bf16 v[180:183],v[220:223],v[136:139],v[164:167]
ds_read_b64_tr_b16 v[220:221],v246
ds_read_b64_tr_b16 v[222:223],v246 offset:6144
s_add_u32 s95,s95,12288
s_add_u32 s89,s49,36864
s_add_u32 s97,s97,12288
s_cmp_eq_u32 s97,s89
s_cselect_b32 s97,s49,s97
v_mfma_f32_16x16x32_bf16 v[176:179],v[224:227],v[116:119],v[176:179]
v_mfma_f32_16x16x32_bf16 v[180:183],v[224:227],v[140:143],v[180:183]
ds_read_b64_tr_b16 v[224:225],v247
ds_read_b64_tr_b16 v[226:227],v247 offset:6144
v_exp_f32 v236,v236
v_exp_f32 v168,v168
s_mov_b32 s90,s98
s_add_u32 s98,s98,12288
s_cmp_eq_u32 s98,36864
s_cmov_b32 s98,0
s_sub_u32 s46,s98,s90
v_add_u32 v244,s46,v244
v_add_u32 v245,s46,v245
v_mfma_f32_16x16x32_bf16 v[176:179],v[204:207],v[120:123],v[176:179]
v_exp_f32 v237,v237
v_exp_f32 v169,v169
v_mfma_f32_16x16x32_bf16 v[180:183],v[204:207],v[144:147],v[180:183]
ds_read_b64_tr_b16 v[204:205],v248
ds_read_b64_tr_b16 v[206:207],v248 offset:6144
v_exp_f32 v238,v238
v_add_f32 v241,v241,v168
v_exp_f32 v170,v170
v_mfma_f32_16x16x32_bf16 v[176:179],v[208:211],v[124:127],v[176:179]
v_exp_f32 v239,v239
v_add_f32 v241,v241,v169
v_exp_f32 v171,v171
v_cvt_pk_bf16_f32 v192,v168,v169
v_mfma_f32_16x16x32_bf16 v[180:183],v[208:211],v[148:151],v[180:183]
ds_read_b64_tr_b16 v[208:209],v249
ds_read_b64_tr_b16 v[210:211],v249 offset:6144
v_add_f32 v241,v241,v170
v_exp_f32 v172,v172
s_waitcnt lgkmcnt(9)
v_mfma_f32_16x16x32_bf16 v[176:179],v[212:215],v[128:131],v[176:179]
v_cvt_pk_bf16_f32 v0,v236,v237
v_cvt_pk_bf16_f32 v1,v238,v239
v_add_f32 v241,v241,v171
v_exp_f32 v173,v173
v_cvt_pk_bf16_f32 v193,v170,v171
v_mfma_f32_16x16x32_bf16 v[180:183],v[212:215],v[152:155],v[180:183]
ds_read_b64_tr_b16 v[212:213],v246 offset:1024
ds_read_b64_tr_b16 v[214:215],v246 offset:7168
ds_write_b64 v250,v[0:1] offset:7168
v_add_f32 v242,v242,v172
v_exp_f32 v174,v174
s_waitcnt lgkmcnt(11)
v_mfma_f32_16x16x32_bf16 v[176:179],v[216:219],v[132:135],v[176:179]
v_add_f32 v242,v242,v173
v_exp_f32 v175,v175
v_cvt_pk_bf16_f32 v196,v172,v173
v_mfma_f32_16x16x32_bf16 v[180:183],v[216:219],v[156:159],v[180:183]
ds_read_b64_tr_b16 v[216:217],v247 offset:1024
ds_read_b64_tr_b16 v[218:219],v247 offset:7168
v_add_f32 v242,v242,v174
s_waitcnt lgkmcnt(11)
v_mfma_f32_16x16x32_bf16 v[4:7],v[220:223],v[184:187],v[4:7]
v_add_f32 v242,v242,v175
v_cvt_pk_bf16_f32 v197,v174,v175
v_mfma_f32_16x16x32_bf16 v[52:55],v[220:223],v[188:191],v[52:55]
v_mfma_f32_16x16x32_bf16 v[100:103],v[220:223],v[200:203],v[100:103]
ds_read_b64_tr_b16 v[220:221],v248 offset:1024
ds_read_b64_tr_b16 v[222:223],v248 offset:7168
s_waitcnt lgkmcnt(11)
v_mfma_f32_16x16x32_bf16 v[8:11],v[224:227],v[184:187],v[8:11]
v_mfma_f32_16x16x32_bf16 v[56:59],v[224:227],v[188:191],v[56:59]
ds_read_b64_tr_b16 v[224:225],v249 offset:1024
ds_read_b64_tr_b16 v[226:227],v249 offset:7168
s_waitcnt lgkmcnt(11)
v_mfma_f32_16x16x32_bf16 v[12:15],v[204:207],v[184:187],v[12:15]
v_mfma_f32_16x16x32_bf16 v[60:63],v[204:207],v[188:191],v[60:63]
ds_read_b64_tr_b16 v[204:205],v246 offset:2048
ds_read_b64_tr_b16 v[206:207],v246 offset:8192
s_waitcnt lgkmcnt(11)
v_mfma_f32_16x16x32_bf16 v[16:19],v[208:211],v[184:187],v[16:19]
v_mfma_f32_16x16x32_bf16 v[64:67],v[208:211],v[188:191],v[64:67]
ds_read_b64_tr_b16 v[208:209],v247 offset:2048
ds_read_b64_tr_b16 v[210:211],v247 offset:8192
s_waitcnt lgkmcnt(11)
v_mfma_f32_16x16x32_bf16 v[20:23],v[212:215],v[184:187],v[20:23]
v_mfma_f32_16x16x32_bf16 v[68:71],v[212:215],v[188:191],v[68:71]
v_mfma_f32_16x16x32_bf16 v[104:107],v[212:215],v[200:203],v[104:107]
ds_read_b64_tr_b16 v[212:213],v248 offset:2048
ds_read_b64_tr_b16 v[214:215],v248 offset:8192
s_waitcnt lgkmcnt(10)
v_mfma_f32_16x16x32_bf16 v[24:27],v[216:219],v[184:187],v[24:27]
v_mfma_f32_16x16x32_bf16 v[72:75],v[216:219],v[188:191],v[72:75]
ds_read_b64_tr_b16 v[216:217],v249 offset:2048
ds_read_b64_tr_b16 v[218:219],v249 offset:8192
s_waitcnt lgkmcnt(10)
v_mfma_f32_16x16x32_bf16 v[28:31],v[220:223],v[184:187],v[28:31]
s_mov_b32 s90,s99
s_add_u32 s99,s99,12288
s_cmp_eq_u32 s99,36864
s_cmov_b32 s99,0
s_sub_u32 s46,s99,s90
v_add_u32 v246,s46,v246
v_add_u32 v247,s46,v247
v_add_u32 v248,s46,v248
v_add_u32 v249,s46,v249
v_mfma_f32_16x16x32_bf16 v[76:79],v[220:223],v[188:191],v[76:79]
s_waitcnt lgkmcnt(8)
v_mfma_f32_16x16x32_bf16 v[32:35],v[224:227],v[184:187],v[32:35]
v_mfma_f32_16x16x32_bf16 v[80:83],v[224:227],v[188:191],v[80:83]
s_cmp_lg_u32 s45,0
s_cbranch_scc1 W2_LOOP
s_cmp_eq_u32 s47,0
s_cselect_b32 s94,5,3
s_mul_i32 s94,s94,393216
s_waitcnt vmcnt(6) lgkmcnt(0)
s_barrier
ds_read_b128 v[220:223],v244
ds_read_b128 v[224:227],v245
v_mfma_f32_16x16x32_bf16 v[36:39],v[204:207],v[184:187],v[36:39]
v_exp_f32 v176,v176
s_mov_b32 m0,s96
v_mfma_f32_16x16x32_bf16 v[84:87],v[204:207],v[188:191],v[84:87]
v_exp_f32 v177,v177
s_cmp_lg_u32 s34,1
s_cbranch_scc1 LG41
buffer_load_dwordx4 v253,s[80:83],s94 offen lds
LG41:
v_mfma_f32_16x16x32_bf16 v[108:111],v[204:207],v[200:203],v[108:111]
ds_read_b128 v[204:207],v244 offset:1024
v_add_f32 v241,v241,v176
v_exp_f32 v178,v178
v_mfma_f32_16x16x32_bf16 v[40:43],v[208:211],v[184:187],v[40:43]
v_add_f32 v241,v241,v177
v_exp_f32 v179,v179
v_cvt_pk_bf16_f32 v194,v176,v177
v_mfma_f32_16x16x32_bf16 v[88:91],v[208:211],v[188:191],v[88:91]
ds_read_b128 v[208:211],v245 offset:1024
ds_read_b128 v[200:203],v250 offset:7168
v_add_f32 v241,v241,v178
)A"
R"A(v_exp_f32 v180,v180
s_add_u32 m0,s96,896
v_mfma_f32_16x16x32_bf16 v[44:47],v[212:215],v[184:187],v[44:47]
v_add_f32 v241,v241,v179
v_exp_f32 v181,v181
v_cvt_pk_bf16_f32 v195,v178,v179
s_cmp_lg_u32 s34,1
s_cbranch_scc1 LG42
buffer_load_dwordx4 v253,s[80:83],s94 offen offset:128 lds
LG42:
v_mfma_f32_16x16x32_bf16 v[92:95],v[212:215],v[188:191],v[92:95]
ds_read_b128 v[212:215],v244 offset:2048
v_add_f32 v242,v242,v180
v_exp_f32 v182,v182
v_mfma_f32_16x16x32_bf16 v[48:51],v[216:219],v[184:187],v[48:51]
v_add_f32 v242,v242,v181
v_exp_f32 v183,v183
v_cvt_pk_bf16_f32 v198,v180,v181
v_mfma_f32_16x16x32_bf16 v[96:99],v[216:219],v[188:191],v[96:99]
ds_read_b128 v[216:219],v245 offset:2048
v_add_f32 v242,v242,v182
s_add_u32 m0,s96,1792
s_waitcnt lgkmcnt(6)
v_mfma_f32_16x16x32_bf16 v[168:171],v[220:223],v[112:115],v[160:163]
v_add_f32 v242,v242,v183
v_cvt_pk_bf16_f32 v199,v182,v183
s_cmp_lg_u32 s34,1
s_cbranch_scc1 LG43
buffer_load_dwordx4 v253,s[80:83],s94 offen offset:256 lds
LG43:
v_mfma_f32_16x16x32_bf16 v[172:175],v[220:223],v[136:139],v[164:167]
ds_read_b128 v[220:223],v244 offset:6144
s_waitcnt lgkmcnt(6)
v_mfma_f32_16x16x32_bf16 v[168:171],v[224:227],v[116:119],v[168:171]
v_mfma_f32_16x16x32_bf16 v[172:175],v[224:227],v[140:143],v[172:175]
ds_read_b128 v[224:227],v245 offset:6144
s_mov_b32 m0,s97
s_waitcnt lgkmcnt(6)
v_mfma_f32_16x16x32_bf16 v[168:171],v[204:207],v[120:123],v[168:171]
buffer_load_dwordx4 v252,s[68:71],s95 offen nt lds
v_mfma_f32_16x16x32_bf16 v[172:175],v[204:207],v[144:147],v[172:175]
ds_read_b128 v[204:207],v244 offset:7168
s_waitcnt lgkmcnt(6)
v_mfma_f32_16x16x32_bf16 v[168:171],v[208:211],v[124:127],v[168:171]
s_waitcnt lgkmcnt(5)
v_dot2c_f32_bf16 v243,s55,v200
v_mfma_f32_16x16x32_bf16 v[172:175],v[208:211],v[148:151],v[172:175]
ds_read_b128 v[208:211],v245 offset:7168
s_add_u32 m0,s97,896
s_waitcnt lgkmcnt(5)
v_mfma_f32_16x16x32_bf16 v[168:171],v[212:215],v[128:131],v[168:171]
v_dot2c_f32_bf16 v243,s55,v201
buffer_load_dwordx4 v252,s[68:71],s95 offen offset:128 nt lds
v_mfma_f32_16x16x32_bf16 v[172:175],v[212:215],v[152:155],v[172:175]
ds_read_b128 v[212:215],v244 offset:8192
s_waitcnt lgkmcnt(5)
v_mfma_f32_16x16x32_bf16 v[168:171],v[216:219],v[132:135],v[168:171]
v_dot2c_f32_bf16 v243,s55,v202
v_mfma_f32_16x16x32_bf16 v[172:175],v[216:219],v[156:159],v[172:175]
ds_read_b128 v[216:219],v245 offset:8192
s_add_u32 m0,s97,1792
s_waitcnt lgkmcnt(5)
v_mfma_f32_16x16x32_bf16 v[176:179],v[220:223],v[112:115],v[160:163]
v_dot2c_f32_bf16 v243,s55,v203
buffer_load_dwordx4 v252,s[68:71],s95 offen offset:256 nt lds
v_mfma_f32_16x16x32_bf16 v[180:183],v[220:223],v[136:139],v[164:167]
ds_read_b64_tr_b16 v[220:221],v246
ds_read_b64_tr_b16 v[222:223],v246 offset:6144
s_waitcnt lgkmcnt(6)
v_mfma_f32_16x16x32_bf16 v[176:179],v[224:227],v[116:119],v[176:179]
v_mfma_f32_16x16x32_bf16 v[180:183],v[224:227],v[140:143],v[180:183]
ds_read_b64_tr_b16 v[224:225],v247
ds_read_b64_tr_b16 v[226:227],v247 offset:6144
v_exp_f32 v168,v168
s_mov_b32 s90,s98
s_add_u32 s98,s98,12288
s_cmp_eq_u32 s98,36864
s_cmov_b32 s98,0
s_sub_u32 s46,s98,s90
v_add_u32 v244,s46,v244
v_add_u32 v245,s46,v245
s_waitcnt lgkmcnt(7)
v_mfma_f32_16x16x32_bf16 v[176:179],v[204:207],v[120:123],v[176:179]
v_exp_f32 v169,v169
v_mfma_f32_16x16x32_bf16 v[180:183],v[204:207],v[144:147],v[180:183]
ds_read_b64_tr_b16 v[204:205],v248
ds_read_b64_tr_b16 v[206:207],v248 offset:6144
v_add_f32 v241,v241,v168
v_exp_f32 v170,v170
s_add_u32 s89,s48,36864
s_add_u32 s96,s96,12288
s_cmp_eq_u32 s96,s89
s_cselect_b32 s96,s48,s96
s_waitcnt lgkmcnt(8)
v_mfma_f32_16x16x32_bf16 v[176:179],v[208:211],v[124:127],v[176:179]
v_add_f32 v241,v241,v169
v_exp_f32 v171,v171
v_cvt_pk_bf16_f32 v184,v168,v169
v_mfma_f32_16x16x32_bf16 v[180:183],v[208:211],v[148:151],v[180:183]
ds_read_b64_tr_b16 v[208:209],v249
ds_read_b64_tr_b16 v[210:211],v249 offset:6144
v_add_f32 v241,v241,v170
v_exp_f32 v172,v172
s_cmp_eq_u32 s47,0
s_cselect_b32 s94,0,1
s_mul_i32 s94,s94,393216
s_waitcnt lgkmcnt(9)
v_mfma_f32_16x16x32_bf16 v[176:179],v[212:215],v[128:131],v[176:179]
v_add_f32 v241,v241,v171
v_exp_f32 v173,v173
v_cvt_pk_bf16_f32 v185,v170,v171
v_mfma_f32_16x16x32_bf16 v[180:183],v[212:215],v[152:155],v[180:183]
ds_read_b64_tr_b16 v[212:213],v246 offset:1024
ds_read_b64_tr_b16 v[214:215],v246 offset:7168
v_add_f32 v242,v242,v172
v_exp_f32 v174,v174
s_waitcnt lgkmcnt(10)
v_mfma_f32_16x16x32_bf16 v[176:179],v[216:219],v[132:135],v[176:179]
v_add_f32 v242,v242,v173
v_exp_f32 v175,v175
v_cvt_pk_bf16_f32 v188,v172,v173
v_mfma_f32_16x16x32_bf16 v[180:183],v[216:219],v[156:159],v[180:183]
ds_read_b64_tr_b16 v[216:217],v247 offset:1024
ds_read_b64_tr_b16 v[218:219],v247 offset:7168
v_add_f32 v242,v242,v174
s_waitcnt lgkmcnt(10)
v_mfma_f32_16x16x32_bf16 v[4:7],v[220:223],v[192:195],v[4:7]
v_add_f32 v242,v242,v175
v_cvt_pk_bf16_f32 v189,v174,v175
v_mfma_f32_16x16x32_bf16 v[52:55],v[220:223],v[196:199],v[52:55]
v_mfma_f32_16x16x32_bf16 v[100:103],v[220:223],v[200:203],v[100:103]
ds_read_b64_tr_b16 v[220:221],v248 offset:1024
ds_read_b64_tr_b16 v[222:223],v248 offset:7168
s_waitcnt lgkmcnt(10)
v_mfma_f32_16x16x32_bf16 v[8:11],v[224:227],v[192:195],v[8:11]
v_mfma_f32_16x16x32_bf16 v[56:59],v[224:227],v[196:199],v[56:59]
ds_read_b64_tr_b16 v[224:225],v249 offset:1024
ds_read_b64_tr_b16 v[226:227],v249 offset:7168
s_waitcnt lgkmcnt(10)
v_mfma_f32_16x16x32_bf16 v[12:15],v[204:207],v[192:195],v[12:15]
v_mfma_f32_16x16x32_bf16 v[60:63],v[204:207],v[196:199],v[60:63]
ds_read_b64_tr_b16 v[204:205],v246 offset:2048
ds_read_b64_tr_b16 v[206:207],v246 offset:8192
s_waitcnt lgkmcnt(10)
v_mfma_f32_16x16x32_bf16 v[16:19],v[208:211],v[192:195],v[16:19]
v_mfma_f32_16x16x32_bf16 v[64:67],v[208:211],v[196:199],v[64:67]
ds_read_b64_tr_b16 v[208:209],v247 offset:2048
ds_read_b64_tr_b16 v[210:211],v247 offset:8192
s_waitcnt lgkmcnt(10)
v_mfma_f32_16x16x32_bf16 v[20:23],v[212:215],v[192:195],v[20:23]
v_mfma_f32_16x16x32_bf16 v[68:71],v[212:215],v[196:199],v[68:71]
v_mfma_f32_16x16x32_bf16 v[104:107],v[212:215],v[200:203],v[104:107]
ds_read_b64_tr_b16 v[212:213],v248 offset:2048
ds_read_b64_tr_b16 v[214:215],v248 offset:8192
s_waitcnt lgkmcnt(10)
v_mfma_f32_16x16x32_bf16 v[24:27],v[216:219],v[192:195],v[24:27]
v_mfma_f32_16x16x32_bf16 v[72:75],v[216:219],v[196:199],v[72:75]
ds_read_b64_tr_b16 v[216:217],v249 offset:2048
ds_read_b64_tr_b16 v[218:219],v249 offset:8192
s_waitcnt lgkmcnt(10)
v_mfma_f32_16x16x32_bf16 v[28:31],v[220:223],v[192:195],v[28:31]
s_mov_b32 s90,s99
s_add_u32 s99,s99,12288
s_cmp_eq_u32 s99,36864
s_cmov_b32 s99,0
s_sub_u32 s46,s99,s90
v_add_u32 v246,s46,v246
v_add_u32 v247,s46,v247
v_add_u32 v248,s46,v248
v_add_u32 v249,s46,v249
v_mfma_f32_16x16x32_bf16 v[76:79],v[220:223],v[196:199],v[76:79]
s_waitcnt lgkmcnt(8)
v_mfma_f32_16x16x32_bf16 v[32:35],v[224:227],v[192:195],v[32:35]
v_mfma_f32_16x16x32_bf16 v[80:83],v[224:227],v[196:199],v[80:83]
s_waitcnt vmcnt(3) lgkmcnt(0)
s_barrier
ds_read_b128 v[220:223],v244
ds_read_b128 v[224:227],v245
v_mfma_f32_16x16x32_bf16 v[36:39],v[204:207],v[192:195],v[36:39]
v_exp_f32 v176,v176
s_mov_b32 m0,s96
v_mfma_f32_16x16x32_bf16 v[84:87],v[204:207],v[196:199],v[84:87]
v_exp_f32 v177,v177
s_cmp_lg_u32 s34,1
s_cbranch_scc1 LG44
buffer_load_dwordx4 v253,s[80:83],s94 offen lds
LG44:
v_mfma_f32_16x16x32_bf16 v[108:111],v[204:207],v[200:203],v[108:111]
ds_read_b128 v[204:207],v244 offset:1024
ds_read_b128 v[228:231],v250
ds_read_b128 v[232:235],v250 offset:1024
v_add_f32 v241,v241,v176
v_exp_f32 v178,v178
v_mfma_f32_16x16x32_bf16 v[40:43],v[208:211],v[192:195],v[40:43]
v_add_f32 v241,v241,v177
v_exp_f32 v179,v179
v_cvt_pk_bf16_f32 v186,v176,v177
v_mfma_f32_16x16x32_bf16 v[88:91],v[208:211],v[196:199],v[88:91]
ds_read_b128 v[208:211],v245 offset:1024
ds_read_b128 v[200:203],v250 offset:6144
v_add_f32 v241,v241,v178
v_exp_f32 v180,v180
s_add_u32 m0,s96,896
v_mfma_f32_16x16x32_bf16 v[44:47],v[212:215],v[192:195],v[44:47]
v_mov_b32 v236,v240
v_mov_b32 v237,v240
v_mov_b32 v238,v240
v_mov_b32 v239,v240
v_add_f32 v241,v241,v179
v_exp_f32 v181,v181
v_cvt_pk_bf16_f32 v187,v178,v179
s_cmp_lg_u32 s34,1
s_cbranch_scc1 LG45
buffer_load_dwordx4 v253,s[80:83],s94 offen offset:128 lds
LG45:
v_mfma_f32_16x16x32_bf16 v[92:95],v[212:215],v[196:199],v[92:95]
ds_read_b128 v[212:215],v244 offset:2048
v_add_f32 v242,v242,v180
v_exp_f32 v182,v182
v_mfma_f32_16x16x32_bf16 v[48:51],v[216:219],v[192:195],v[48:51]
v_add_f32 v242,v242,v181
v_exp_f32 v183,v183
v_cvt_pk_bf16_f32 v190,v180,v181
v_mfma_f32_16x16x32_bf16 v[96:99],v[216:219],v[196:199],v[96:99]
ds_read_b128 v[216:219],v245 offset:2048
v_add_f32 v242,v242,v182
s_add_u32 m0,s96,1792
s_waitcnt lgkmcnt(8)
v_mfma_f32_16x16x32_bf16 v[168:171],v[220:223],v[112:115],v[160:163]
v_add_f32 v242,v242,v183
v_cvt_pk_bf16_f32 v191,v182,v183
s_cmp_lg_u32 s34,1
s_cbranch_scc1 LG46
buffer_load_dwordx4 v253,s[80:83],s94 offen offset:256 lds
LG46:
v_mfma_f32_16x16x32_bf16 v[172:175],v[220:223],v[136:139],v[164:167]
s_waitcnt lgkmcnt(5)
v_mfma_f32_16x16x32_bf16 v[236:239],v[220:223],v[228:231],v[236:239]
ds_read_b128 v[220:223],v244 offset:6144
ds_read_b128 v[228:231],v250 offset:2048
v_mfma_f32_16x16x32_bf16 v[168:171],v[224:227],v[116:119],v[168:171]
v_mfma_f32_16x16x32_bf16 v[172:175],v[224:227],v[140:143],v[172:175]
s_waitcnt lgkmcnt(6)
v_mfma_f32_16x16x32_bf16 v[236:239],v[224:227],v[232:235],v[236:239]
ds_read_b128 v[224:227],v245 offset:6144
ds_read_b128 v[232:235],v250 offset:3072
s_add_u32 s89,s48,36864
s_add_u32 s96,s96,12288
s_cmp_eq_u32 s96,s89
s_cselect_b32 s96,s48,s96
v_mfma_f32_16x16x32_bf16 v[168:171],v[204:207],v[120:123],v[168:171]
v_mfma_f32_16x16x32_bf16 v[172:175],v[204:207],v[144:147],v[172:175]
s_cmp_eq_u32 s47,0
s_cselect_b32 s94,4,2
s_mul_i32 s94,s94,393216
s_waitcnt lgkmcnt(2)
v_mfma_f32_16x16x32_bf16 v[236:239],v[204:207],v[228:231],v[236:239]
ds_read_b128 v[204:207],v244 offset:7168
ds_read_b128 v[228:231],v250 offset:4096
v_mfma_f32_16x16x32_bf16 v[168:171],v[208:211],v[124:127],v[168:171]
v_dot2c_f32_bf16 v243,s55,v200
s_xor_b32 s90,s33,1
s_mul_i32 s90,s90,18432
s_lshl_b32 s97,s84,10
s_add_u32 s97,s97,s51
s_add_u32 s97,s97,73728
s_add_u32 s97,s97,s90
s_mov_b32 s95,0
v_mfma_f32_16x16x32_bf16 v[172:175],v[208:211],v[148:151],v[172:175]
s_waitcnt lgkmcnt(2)
v_mfma_f32_16x16x32_bf16 v[236:239],v[208:211],v[232:235],v[236:239]
ds_read_b128 v[208:211],v245 offset:7168
ds_read_b128 v[232:235],v250 offset:5120
v_mfma_f32_16x16x32_bf16 v[168:171],v[212:215],v[128:131],v[168:171]
v_dot2c_f32_bf16 v243,s55,v201
v_mfma_f32_16x16x32_bf16 v[172:175],v[212:215],v[152:155],v[172:175]
s_waitcnt lgkmcnt(2)
v_mfma_f32_16x16x32_bf16 v[236:239],v[212:215],v[228:231],v[236:239]
ds_read_b128 v[212:215],v244 offset:8192
v_mfma_f32_16x16x32_bf16 v[168:171],v[216:219],v[132:135],v[168:171]
v_dot2c_f32_bf16 v243,s55,v202
v_mfma_f32_16x16x32_bf16 v[172:175],v[216:219],v[156:159],v[172:175]
s_waitcnt lgkmcnt(1)
v_mfma_f32_16x16x32_bf16 v[236:239],v[216:219],v[232:235],v[236:239]
ds_read_b128 v[216:219],v245 offset:8192
v_mfma_f32_16x16x32_bf16 v[176:179],v[220:223],v[112:115],v[160:163]
v_dot2c_f32_bf16 v243,s55,v203
v_mfma_f32_16x16x32_bf16 v[180:183],v[220:223],v[136:139],v[164:167]
ds_read_b64_tr_b16 v[220:221],v246
ds_read_b64_tr_b16 v[222:223],v246 offset:6144
v_mfma_f32_16x16x32_bf16 v[176:179],v[224:227],v[116:119],v[176:179]
v_mfma_f32_16x16x32_bf16 v[180:183],v[224:227],v[140:143],v[180:183]
ds_read_b64_tr_b16 v[224:225],v247
ds_read_b64_tr_b16 v[226:227],v247 offset:6144
v_exp_f32 v236,v236
v_exp_f32 v168,v168
s_mov_b32 s90,s98
s_add_u32 s98,s98,12288
s_cmp_eq_u32 s98,36864
s_cmov_b32 s98,0
s_sub_u32 s46,s98,s90
v_add_u32 v244,s46,v244
v_add_u32 v245,s46,v245
v_mfma_f32_16x16x32_bf16 v[176:179],v[204:207],v[120:123],v[176:179]
v_exp_f32 v237,v237
v_exp_f32 v169,v169
v_mfma_f32_16x16x32_bf16 v[180:183],v[204:207],v[144:147],v[180:183]
ds_read_b64_tr_b16 v[204:205],v248
ds_read_b64_tr_b16 v[206:207],v248 offset:6144
v_exp_f32 v238,v238
v_add_f32 v241,v241,v168
v_exp_f32 v170,v170
v_mfma_f32_16x16x32_bf16 v[176:179],v[208:211],v[124:127],v[176:179]
v_exp_f32 v239,v239
v_add_f32 v241,v241,v169
v_exp_f32 v171,v171
v_cvt_pk_bf16_f32 v192,v168,v169
v_mfma_f32_16x16x32_bf16 v[180:183],v[208:211],v[148:151],v[180:183]
ds_read_b64_tr_b16 v[208:209],v249
ds_read_b64_tr_b16 v[210:211],v249 offset:6144
v_add_f32 v241,v241,v170
v_exp_f32 v172,v172
s_waitcnt lgkmcnt(9)
v_mfma_f32_16x16x32_bf16 v[176:179],v[212:215],v[128:131],v[176:179]
v_cvt_pk_bf16_f32 v0,v236,v237
v_cvt_pk_bf16_f32 v1,v238,v239
v_add_f32 v241,v241,v171
v_exp_f32 v173,v173
v_cvt_pk_bf16_f32 v193,v170,v171
v_mfma_f32_16x16x32_bf16 v[180:183],v[212:215],v[152:155],v[180:183]
ds_read_b64_tr_b16 v[212:213],v246 offset:1024
ds_read_b64_tr_b16 v[214:215],v246 offset:7168
ds_write_b64 v250,v[0:1] offset:7168
v_add_f32 v242,v242,v172
v_exp_f32 v174,v174
s_waitcnt lgkmcnt(11)
v_mfma_f32_16x16x32_bf16 v[176:179],v[216:219],v[132:135],v[176:179]
v_add_f32 v242,v242,v173
v_exp_f32 v175,v175
v_cvt_pk_bf16_f32 v196,v172,v173
v_mfma_f32_16x16x32_bf16 v[180:183],v[216:219],v[156:159],v[180:183]
ds_read_b64_tr_b16 v[216:217],v247 offset:1024
ds_read_b64_tr_b16 v[218:219],v247 offset:7168
v_add_f32 v242,v242,v174
s_waitcnt lgkmcnt(11)
v_mfma_f32_16x16x32_bf16 v[4:7],v[220:223],v[184:187],v[4:7]
v_add_f32 v242,v242,v175
v_cvt_pk_bf16_f32 v197,v174,v175
v_mfma_f32_16x16x32_bf16 v[52:55],v[220:223],v[188:191],v[52:55]
v_mfma_f32_16x16x32_bf16 v[100:103],v[220:223],v[200:203],v[100:103]
ds_read_b64_tr_b16 v[220:221],v248 offset:1024
ds_read_b64_tr_b16 v[222:223],v248 offset:7168
s_waitcnt lgkmcnt(11)
v_mfma_f32_16x16x32_bf16 v[8:11],v[224:227],v[184:187],v[8:11]
v_mfma_f32_16x16x32_bf16 v[56:59],v[224:227],v[188:191],v[56:59]
ds_read_b64_tr_b16 v[224:225],v249 offset:1024
ds_read_b64_tr_b16 v[226:227],v249 offset:7168
s_waitcnt lgkmcnt(11)
v_mfma_f32_16x16x32_bf16 v[12:15],v[204:207],v[184:187],v[12:15]
v_mfma_f32_16x16x32_bf16 v[60:63],v[204:207],v[188:191],v[60:63]
ds_read_b64_tr_b16 v[204:205],v246 offset:2048
ds_read_b64_tr_b16 v[206:207],v246 offset:8192
s_waitcnt lgkmcnt(11)
v_mfma_f32_16x16x32_bf16 v[16:19],v[208:211],v[184:187],v[16:19]
v_mfma_f32_16x16x32_bf16 v[64:67],v[208:211],v[188:191],v[64:67]
ds_read_b64_tr_b16 v[208:209],v247 offset:2048
ds_read_b64_tr_b16 v[210:211],v247 offset:8192
s_waitcnt lgkmcnt(11)
v_mfma_f32_16x16x32_bf16 v[20:23],v[212:215],v[184:187],v[20:23]
v_mfma_f32_16x16x32_bf16 v[68:71],v[212:215],v[188:191],v[68:71]
v_mfma_f32_16x16x32_bf16 v[104:107],v[212:215],v[200:203],v[104:107]
ds_read_b64_tr_b16 v[212:213],v248 offset:2048
ds_read_b64_tr_b16 v[214:215],v248 offset:8192
s_waitcnt lgkmcnt(10)
v_mfma_f32_16x16x32_bf16 v[24:27],v[216:219],v[184:187],v[24:27]
v_mfma_f32_16x16x32_bf16 v[72:75],v[216:219],v[188:191],v[72:75]
ds_read_b64_tr_b16 v[216:217],v249 offset:2048
ds_read_b64_tr_b16 v[218:219],v249 offset:8192
s_waitcnt lgkmcnt(10)
v_mfma_f32_16x16x32_bf16 v[28:31],v[220:223],v[184:187],v[28:31]
s_mov_b32 s90,s99
s_add_u32 s99,s99,12288
s_cmp_eq_u32 s99,36864
s_cmov_b32 s99,0
s_sub_u32 s46,s99,s90
v_add_u32 v246,s46,v246
v_add_u32 v247,s46,v247
v_add_u32 v248,s46,v248
v_add_u32 v249,s46,v249
v_mfma_f32_16x16x32_bf16 v[76:79],v[220:223],v[188:191],v[76:79]
s_waitcnt lgkmcnt(8)
v_mfma_f32_16x16x32_bf16 v[32:35],v[224:227],v[184:187],v[32:35]
v_mfma_f32_16x16x32_bf16 v[80:83],v[224:227],v[188:191],v[80:83]
s_waitcnt vmcnt(0) lgkmcnt(0)
s_barrier
ds_read_b64_tr_b16 v[220:221],v246
ds_read_b64_tr_b16 v[222:223],v246 offset:6144
ds_read_b64_tr_b16 v[224:225],v247
ds_read_b64_tr_b16 v[226:227],v247 offset:6144
v_mfma_f32_16x16x32_bf16 v[36:39],v[204:207],v[184:187],v[36:39]
)A"
R"A(v_exp_f32 v176,v176
s_mov_b32 m0,s96
v_mfma_f32_16x16x32_bf16 v[84:87],v[204:207],v[188:191],v[84:87]
v_exp_f32 v177,v177
s_cmp_lg_u32 s34,1
s_cbranch_scc1 LG47
buffer_load_dwordx4 v253,s[80:83],s94 offen lds
LG47:
v_mfma_f32_16x16x32_bf16 v[108:111],v[204:207],v[200:203],v[108:111]
ds_read_b64_tr_b16 v[204:205],v248
ds_read_b64_tr_b16 v[206:207],v248 offset:6144
v_add_f32 v241,v241,v176
v_exp_f32 v178,v178
s_add_u32 m0,s96,896
v_mfma_f32_16x16x32_bf16 v[40:43],v[208:211],v[184:187],v[40:43]
v_add_f32 v241,v241,v177
v_exp_f32 v179,v179
v_cvt_pk_bf16_f32 v194,v176,v177
s_cmp_lg_u32 s34,1
s_cbranch_scc1 LG48
buffer_load_dwordx4 v253,s[80:83],s94 offen offset:128 lds
LG48:
v_mfma_f32_16x16x32_bf16 v[88:91],v[208:211],v[188:191],v[88:91]
ds_read_b64_tr_b16 v[208:209],v249
ds_read_b64_tr_b16 v[210:211],v249 offset:6144
ds_read_b128 v[200:203],v250 offset:7168
v_add_f32 v241,v241,v178
v_exp_f32 v180,v180
s_add_u32 m0,s96,1792
v_mfma_f32_16x16x32_bf16 v[44:47],v[212:215],v[184:187],v[44:47]
v_add_f32 v241,v241,v179
v_exp_f32 v181,v181
v_cvt_pk_bf16_f32 v195,v178,v179
s_cmp_lg_u32 s34,1
s_cbranch_scc1 LG49
buffer_load_dwordx4 v253,s[80:83],s94 offen offset:256 lds
LG49:
v_mfma_f32_16x16x32_bf16 v[92:95],v[212:215],v[188:191],v[92:95]
ds_read_b64_tr_b16 v[212:213],v246 offset:1024
ds_read_b64_tr_b16 v[214:215],v246 offset:7168
v_add_f32 v242,v242,v180
v_exp_f32 v182,v182
s_mov_b32 m0,s97
s_mov_b32 s88,s95
v_mfma_f32_16x16x32_bf16 v[48:51],v[216:219],v[184:187],v[48:51]
v_add_f32 v242,v242,v181
v_exp_f32 v183,v183
v_cvt_pk_bf16_f32 v198,v180,v181
s_cmp_lg_u32 s34,1
s_cbranch_scc1 LG50
buffer_load_dwordx4 v254,s[76:79],s88 offen lds
LG50:
v_mfma_f32_16x16x32_bf16 v[96:99],v[216:219],v[188:191],v[96:99]
ds_read_b64_tr_b16 v[216:217],v247 offset:1024
ds_read_b64_tr_b16 v[218:219],v247 offset:7168
v_add_f32 v242,v242,v182
v_add_f32 v242,v242,v183
v_cvt_pk_bf16_f32 v199,v182,v183
s_add_u32 m0,s97,4096
s_add_u32 s88,s95,40960
s_waitcnt lgkmcnt(11)
v_mfma_f32_16x16x32_bf16 v[4:7],v[220:223],v[192:195],v[4:7]
s_cmp_lg_u32 s34,1
s_cbranch_scc1 LG51
buffer_load_dwordx4 v254,s[76:79],s88 offen lds
LG51:
v_mfma_f32_16x16x32_bf16 v[52:55],v[220:223],v[196:199],v[52:55]
s_add_u32 m0,s97,8192
s_add_u32 s88,s95,81920
s_waitcnt lgkmcnt(4)
v_mfma_f32_16x16x32_bf16 v[100:103],v[220:223],v[200:203],v[100:103]
ds_read_b64_tr_b16 v[220:221],v248 offset:1024
ds_read_b64_tr_b16 v[222:223],v248 offset:7168
s_cmp_lg_u32 s34,1
s_cbranch_scc1 LG52
buffer_load_dwordx4 v254,s[76:79],s88 offen lds
LG52:
v_mfma_f32_16x16x32_bf16 v[8:11],v[224:227],v[192:195],v[8:11]
s_add_u32 m0,s97,12288
s_add_u32 s88,s95,122880
v_mov_b32 v164,v241
s_nop 1
v_permlane32_swap_b32 v241,v164
v_add_f32 v241,v241,v164
v_mov_b32 v164,v241
s_nop 1
v_permlane16_swap_b32 v241,v164
v_add_f32 v241,v241,v164
v_rcp_f32 v0,v241
v_mfma_f32_16x16x32_bf16 v[56:59],v[224:227],v[196:199],v[56:59]
ds_read_b64_tr_b16 v[224:225],v249 offset:1024
ds_read_b64_tr_b16 v[226:227],v249 offset:7168
s_cmp_lg_u32 s34,1
s_cbranch_scc1 LG53
buffer_load_dwordx4 v254,s[76:79],s88 offen lds
LG53:
v_mov_b32 v165,v242
s_nop 1
v_permlane32_swap_b32 v242,v165
v_add_f32 v242,v242,v165
v_mov_b32 v165,v242
s_nop 1
v_permlane16_swap_b32 v242,v165
v_add_f32 v242,v242,v165
v_rcp_f32 v2,v242
v_mfma_f32_16x16x32_bf16 v[12:15],v[204:207],v[192:195],v[12:15]
s_add_u32 m0,s97,16384
s_add_u32 s88,s95,163840
v_mfma_f32_16x16x32_bf16 v[60:63],v[204:207],v[196:199],v[60:63]
ds_read_b64_tr_b16 v[204:205],v246 offset:2048
ds_read_b64_tr_b16 v[206:207],v246 offset:8192
s_cmp_lg_u32 s34,1
s_cbranch_scc1 LG54
s_cmp_ge_u32 s84,2
s_cbranch_scc1 LX55
buffer_load_dwordx4 v254,s[76:79],s88 offen lds
LX55:
LG54:
v_mfma_f32_16x16x32_bf16 v[16:19],v[208:211],v[192:195],v[16:19]
v_dot2c_f32_bf16 v243,s55,v200
v_mfma_f32_16x16x32_bf16 v[64:67],v[208:211],v[196:199],v[64:67]
ds_read_b64_tr_b16 v[208:209],v247 offset:2048
ds_read_b64_tr_b16 v[210:211],v247 offset:8192
s_waitcnt lgkmcnt(10)
v_mfma_f32_16x16x32_bf16 v[20:23],v[212:215],v[192:195],v[20:23]
v_dot2c_f32_bf16 v243,s55,v201
v_mfma_f32_16x16x32_bf16 v[68:71],v[212:215],v[196:199],v[68:71]
v_mfma_f32_16x16x32_bf16 v[104:107],v[212:215],v[200:203],v[104:107]
ds_read_b64_tr_b16 v[212:213],v248 offset:2048
ds_read_b64_tr_b16 v[214:215],v248 offset:8192
s_waitcnt lgkmcnt(10)
v_mfma_f32_16x16x32_bf16 v[24:27],v[216:219],v[192:195],v[24:27]
v_dot2c_f32_bf16 v243,s55,v202
v_mfma_f32_16x16x32_bf16 v[72:75],v[216:219],v[196:199],v[72:75]
ds_read_b64_tr_b16 v[216:217],v249 offset:2048
ds_read_b64_tr_b16 v[218:219],v249 offset:8192
s_waitcnt lgkmcnt(10)
v_mfma_f32_16x16x32_bf16 v[28:31],v[220:223],v[192:195],v[28:31]
v_dot2c_f32_bf16 v243,s55,v203
v_mfma_f32_16x16x32_bf16 v[76:79],v[220:223],v[196:199],v[76:79]
s_waitcnt lgkmcnt(8)
v_mfma_f32_16x16x32_bf16 v[32:35],v[224:227],v[192:195],v[32:35]
v_mfma_f32_16x16x32_bf16 v[80:83],v[224:227],v[196:199],v[80:83]
v_mov_b32 v166,v243
s_nop 1
v_permlane32_swap_b32 v243,v166
v_add_f32 v243,v243,v166
v_mov_b32 v166,v243
s_nop 1
v_permlane16_swap_b32 v243,v166
v_add_f32 v243,v243,v166
v_rcp_f32 v160,v243
s_waitcnt lgkmcnt(6)
v_mfma_f32_16x16x32_bf16 v[36:39],v[204:207],v[192:195],v[36:39]
s_mov_b32 s90,0x5d800000
v_cmp_ngt_f32 s[92:93],s90,v241
v_cmp_ngt_f32 vcc,s90,v242
s_nop 0
s_or_b64 s[92:93],s[92:93],vcc
v_cmp_ngt_f32 vcc,s90,v243
s_nop 0
s_or_b64 s[92:93],s[92:93],vcc
s_cselect_b32 s90,1,0
v_mov_b32 v164,s90
s_lshl_b32 s91,s47,4
s_add_u32 s91,s91,153608
v_mov_b32 v165,s91
ds_write_b32 v165,v164
v_mbcnt_lo_u32_b32 v167,-1,0
v_mbcnt_hi_u32_b32 v167,-1,v167
v_lshlrev_b32 v167,4,v167
s_cmp_eq_u32 s33,0
s_movk_i32 s91,18432
s_cselect_b32 s91,12288,s91
s_add_u32 s91,s91,s51
s_add_u32 s91,s91,73728
v_mbcnt_lo_u32_b32 v162,-1,0
v_mbcnt_hi_u32_b32 v162,-1,v162
v_lshlrev_b32 v162,3,v162
v_add_u32 v162,s91,v162
v_mfma_f32_16x16x32_bf16 v[84:87],v[204:207],v[196:199],v[84:87]
v_pk_mul_f32 v[4:5],v[4:5],v[0:1] op_sel_hi:[1,0]
v_pk_mul_f32 v[6:7],v[6:7],v[0:1] op_sel_hi:[1,0]
v_pk_mul_f32 v[8:9],v[8:9],v[0:1] op_sel_hi:[1,0]
v_pk_mul_f32 v[10:11],v[10:11],v[0:1] op_sel_hi:[1,0]
v_cvt_pk_bf16_f32 v4,v4,v5
v_cvt_pk_bf16_f32 v5,v6,v7
v_cvt_pk_bf16_f32 v6,v8,v9
v_cvt_pk_bf16_f32 v7,v10,v11
s_mov_b32 s91,0xc0400
buffer_store_dwordx4 v[4:7],v167,s[72:75],s91 offen sc1
v_mfma_f32_16x16x32_bf16 v[108:111],v[204:207],v[200:203],v[108:111]
v_pk_mul_f32 v[52:53],v[52:53],v[2:3] op_sel_hi:[1,0]
v_pk_mul_f32 v[54:55],v[54:55],v[2:3] op_sel_hi:[1,0]
v_pk_mul_f32 v[56:57],v[56:57],v[2:3] op_sel_hi:[1,0]
v_pk_mul_f32 v[58:59],v[58:59],v[2:3] op_sel_hi:[1,0]
v_cvt_pk_bf16_f32 v52,v52,v53
v_cvt_pk_bf16_f32 v53,v54,v55
v_cvt_pk_bf16_f32 v54,v56,v57
v_cvt_pk_bf16_f32 v55,v58,v59
s_mov_b32 s91,0xf0400
buffer_store_dwordx4 v[52:55],v167,s[72:75],s91 offen sc1
s_waitcnt lgkmcnt(5)
v_mfma_f32_16x16x32_bf16 v[40:43],v[208:211],v[192:195],v[40:43]
v_pk_mul_f32 v[12:13],v[12:13],v[0:1] op_sel_hi:[1,0]
v_pk_mul_f32 v[14:15],v[14:15],v[0:1] op_sel_hi:[1,0]
v_pk_mul_f32 v[16:17],v[16:17],v[0:1] op_sel_hi:[1,0]
v_pk_mul_f32 v[18:19],v[18:19],v[0:1] op_sel_hi:[1,0]
v_cvt_pk_bf16_f32 v12,v12,v13
v_cvt_pk_bf16_f32 v13,v14,v15
v_cvt_pk_bf16_f32 v14,v16,v17
v_cvt_pk_bf16_f32 v15,v18,v19
s_mov_b32 s91,0xc0000
buffer_store_dwordx4 v[12:15],v167,s[72:75],s91 offen sc1
v_mfma_f32_16x16x32_bf16 v[88:91],v[208:211],v[196:199],v[88:91]
v_pk_mul_f32 v[60:61],v[60:61],v[2:3] op_sel_hi:[1,0]
v_pk_mul_f32 v[62:63],v[62:63],v[2:3] op_sel_hi:[1,0]
v_pk_mul_f32 v[64:65],v[64:65],v[2:3] op_sel_hi:[1,0]
v_pk_mul_f32 v[66:67],v[66:67],v[2:3] op_sel_hi:[1,0]
v_cvt_pk_bf16_f32 v60,v60,v61
v_cvt_pk_bf16_f32 v61,v62,v63
v_cvt_pk_bf16_f32 v62,v64,v65
v_cvt_pk_bf16_f32 v63,v66,v67
s_mov_b32 s91,0xf0000
buffer_store_dwordx4 v[60:63],v167,s[72:75],s91 offen sc1
s_waitcnt lgkmcnt(3)
v_mfma_f32_16x16x32_bf16 v[44:47],v[212:215],v[192:195],v[44:47]
v_pk_mul_f32 v[100:101],v[100:101],v[160:161] op_sel_hi:[1,0]
v_pk_mul_f32 v[102:103],v[102:103],v[160:161] op_sel_hi:[1,0]
v_cvt_pk_bf16_f32 v100,v100,v101
v_cvt_pk_bf16_f32 v101,v102,v103
v_mfma_f32_16x16x32_bf16 v[92:95],v[212:215],v[196:199],v[92:95]
v_pk_mul_f32 v[20:21],v[20:21],v[0:1] op_sel_hi:[1,0]
v_pk_mul_f32 v[22:23],v[22:23],v[0:1] op_sel_hi:[1,0]
v_pk_mul_f32 v[24:25],v[24:25],v[0:1] op_sel_hi:[1,0]
v_pk_mul_f32 v[26:27],v[26:27],v[0:1] op_sel_hi:[1,0]
v_cvt_pk_bf16_f32 v20,v20,v21
v_cvt_pk_bf16_f32 v21,v22,v23
v_cvt_pk_bf16_f32 v22,v24,v25
v_cvt_pk_bf16_f32 v23,v26,v27
s_mov_b32 s91,0xc0c00
buffer_store_dwordx4 v[20:23],v167,s[72:75],s91 offen sc1
s_waitcnt lgkmcnt(1)
v_mfma_f32_16x16x32_bf16 v[48:51],v[216:219],v[192:195],v[48:51]
v_pk_mul_f32 v[68:69],v[68:69],v[2:3] op_sel_hi:[1,0]
v_pk_mul_f32 v[70:71],v[70:71],v[2:3] op_sel_hi:[1,0]
v_pk_mul_f32 v[72:73],v[72:73],v[2:3] op_sel_hi:[1,0]
v_pk_mul_f32 v[74:75],v[74:75],v[2:3] op_sel_hi:[1,0]
v_cvt_pk_bf16_f32 v68,v68,v69
v_cvt_pk_bf16_f32 v69,v70,v71
v_cvt_pk_bf16_f32 v70,v72,v73
v_cvt_pk_bf16_f32 v71,v74,v75
s_mov_b32 s91,0xf0c00
buffer_store_dwordx4 v[68:71],v167,s[72:75],s91 offen sc1
v_mfma_f32_16x16x32_bf16 v[96:99],v[216:219],v[196:199],v[96:99]
v_pk_mul_f32 v[28:29],v[28:29],v[0:1] op_sel_hi:[1,0]
v_pk_mul_f32 v[30:31],v[30:31],v[0:1] op_sel_hi:[1,0]
v_pk_mul_f32 v[32:33],v[32:33],v[0:1] op_sel_hi:[1,0]
v_pk_mul_f32 v[34:35],v[34:35],v[0:1] op_sel_hi:[1,0]
v_cvt_pk_bf16_f32 v28,v28,v29
v_cvt_pk_bf16_f32 v29,v30,v31
v_cvt_pk_bf16_f32 v30,v32,v33
v_cvt_pk_bf16_f32 v31,v34,v35
s_mov_b32 s91,0xc0800
buffer_store_dwordx4 v[28:31],v167,s[72:75],s91 offen sc1
v_pk_mul_f32 v[76:77],v[76:77],v[2:3] op_sel_hi:[1,0]
v_pk_mul_f32 v[78:79],v[78:79],v[2:3] op_sel_hi:[1,0]
v_pk_mul_f32 v[80:81],v[80:81],v[2:3] op_sel_hi:[1,0]
v_pk_mul_f32 v[82:83],v[82:83],v[2:3] op_sel_hi:[1,0]
v_cvt_pk_bf16_f32 v76,v76,v77
v_cvt_pk_bf16_f32 v77,v78,v79
v_cvt_pk_bf16_f32 v78,v80,v81
v_cvt_pk_bf16_f32 v79,v82,v83
s_mov_b32 s91,0xf0800
buffer_store_dwordx4 v[76:79],v167,s[72:75],s91 offen sc1
v_pk_mul_f32 v[104:105],v[104:105],v[160:161] op_sel_hi:[1,0]
v_pk_mul_f32 v[106:107],v[106:107],v[160:161] op_sel_hi:[1,0]
v_cvt_pk_bf16_f32 v104,v104,v105
v_cvt_pk_bf16_f32 v105,v106,v107
v_pk_mul_f32 v[36:37],v[36:37],v[0:1] op_sel_hi:[1,0]
v_pk_mul_f32 v[38:39],v[38:39],v[0:1] op_sel_hi:[1,0]
v_pk_mul_f32 v[40:41],v[40:41],v[0:1] op_sel_hi:[1,0]
v_pk_mul_f32 v[42:43],v[42:43],v[0:1] op_sel_hi:[1,0]
v_cvt_pk_bf16_f32 v36,v36,v37
v_cvt_pk_bf16_f32 v37,v38,v39
v_cvt_pk_bf16_f32 v38,v40,v41
v_cvt_pk_bf16_f32 v39,v42,v43
s_mov_b32 s91,0xc1400
buffer_store_dwordx4 v[36:39],v167,s[72:75],s91 offen sc1
v_pk_mul_f32 v[84:85],v[84:85],v[2:3] op_sel_hi:[1,0]
v_pk_mul_f32 v[86:87],v[86:87],v[2:3] op_sel_hi:[1,0]
v_pk_mul_f32 v[88:89],v[88:89],v[2:3] op_sel_hi:[1,0]
v_pk_mul_f32 v[90:91],v[90:91],v[2:3] op_sel_hi:[1,0]
v_cvt_pk_bf16_f32 v84,v84,v85
v_cvt_pk_bf16_f32 v85,v86,v87
v_cvt_pk_bf16_f32 v86,v88,v89
v_cvt_pk_bf16_f32 v87,v90,v91
s_mov_b32 s91,0xf1400
buffer_store_dwordx4 v[84:87],v167,s[72:75],s91 offen sc1
v_pk_mul_f32 v[44:45],v[44:45],v[0:1] op_sel_hi:[1,0]
v_pk_mul_f32 v[46:47],v[46:47],v[0:1] op_sel_hi:[1,0]
v_pk_mul_f32 v[48:49],v[48:49],v[0:1] op_sel_hi:[1,0]
v_pk_mul_f32 v[50:51],v[50:51],v[0:1] op_sel_hi:[1,0]
v_cvt_pk_bf16_f32 v44,v44,v45
v_cvt_pk_bf16_f32 v45,v46,v47
v_cvt_pk_bf16_f32 v46,v48,v49
v_cvt_pk_bf16_f32 v47,v50,v51
s_mov_b32 s91,0xc1000
buffer_store_dwordx4 v[44:47],v167,s[72:75],s91 offen sc1
v_pk_mul_f32 v[92:93],v[92:93],v[2:3] op_sel_hi:[1,0]
v_pk_mul_f32 v[94:95],v[94:95],v[2:3] op_sel_hi:[1,0]
v_pk_mul_f32 v[96:97],v[96:97],v[2:3] op_sel_hi:[1,0]
v_pk_mul_f32 v[98:99],v[98:99],v[2:3] op_sel_hi:[1,0]
v_cvt_pk_bf16_f32 v92,v92,v93
v_cvt_pk_bf16_f32 v93,v94,v95
v_cvt_pk_bf16_f32 v94,v96,v97
v_cvt_pk_bf16_f32 v95,v98,v99
s_mov_b32 s91,0xf1000
buffer_store_dwordx4 v[92:95],v167,s[72:75],s91 offen sc1
v_pk_mul_f32 v[108:109],v[108:109],v[160:161] op_sel_hi:[1,0]
v_pk_mul_f32 v[110:111],v[110:111],v[160:161] op_sel_hi:[1,0]
v_cvt_pk_bf16_f32 v108,v108,v109
v_cvt_pk_bf16_f32 v109,v110,v111
s_waitcnt lgkmcnt(0)
s_barrier
ds_read_b64 v[102:103],v162 offset:1536
ds_read_b64 v[106:107],v162 offset:2048
ds_read_b64 v[110:111],v162 offset:2560
s_mov_b32 s91,0x180400
s_waitcnt lgkmcnt(2)
buffer_store_dwordx4 v[100:103],v167,s[72:75],s91 offen sc1
s_mov_b32 s91,0x180c00
s_waitcnt lgkmcnt(1)
buffer_store_dwordx4 v[104:107],v167,s[72:75],s91 offen sc1
s_mov_b32 s91,0x181400
s_waitcnt lgkmcnt(0)
buffer_store_dwordx4 v[108:111],v167,s[72:75],s91 offen sc1
v_mov_b32 v1,0x25800
ds_read_b128 v[168:171],v1
ds_read_b128 v[172:175],v1 offset:16
s_waitcnt lgkmcnt(1)
v_or_b32 v0,v168,v169
v_or3_b32 v0,v0,v170,v171
s_waitcnt lgkmcnt(0)
v_or3_b32 v0,v0,v172,v173
v_or3_b32 v0,v0,v174,v175
s_nop 0
v_readfirstlane_b32 s90,v0
s_nop 0
s_andn2_b32 s90,s90,s44
s_cbranch_scc0 W2_EPI
s_mov_b32 s44,1
v_mov_b32 v0,0xff800000
v_mov_b32 v1,0xff800000
v_mov_b32 v2,0xff800000
s_mov_b32 s98,0
v_mbcnt_lo_u32_b32 v168,-1,0
v_mbcnt_hi_u32_b32 v168,-1,v168
v_and_b32 v169,15,v168
v_lshrrev_b32 v170,4,v168
v_lshrrev_b32 v168,3,v169
s_movk_i32 s88,3072
v_mul_u32_u24 v244,s88,v168
v_and_b32 v168,7,v169
v_lshl_add_u32 v244,v168,7,v244
v_lshrrev_b32 v168,1,v169
v_xor_b32 v168,v168,v170
v_lshl_add_u32 v244,v168,4,v244
v_xor_b32 v245,64,v244
s_add_u32 s88,s51,s98
v_add_u32 v244,s88,v244
v_add_u32 v245,s88,v245
s_mov_b32 s94,0
s_mov_b32 s96,s48
W2_MAX:
s_waitcnt vmcnt(0) lgkmcnt(0)
s_barrier
s_mov_b32 m0,s96
s_nop 0
buffer_load_dwordx4 v251,s[64:67],s94 offen nt lds
s_add_u32 m0,s96,896
s_nop 0
buffer_load_dwordx4 v251,s[64:67],s94 offen offset:128 nt lds
s_add_u32 m0,s96,1792
s_nop 0
buffer_load_dwordx4 v251,s[64:67],s94 offen offset:256 nt lds
s_waitcnt vmcnt(0)
s_barrier
ds_read_b128 v[204:207],v244
ds_read_b128 v[208:211],v245
ds_read_b128 v[212:215],v244 offset:1024
ds_read_b128 v[216:219],v245 offset:1024
ds_read_b128 v[220:223],v244 offset:2048
ds_read_b128 v[224:227],v245 offset:2048
ds_read_b128 v[228:231],v250
s_waitcnt lgkmcnt(6)
v_mfma_f32_16x16x32_bf16 v[168:171],v[204:207],v[112:115],0
v_mfma_f32_16x16x32_bf16 v[172:175],v[204:207],v[136:139],0
s_waitcnt lgkmcnt(0)
v_mfma_f32_16x16x32_bf16 v[236:239],v[204:207],v[228:231],0
)A"
R"A(ds_read_b128 v[232:235],v250 offset:1024
v_mfma_f32_16x16x32_bf16 v[168:171],v[208:211],v[116:119],v[168:171]
v_mfma_f32_16x16x32_bf16 v[172:175],v[208:211],v[140:143],v[172:175]
s_waitcnt lgkmcnt(0)
v_mfma_f32_16x16x32_bf16 v[236:239],v[208:211],v[232:235],v[236:239]
ds_read_b128 v[228:231],v250 offset:2048
v_mfma_f32_16x16x32_bf16 v[168:171],v[212:215],v[120:123],v[168:171]
v_mfma_f32_16x16x32_bf16 v[172:175],v[212:215],v[144:147],v[172:175]
s_waitcnt lgkmcnt(0)
v_mfma_f32_16x16x32_bf16 v[236:239],v[212:215],v[228:231],v[236:239]
ds_read_b128 v[232:235],v250 offset:3072
v_mfma_f32_16x16x32_bf16 v[168:171],v[216:219],v[124:127],v[168:171]
v_mfma_f32_16x16x32_bf16 v[172:175],v[216:219],v[148:151],v[172:175]
s_waitcnt lgkmcnt(0)
v_mfma_f32_16x16x32_bf16 v[236:239],v[216:219],v[232:235],v[236:239]
ds_read_b128 v[228:231],v250 offset:4096
v_mfma_f32_16x16x32_bf16 v[168:171],v[220:223],v[128:131],v[168:171]
v_mfma_f32_16x16x32_bf16 v[172:175],v[220:223],v[152:155],v[172:175]
s_waitcnt lgkmcnt(0)
v_mfma_f32_16x16x32_bf16 v[236:239],v[220:223],v[228:231],v[236:239]
ds_read_b128 v[232:235],v250 offset:5120
v_mfma_f32_16x16x32_bf16 v[168:171],v[224:227],v[132:135],v[168:171]
v_mfma_f32_16x16x32_bf16 v[172:175],v[224:227],v[156:159],v[172:175]
s_waitcnt lgkmcnt(0)
v_mfma_f32_16x16x32_bf16 v[236:239],v[224:227],v[232:235],v[236:239]
s_nop 4
v_max3_f32 v0,v0,v168,v169
v_max3_f32 v0,v0,v170,v171
v_max3_f32 v1,v1,v172,v173
v_max3_f32 v1,v1,v174,v175
v_max3_f32 v2,v2,v236,v237
v_max3_f32 v2,v2,v238,v239
ds_read_b128 v[204:207],v244 offset:6144
ds_read_b128 v[208:211],v245 offset:6144
ds_read_b128 v[212:215],v244 offset:7168
ds_read_b128 v[216:219],v245 offset:7168
ds_read_b128 v[220:223],v244 offset:8192
ds_read_b128 v[224:227],v245 offset:8192
ds_read_b128 v[228:231],v250
s_waitcnt lgkmcnt(6)
v_mfma_f32_16x16x32_bf16 v[168:171],v[204:207],v[112:115],0
v_mfma_f32_16x16x32_bf16 v[172:175],v[204:207],v[136:139],0
s_waitcnt lgkmcnt(0)
v_mfma_f32_16x16x32_bf16 v[236:239],v[204:207],v[228:231],0
ds_read_b128 v[232:235],v250 offset:1024
v_mfma_f32_16x16x32_bf16 v[168:171],v[208:211],v[116:119],v[168:171]
v_mfma_f32_16x16x32_bf16 v[172:175],v[208:211],v[140:143],v[172:175]
s_waitcnt lgkmcnt(0)
v_mfma_f32_16x16x32_bf16 v[236:239],v[208:211],v[232:235],v[236:239]
ds_read_b128 v[228:231],v250 offset:2048
v_mfma_f32_16x16x32_bf16 v[168:171],v[212:215],v[120:123],v[168:171]
v_mfma_f32_16x16x32_bf16 v[172:175],v[212:215],v[144:147],v[172:175]
s_waitcnt lgkmcnt(0)
v_mfma_f32_16x16x32_bf16 v[236:239],v[212:215],v[228:231],v[236:239]
ds_read_b128 v[232:235],v250 offset:3072
v_mfma_f32_16x16x32_bf16 v[168:171],v[216:219],v[124:127],v[168:171]
v_mfma_f32_16x16x32_bf16 v[172:175],v[216:219],v[148:151],v[172:175]
s_waitcnt lgkmcnt(0)
v_mfma_f32_16x16x32_bf16 v[236:239],v[216:219],v[232:235],v[236:239]
ds_read_b128 v[228:231],v250 offset:4096
v_mfma_f32_16x16x32_bf16 v[168:171],v[220:223],v[128:131],v[168:171]
v_mfma_f32_16x16x32_bf16 v[172:175],v[220:223],v[152:155],v[172:175]
s_waitcnt lgkmcnt(0)
v_mfma_f32_16x16x32_bf16 v[236:239],v[220:223],v[228:231],v[236:239]
ds_read_b128 v[232:235],v250 offset:5120
v_mfma_f32_16x16x32_bf16 v[168:171],v[224:227],v[132:135],v[168:171]
v_mfma_f32_16x16x32_bf16 v[172:175],v[224:227],v[156:159],v[172:175]
s_waitcnt lgkmcnt(0)
v_mfma_f32_16x16x32_bf16 v[236:239],v[224:227],v[232:235],v[236:239]
s_nop 4
v_max3_f32 v0,v0,v168,v169
v_max3_f32 v0,v0,v170,v171
v_max3_f32 v1,v1,v172,v173
v_max3_f32 v1,v1,v174,v175
v_max3_f32 v2,v2,v236,v237
v_max3_f32 v2,v2,v238,v239
s_add_u32 s94,s94,12288
s_cmp_lt_u32 s94,196608
s_cbranch_scc1 W2_MAX
v_mov_b32 v3,v0
s_nop 1
v_permlane32_swap_b32 v0,v3
v_max_f32 v0,v0,v3
v_mov_b32 v3,v0
s_nop 1
v_permlane16_swap_b32 v0,v3
v_max_f32 v0,v0,v3
v_mov_b32 v3,v1
s_nop 1
v_permlane32_swap_b32 v1,v3
v_max_f32 v1,v1,v3
v_mov_b32 v3,v1
s_nop 1
v_permlane16_swap_b32 v1,v3
v_max_f32 v1,v1,v3
v_mov_b32 v3,v2
s_nop 1
v_permlane32_swap_b32 v2,v3
v_max_f32 v2,v2,v3
v_mov_b32 v3,v2
s_nop 1
v_permlane16_swap_b32 v2,v3
v_max_f32 v2,v2,v3
v_xor_b32 v160,0x80000000,v0
v_xor_b32 v164,0x80000000,v1
v_xor_b32 v161,0x80000000,v0
v_xor_b32 v165,0x80000000,v1
v_xor_b32 v162,0x80000000,v0
v_xor_b32 v166,0x80000000,v1
v_xor_b32 v163,0x80000000,v0
v_xor_b32 v167,0x80000000,v1
v_xor_b32 v240,0x80000000,v2
s_waitcnt vmcnt(0) lgkmcnt(0)
s_barrier
s_mul_i32 s98,s47,12288
s_add_u32 s96,s48,s98
s_mul_i32 s88,s35,12288
s_add_u32 s97,s88,s49
s_mov_b32 s94,0
s_mov_b32 s95,0
s_mov_b32 m0,s96
s_nop 0
buffer_load_dwordx4 v251,s[64:67],s94 offen nt lds
s_add_u32 m0,s96,896
s_nop 0
buffer_load_dwordx4 v251,s[64:67],s94 offen offset:128 nt lds
s_add_u32 m0,s96,1792
s_nop 0
buffer_load_dwordx4 v251,s[64:67],s94 offen offset:256 nt lds
s_mov_b32 m0,s97
s_nop 0
buffer_load_dwordx4 v252,s[68:71],s95 offen nt lds
s_add_u32 m0,s97,896
s_nop 0
buffer_load_dwordx4 v252,s[68:71],s95 offen offset:128 nt lds
s_add_u32 m0,s97,1792
s_nop 0
buffer_load_dwordx4 v252,s[68:71],s95 offen offset:256 nt lds
s_add_u32 s94,s94,12288
s_add_u32 s89,s48,36864
s_add_u32 s96,s96,12288
s_cmp_eq_u32 s96,s89
s_cselect_b32 s96,s48,s96
s_add_u32 s95,s95,12288
s_add_u32 s89,s49,36864
s_add_u32 s97,s97,12288
s_cmp_eq_u32 s97,s89
s_cselect_b32 s97,s49,s97
s_mov_b32 m0,s96
s_nop 0
buffer_load_dwordx4 v251,s[64:67],s94 offen nt lds
s_add_u32 m0,s96,896
s_nop 0
buffer_load_dwordx4 v251,s[64:67],s94 offen offset:128 nt lds
s_add_u32 m0,s96,1792
s_nop 0
buffer_load_dwordx4 v251,s[64:67],s94 offen offset:256 nt lds
s_add_u32 s94,s94,12288
s_add_u32 s89,s48,36864
s_add_u32 s96,s96,12288
s_cmp_eq_u32 s96,s89
s_cselect_b32 s96,s48,s96
v_mbcnt_lo_u32_b32 v0,-1,0
v_mbcnt_hi_u32_b32 v0,-1,v0
v_and_b32 v1,15,v0
v_lshrrev_b32 v2,4,v0
v_lshrrev_b32 v0,3,v1
s_movk_i32 s88,3072
v_mul_u32_u24 v244,s88,v0
v_and_b32 v0,7,v1
v_lshl_add_u32 v244,v0,7,v244
v_lshrrev_b32 v0,1,v1
v_xor_b32 v0,v0,v2
v_lshl_add_u32 v244,v0,4,v244
v_xor_b32 v245,64,v244
s_add_u32 s88,s51,s98
v_add_u32 v244,s88,v244
v_add_u32 v245,s88,v245
v_mbcnt_lo_u32_b32 v0,-1,0
v_mbcnt_hi_u32_b32 v0,-1,v0
v_and_b32 v1,15,v0
v_lshrrev_b32 v2,4,v0
v_lshlrev_b32 v250,4,v0
v_add_u32 v250,s50,v250
s_movk_i32 s88,3072
v_lshrrev_b32 v3,1,v2
v_mul_u32_u24 v246,s88,v3
v_and_b32 v2,1,v2
v_lshrrev_b32 v1,2,v1
v_lshl_add_u32 v3,v2,2,v1
v_lshl_add_u32 v246,v3,7,v246
v_lshrrev_b32 v1,1,v1
v_lshl_or_b32 v1,v2,1,v1
v_xor_b32 v1,2,v1
v_lshl_add_u32 v246,v1,5,v246
v_and_b32 v0,3,v0
v_lshl_add_u32 v246,v0,3,v246
v_xor_b32 v247,32,v246
v_xor_b32 v248,64,v246
v_xor_b32 v249,0x60,v246
s_mul_i32 s99,s35,12288
s_add_u32 s88,s99,s51
s_add_u32 s88,s88,73728
v_add_u32 v246,s88,v246
v_add_u32 v247,s88,v247
v_add_u32 v248,s88,v248
v_add_u32 v249,s88,v249
s_waitcnt vmcnt(6) lgkmcnt(0)
s_barrier
s_branch W2_ATT
W2_EPI:
s_nop 3
v_mbcnt_lo_u32_b32 v0,-1,0
v_mbcnt_hi_u32_b32 v0,-1,v0
v_lshlrev_b32 v0,4,v0
s_branch LNEXT
W3_START:
v_mbcnt_lo_u32_b32 v0,-1,0
v_mbcnt_hi_u32_b32 v0,-1,v0
v_and_b32 v1,15,v0
v_lshrrev_b32 v2,4,v0
v_lshrrev_b32 v0,3,v1
s_movk_i32 s88,3072
v_mul_u32_u24 v244,s88,v0
v_and_b32 v0,7,v1
v_lshl_add_u32 v244,v0,7,v244
v_lshrrev_b32 v0,1,v1
v_xor_b32 v0,v0,v2
v_lshl_add_u32 v244,v0,4,v244
v_xor_b32 v245,64,v244
s_add_u32 s88,s51,s98
v_add_u32 v244,s88,v244
v_add_u32 v245,s88,v245
v_mbcnt_lo_u32_b32 v0,-1,0
v_mbcnt_hi_u32_b32 v0,-1,v0
v_and_b32 v1,15,v0
v_lshrrev_b32 v2,4,v0
v_lshlrev_b32 v250,4,v0
v_add_u32 v250,s50,v250
s_movk_i32 s88,3072
v_lshrrev_b32 v3,1,v2
v_mul_u32_u24 v246,s88,v3
v_and_b32 v2,1,v2
v_lshrrev_b32 v1,2,v1
v_lshl_add_u32 v3,v2,2,v1
v_lshl_add_u32 v246,v3,7,v246
v_lshrrev_b32 v1,1,v1
v_lshl_or_b32 v1,v2,1,v1
v_xor_b32 v1,3,v1
v_lshl_add_u32 v246,v1,5,v246
v_and_b32 v0,3,v0
v_lshl_add_u32 v246,v0,3,v246
v_xor_b32 v247,32,v246
v_xor_b32 v248,64,v246
v_xor_b32 v249,0x60,v246
s_mul_i32 s99,s35,12288
s_add_u32 s88,s99,s51
s_add_u32 s88,s88,73728
v_add_u32 v246,s88,v246
v_add_u32 v247,s88,v247
v_add_u32 v248,s88,v248
v_add_u32 v249,s88,v249
s_xor_b32 s90,s85,2
s_mul_i32 s88,s90,184320
s_add_u32 s76,s36,s88
s_addc_u32 s77,s37,0
s_and_b32 s77,s77,0xffff
s_mov_b32 s78,0x2d000
s_mov_b32 s79,0x20000
s_mul_i32 s88,s86,384
s_sub_u32 s89,0x780000,s88
s_add_u32 s80,s42,s88
s_addc_u32 s81,s43,0
s_and_b32 s81,s81,0xffff
s_mov_b32 s82,s89
s_mov_b32 s83,0x20000
s_mov_b32 s44,0
s_waitcnt vmcnt(3) lgkmcnt(0)
s_barrier
s_cmp_eq_u32 s35,0
s_cselect_b32 s90,12288,0
s_add_u32 s90,s90,s51
s_add_u32 s90,s90,73728
s_add_u32 s91,s47,1
s_mul_i32 s91,s91,12288
s_add_u32 s91,s91,s51
s_cmp_eq_u32 s47,0
s_cselect_b32 s92,24576,0
s_add_u32 s92,s92,s51
v_mbcnt_lo_u32_b32 v0,-1,0
v_mbcnt_hi_u32_b32 v0,-1,v0
v_lshlrev_b32 v0,4,v0
v_add_u32 v1,s92,v0
ds_read_b128 v[112:115],v1
ds_read_b128 v[116:119],v1 offset:1024
ds_read_b128 v[120:123],v1 offset:2048
ds_read_b128 v[124:127],v1 offset:3072
ds_read_b128 v[128:131],v1 offset:4096
ds_read_b128 v[132:135],v1 offset:5120
v_add_u32 v2,s92,v0
ds_read_b128 v[136:139],v2 offset:6144
ds_read_b128 v[140:143],v2 offset:7168
ds_read_b128 v[144:147],v2 offset:8192
ds_read_b128 v[148:151],v2 offset:9216
ds_read_b128 v[152:155],v2 offset:10240
ds_read_b128 v[156:159],v2 offset:11264
ds_read_b128 v[4:7],v244
ds_read_b128 v[8:11],v245
ds_read_b128 v[12:15],v244 offset:1024
ds_read_b128 v[16:19],v245 offset:1024
ds_read_b128 v[20:23],v244 offset:2048
ds_read_b128 v[24:27],v245 offset:2048
ds_read_b128 v[28:31],v250
s_waitcnt lgkmcnt(6)
v_mfma_f32_16x16x32_bf16 v[36:39],v[4:7],v[112:115],0
v_mfma_f32_16x16x32_bf16 v[40:43],v[4:7],v[136:139],0
s_waitcnt lgkmcnt(0)
v_mfma_f32_16x16x32_bf16 v[44:47],v[4:7],v[28:31],0
ds_read_b128 v[32:35],v250 offset:1024
v_mfma_f32_16x16x32_bf16 v[36:39],v[8:11],v[116:119],v[36:39]
v_mfma_f32_16x16x32_bf16 v[40:43],v[8:11],v[140:143],v[40:43]
s_waitcnt lgkmcnt(0)
v_mfma_f32_16x16x32_bf16 v[44:47],v[8:11],v[32:35],v[44:47]
ds_read_b128 v[28:31],v250 offset:2048
v_mfma_f32_16x16x32_bf16 v[36:39],v[12:15],v[120:123],v[36:39]
v_mfma_f32_16x16x32_bf16 v[40:43],v[12:15],v[144:147],v[40:43]
s_waitcnt lgkmcnt(0)
v_mfma_f32_16x16x32_bf16 v[44:47],v[12:15],v[28:31],v[44:47]
ds_read_b128 v[32:35],v250 offset:3072
v_mfma_f32_16x16x32_bf16 v[36:39],v[16:19],v[124:127],v[36:39]
v_mfma_f32_16x16x32_bf16 v[40:43],v[16:19],v[148:151],v[40:43]
s_waitcnt lgkmcnt(0)
v_mfma_f32_16x16x32_bf16 v[44:47],v[16:19],v[32:35],v[44:47]
ds_read_b128 v[28:31],v250 offset:4096
v_mfma_f32_16x16x32_bf16 v[36:39],v[20:23],v[128:131],v[36:39]
v_mfma_f32_16x16x32_bf16 v[40:43],v[20:23],v[152:155],v[40:43]
s_waitcnt lgkmcnt(0)
v_mfma_f32_16x16x32_bf16 v[44:47],v[20:23],v[28:31],v[44:47]
ds_read_b128 v[32:35],v250 offset:5120
v_mfma_f32_16x16x32_bf16 v[36:39],v[24:27],v[132:135],v[36:39]
v_mfma_f32_16x16x32_bf16 v[40:43],v[24:27],v[156:159],v[40:43]
s_waitcnt lgkmcnt(0)
v_mfma_f32_16x16x32_bf16 v[44:47],v[24:27],v[32:35],v[44:47]
s_nop 4
v_max_f32 v0,v36,v37
v_max3_f32 v0,v0,v38,v39
v_max_f32 v1,v40,v41
v_max3_f32 v1,v1,v42,v43
v_max_f32 v2,v44,v45
v_max3_f32 v2,v2,v46,v47
v_mov_b32 v3,v0
s_nop 1
v_permlane32_swap_b32 v0,v3
v_max_f32 v0,v0,v3
v_mov_b32 v3,v0
s_nop 1
v_permlane16_swap_b32 v0,v3
v_max_f32 v0,v0,v3
v_mov_b32 v3,v1
s_nop 1
v_permlane32_swap_b32 v1,v3
v_max_f32 v1,v1,v3
v_mov_b32 v3,v1
s_nop 1
v_permlane16_swap_b32 v1,v3
v_max_f32 v1,v1,v3
v_mov_b32 v3,v2
s_nop 1
v_permlane32_swap_b32 v2,v3
v_max_f32 v2,v2,v3
v_mov_b32 v3,v2
s_nop 1
v_permlane16_swap_b32 v2,v3
v_max_f32 v2,v2,v3
v_xor_b32 v160,0x80000000,v0
v_xor_b32 v164,0x80000000,v1
v_xor_b32 v161,0x80000000,v0
v_xor_b32 v165,0x80000000,v1
v_xor_b32 v162,0x80000000,v0
v_xor_b32 v166,0x80000000,v1
v_xor_b32 v163,0x80000000,v0
v_xor_b32 v167,0x80000000,v1
v_xor_b32 v240,0x80000000,v2
s_waitcnt lgkmcnt(0)
s_barrier
s_mov_b32 m0,s96
s_nop 0
buffer_load_dwordx4 v251,s[64:67],s94 offen nt lds
s_add_u32 m0,s96,896
s_nop 0
buffer_load_dwordx4 v251,s[64:67],s94 offen offset:128 nt lds
s_add_u32 m0,s96,1792
s_nop 0
buffer_load_dwordx4 v251,s[64:67],s94 offen offset:256 nt lds
s_add_u32 s94,s94,12288
s_add_u32 s89,s48,36864
s_add_u32 s96,s96,12288
s_cmp_eq_u32 s96,s89
s_cselect_b32 s96,s48,s96
W3_ATT:
)A"
R"A(v_mov_b32 v36,0
v_mov_b32 v37,0
v_mov_b32 v38,0
v_mov_b32 v39,0
v_mov_b32 v40,0
v_mov_b32 v41,0
v_mov_b32 v42,0
v_mov_b32 v43,0
v_mov_b32 v44,0
v_mov_b32 v45,0
v_mov_b32 v46,0
v_mov_b32 v47,0
v_mov_b32 v48,0
v_mov_b32 v49,0
v_mov_b32 v50,0
v_mov_b32 v51,0
v_mov_b32 v84,0
v_mov_b32 v85,0
v_mov_b32 v86,0
v_mov_b32 v87,0
v_mov_b32 v88,0
v_mov_b32 v89,0
v_mov_b32 v90,0
v_mov_b32 v91,0
v_mov_b32 v92,0
v_mov_b32 v93,0
v_mov_b32 v94,0
v_mov_b32 v95,0
v_mov_b32 v96,0
v_mov_b32 v97,0
v_mov_b32 v98,0
v_mov_b32 v99,0
v_mov_b32 v108,0
v_mov_b32 v109,0
v_mov_b32 v110,0
v_mov_b32 v111,0
v_mov_b32 v241,0
v_mov_b32 v242,0
v_mov_b32 v243,0
ds_read_b128 v[220:223],v244
ds_read_b128 v[224:227],v245
ds_read_b128 v[204:207],v244 offset:1024
ds_read_b128 v[208:211],v245 offset:1024
ds_read_b128 v[212:215],v244 offset:2048
ds_read_b128 v[216:219],v245 offset:2048
s_waitcnt lgkmcnt(5)
v_mfma_f32_16x16x32_bf16 v[168:171],v[220:223],v[112:115],v[160:163]
s_mov_b32 m0,s96
v_mfma_f32_16x16x32_bf16 v[172:175],v[220:223],v[136:139],v[164:167]
ds_read_b128 v[220:223],v244 offset:6144
buffer_load_dwordx4 v251,s[64:67],s94 offen nt lds
s_waitcnt lgkmcnt(5)
v_mfma_f32_16x16x32_bf16 v[168:171],v[224:227],v[116:119],v[168:171]
v_mfma_f32_16x16x32_bf16 v[172:175],v[224:227],v[140:143],v[172:175]
ds_read_b128 v[224:227],v245 offset:6144
s_waitcnt lgkmcnt(5)
v_mfma_f32_16x16x32_bf16 v[168:171],v[204:207],v[120:123],v[168:171]
s_add_u32 m0,s96,896
v_mfma_f32_16x16x32_bf16 v[172:175],v[204:207],v[144:147],v[172:175]
ds_read_b128 v[204:207],v244 offset:7168
buffer_load_dwordx4 v251,s[64:67],s94 offen offset:128 nt lds
s_waitcnt lgkmcnt(5)
v_mfma_f32_16x16x32_bf16 v[168:171],v[208:211],v[124:127],v[168:171]
v_mfma_f32_16x16x32_bf16 v[172:175],v[208:211],v[148:151],v[172:175]
ds_read_b128 v[208:211],v245 offset:7168
s_waitcnt lgkmcnt(5)
v_mfma_f32_16x16x32_bf16 v[168:171],v[212:215],v[128:131],v[168:171]
s_add_u32 m0,s96,1792
v_mfma_f32_16x16x32_bf16 v[172:175],v[212:215],v[152:155],v[172:175]
ds_read_b128 v[212:215],v244 offset:8192
buffer_load_dwordx4 v251,s[64:67],s94 offen offset:256 nt lds
s_waitcnt lgkmcnt(5)
v_mfma_f32_16x16x32_bf16 v[168:171],v[216:219],v[132:135],v[168:171]
v_mfma_f32_16x16x32_bf16 v[172:175],v[216:219],v[156:159],v[172:175]
ds_read_b128 v[216:219],v245 offset:8192
s_waitcnt lgkmcnt(5)
v_mfma_f32_16x16x32_bf16 v[176:179],v[220:223],v[112:115],v[160:163]
s_mov_b32 m0,s97
v_mfma_f32_16x16x32_bf16 v[180:183],v[220:223],v[136:139],v[164:167]
buffer_load_dwordx4 v252,s[68:71],s95 offen nt lds
s_waitcnt lgkmcnt(4)
v_mfma_f32_16x16x32_bf16 v[176:179],v[224:227],v[116:119],v[176:179]
v_mfma_f32_16x16x32_bf16 v[180:183],v[224:227],v[140:143],v[180:183]
v_exp_f32 v168,v168
s_mov_b32 s90,s98
s_add_u32 s98,s98,12288
s_cmp_eq_u32 s98,36864
s_cmov_b32 s98,0
s_sub_u32 s46,s98,s90
v_add_u32 v244,s46,v244
v_add_u32 v245,s46,v245
s_waitcnt lgkmcnt(3)
v_mfma_f32_16x16x32_bf16 v[176:179],v[204:207],v[120:123],v[176:179]
v_exp_f32 v169,v169
s_add_u32 m0,s97,896
v_mfma_f32_16x16x32_bf16 v[180:183],v[204:207],v[144:147],v[180:183]
v_add_f32 v241,v241,v168
v_exp_f32 v170,v170
buffer_load_dwordx4 v252,s[68:71],s95 offen offset:128 nt lds
s_waitcnt lgkmcnt(2)
v_mfma_f32_16x16x32_bf16 v[176:179],v[208:211],v[124:127],v[176:179]
v_add_f32 v241,v241,v169
v_exp_f32 v171,v171
v_cvt_pk_bf16_f32 v184,v168,v169
v_mfma_f32_16x16x32_bf16 v[180:183],v[208:211],v[148:151],v[180:183]
v_add_f32 v241,v241,v170
v_exp_f32 v172,v172
s_waitcnt lgkmcnt(1)
v_mfma_f32_16x16x32_bf16 v[176:179],v[212:215],v[128:131],v[176:179]
v_add_f32 v241,v241,v171
v_exp_f32 v173,v173
v_cvt_pk_bf16_f32 v185,v170,v171
s_add_u32 m0,s97,1792
v_mfma_f32_16x16x32_bf16 v[180:183],v[212:215],v[152:155],v[180:183]
v_add_f32 v242,v242,v172
v_exp_f32 v174,v174
buffer_load_dwordx4 v252,s[68:71],s95 offen offset:256 nt lds
s_waitcnt lgkmcnt(0)
v_mfma_f32_16x16x32_bf16 v[176:179],v[216:219],v[132:135],v[176:179]
v_add_f32 v242,v242,v173
v_exp_f32 v175,v175
v_cvt_pk_bf16_f32 v188,v172,v173
v_mfma_f32_16x16x32_bf16 v[180:183],v[216:219],v[156:159],v[180:183]
v_add_f32 v242,v242,v174
v_add_f32 v242,v242,v175
v_cvt_pk_bf16_f32 v189,v174,v175
s_add_u32 s94,s94,12288
s_add_u32 s89,s48,36864
s_add_u32 s96,s96,12288
s_cmp_eq_u32 s96,s89
s_cselect_b32 s96,s48,s96
s_add_u32 s95,s95,12288
s_add_u32 s89,s49,36864
s_add_u32 s97,s97,12288
s_cmp_eq_u32 s97,s89
s_cselect_b32 s97,s49,s97
s_waitcnt vmcnt(6) lgkmcnt(0)
s_barrier
ds_read_b128 v[220:223],v244
ds_read_b128 v[224:227],v245
ds_read_b128 v[204:207],v244 offset:1024
ds_read_b128 v[208:211],v245 offset:1024
ds_read_b128 v[212:215],v244 offset:2048
ds_read_b128 v[216:219],v245 offset:2048
ds_read_b128 v[200:203],v250 offset:6144
v_exp_f32 v176,v176
s_waitcnt lgkmcnt(6)
v_mfma_f32_16x16x32_bf16 v[168:171],v[220:223],v[112:115],v[160:163]
v_exp_f32 v177,v177
s_mov_b32 m0,s96
v_mfma_f32_16x16x32_bf16 v[172:175],v[220:223],v[136:139],v[164:167]
ds_read_b128 v[220:223],v244 offset:6144
v_add_f32 v241,v241,v176
v_exp_f32 v178,v178
buffer_load_dwordx4 v251,s[64:67],s94 offen nt lds
s_waitcnt lgkmcnt(6)
v_mfma_f32_16x16x32_bf16 v[168:171],v[224:227],v[116:119],v[168:171]
v_add_f32 v241,v241,v177
v_exp_f32 v179,v179
v_cvt_pk_bf16_f32 v186,v176,v177
v_mfma_f32_16x16x32_bf16 v[172:175],v[224:227],v[140:143],v[172:175]
ds_read_b128 v[224:227],v245 offset:6144
v_add_f32 v241,v241,v178
v_exp_f32 v180,v180
s_waitcnt lgkmcnt(6)
v_mfma_f32_16x16x32_bf16 v[168:171],v[204:207],v[120:123],v[168:171]
v_add_f32 v241,v241,v179
v_exp_f32 v181,v181
v_cvt_pk_bf16_f32 v187,v178,v179
s_add_u32 m0,s96,896
v_mfma_f32_16x16x32_bf16 v[172:175],v[204:207],v[144:147],v[172:175]
ds_read_b128 v[204:207],v244 offset:7168
ds_read_b128 v[228:231],v250
ds_read_b128 v[232:235],v250 offset:1024
v_add_f32 v242,v242,v180
v_exp_f32 v182,v182
buffer_load_dwordx4 v251,s[64:67],s94 offen offset:128 nt lds
s_waitcnt lgkmcnt(8)
v_mfma_f32_16x16x32_bf16 v[168:171],v[208:211],v[124:127],v[168:171]
s_waitcnt lgkmcnt(5)
v_dot2c_f32_bf16 v243,s55,v200
v_add_f32 v242,v242,v181
v_exp_f32 v183,v183
v_cvt_pk_bf16_f32 v190,v180,v181
v_mfma_f32_16x16x32_bf16 v[172:175],v[208:211],v[148:151],v[172:175]
ds_read_b128 v[208:211],v245 offset:7168
v_add_f32 v242,v242,v182
v_mfma_f32_16x16x32_bf16 v[168:171],v[212:215],v[128:131],v[168:171]
v_dot2c_f32_bf16 v243,s55,v201
v_mov_b32 v236,v240
v_mov_b32 v237,v240
v_mov_b32 v238,v240
v_mov_b32 v239,v240
v_add_f32 v242,v242,v183
v_cvt_pk_bf16_f32 v191,v182,v183
s_add_u32 m0,s96,1792
v_mfma_f32_16x16x32_bf16 v[172:175],v[212:215],v[152:155],v[172:175]
ds_read_b128 v[212:215],v244 offset:8192
buffer_load_dwordx4 v251,s[64:67],s94 offen offset:256 nt lds
v_mfma_f32_16x16x32_bf16 v[168:171],v[216:219],v[132:135],v[168:171]
v_dot2c_f32_bf16 v243,s55,v202
v_mfma_f32_16x16x32_bf16 v[172:175],v[216:219],v[156:159],v[172:175]
ds_read_b128 v[216:219],v245 offset:8192
s_waitcnt lgkmcnt(7)
v_mfma_f32_16x16x32_bf16 v[176:179],v[220:223],v[112:115],v[160:163]
v_dot2c_f32_bf16 v243,s55,v203
s_mov_b32 m0,s97
v_mfma_f32_16x16x32_bf16 v[180:183],v[220:223],v[136:139],v[164:167]
buffer_load_dwordx4 v252,s[68:71],s95 offen nt lds
s_waitcnt lgkmcnt(4)
v_mfma_f32_16x16x32_bf16 v[236:239],v[220:223],v[228:231],v[236:239]
ds_read_b64_tr_b16 v[220:221],v246
ds_read_b64_tr_b16 v[222:223],v246 offset:6144
ds_read_b128 v[228:231],v250 offset:2048
v_mfma_f32_16x16x32_bf16 v[176:179],v[224:227],v[116:119],v[176:179]
v_exp_f32 v168,v168
v_mfma_f32_16x16x32_bf16 v[180:183],v[224:227],v[140:143],v[180:183]
v_exp_f32 v169,v169
s_add_u32 m0,s97,896
s_waitcnt lgkmcnt(6)
v_mfma_f32_16x16x32_bf16 v[236:239],v[224:227],v[232:235],v[236:239]
ds_read_b64_tr_b16 v[224:225],v247
ds_read_b64_tr_b16 v[226:227],v247 offset:6144
ds_read_b128 v[232:235],v250 offset:3072
v_add_f32 v241,v241,v168
v_exp_f32 v170,v170
buffer_load_dwordx4 v252,s[68:71],s95 offen offset:128 nt lds
s_mov_b32 s90,s98
s_add_u32 s98,s98,12288
s_cmp_eq_u32 s98,36864
s_cmov_b32 s98,0
s_sub_u32 s46,s98,s90
v_add_u32 v244,s46,v244
v_add_u32 v245,s46,v245
v_mfma_f32_16x16x32_bf16 v[176:179],v[204:207],v[120:123],v[176:179]
v_add_f32 v241,v241,v169
v_exp_f32 v171,v171
v_cvt_pk_bf16_f32 v192,v168,v169
v_mfma_f32_16x16x32_bf16 v[180:183],v[204:207],v[144:147],v[180:183]
v_add_f32 v241,v241,v170
v_exp_f32 v172,v172
s_waitcnt lgkmcnt(3)
v_mfma_f32_16x16x32_bf16 v[236:239],v[204:207],v[228:231],v[236:239]
ds_read_b64_tr_b16 v[204:205],v248
ds_read_b64_tr_b16 v[206:207],v248 offset:6144
ds_read_b128 v[228:231],v250 offset:4096
v_add_f32 v241,v241,v171
v_exp_f32 v173,v173
v_cvt_pk_bf16_f32 v193,v170,v171
s_add_u32 m0,s97,1792
v_mfma_f32_16x16x32_bf16 v[176:179],v[208:211],v[124:127],v[176:179]
v_add_f32 v242,v242,v172
v_exp_f32 v174,v174
buffer_load_dwordx4 v252,s[68:71],s95 offen offset:256 nt lds
v_mfma_f32_16x16x32_bf16 v[180:183],v[208:211],v[148:151],v[180:183]
v_add_f32 v242,v242,v173
v_exp_f32 v175,v175
v_cvt_pk_bf16_f32 v196,v172,v173
s_waitcnt lgkmcnt(3)
v_mfma_f32_16x16x32_bf16 v[236:239],v[208:211],v[232:235],v[236:239]
ds_read_b64_tr_b16 v[208:209],v249
ds_read_b64_tr_b16 v[210:211],v249 offset:6144
ds_read_b128 v[232:235],v250 offset:5120
v_add_f32 v242,v242,v174
v_mfma_f32_16x16x32_bf16 v[176:179],v[212:215],v[128:131],v[176:179]
v_add_f32 v242,v242,v175
v_cvt_pk_bf16_f32 v197,v174,v175
v_mfma_f32_16x16x32_bf16 v[180:183],v[212:215],v[152:155],v[180:183]
s_waitcnt lgkmcnt(3)
v_mfma_f32_16x16x32_bf16 v[236:239],v[212:215],v[228:231],v[236:239]
ds_read_b64_tr_b16 v[212:213],v246 offset:1024
ds_read_b64_tr_b16 v[214:215],v246 offset:7168
s_add_u32 s94,s94,12288
s_add_u32 s89,s48,36864
s_add_u32 s96,s96,12288
s_cmp_eq_u32 s96,s89
s_cselect_b32 s96,s48,s96
v_mfma_f32_16x16x32_bf16 v[176:179],v[216:219],v[132:135],v[176:179]
v_mfma_f32_16x16x32_bf16 v[180:183],v[216:219],v[156:159],v[180:183]
s_add_u32 s95,s95,12288
s_add_u32 s89,s49,36864
s_add_u32 s97,s97,12288
s_cmp_eq_u32 s97,s89
s_cselect_b32 s97,s49,s97
s_waitcnt lgkmcnt(2)
v_mfma_f32_16x16x32_bf16 v[236:239],v[216:219],v[232:235],v[236:239]
ds_read_b64_tr_b16 v[216:217],v247 offset:1024
ds_read_b64_tr_b16 v[218:219],v247 offset:7168
v_mfma_f32_16x16x32_bf16 v[4:7],v[220:223],v[184:187],0
v_mfma_f32_16x16x32_bf16 v[52:55],v[220:223],v[188:191],0
v_mfma_f32_16x16x32_bf16 v[100:103],v[220:223],v[200:203],0
ds_read_b64_tr_b16 v[220:221],v248 offset:1024
ds_read_b64_tr_b16 v[222:223],v248 offset:7168
v_mfma_f32_16x16x32_bf16 v[8:11],v[224:227],v[184:187],0
v_exp_f32 v236,v236
v_mfma_f32_16x16x32_bf16 v[56:59],v[224:227],v[188:191],0
ds_read_b64_tr_b16 v[224:225],v249 offset:1024
ds_read_b64_tr_b16 v[226:227],v249 offset:7168
v_exp_f32 v237,v237
v_mfma_f32_16x16x32_bf16 v[12:15],v[204:207],v[184:187],0
v_exp_f32 v238,v238
v_mfma_f32_16x16x32_bf16 v[60:63],v[204:207],v[188:191],0
ds_read_b64_tr_b16 v[204:205],v246 offset:2048
ds_read_b64_tr_b16 v[206:207],v246 offset:8192
v_exp_f32 v239,v239
v_mfma_f32_16x16x32_bf16 v[16:19],v[208:211],v[184:187],0
v_mfma_f32_16x16x32_bf16 v[64:67],v[208:211],v[188:191],0
ds_read_b64_tr_b16 v[208:209],v247 offset:2048
ds_read_b64_tr_b16 v[210:211],v247 offset:8192
v_cvt_pk_bf16_f32 v0,v236,v237
v_cvt_pk_bf16_f32 v1,v238,v239
s_waitcnt lgkmcnt(10)
v_mfma_f32_16x16x32_bf16 v[20:23],v[212:215],v[184:187],0
ds_write_b64 v250,v[0:1] offset:7176
v_mfma_f32_16x16x32_bf16 v[68:71],v[212:215],v[188:191],0
v_mfma_f32_16x16x32_bf16 v[104:107],v[212:215],v[200:203],0
ds_read_b64_tr_b16 v[212:213],v248 offset:2048
ds_read_b64_tr_b16 v[214:215],v248 offset:8192
s_waitcnt lgkmcnt(11)
v_mfma_f32_16x16x32_bf16 v[24:27],v[216:219],v[184:187],0
v_mfma_f32_16x16x32_bf16 v[72:75],v[216:219],v[188:191],0
ds_read_b64_tr_b16 v[216:217],v249 offset:2048
ds_read_b64_tr_b16 v[218:219],v249 offset:8192
s_waitcnt lgkmcnt(11)
v_mfma_f32_16x16x32_bf16 v[28:31],v[220:223],v[184:187],0
s_mov_b32 s90,s99
s_add_u32 s99,s99,12288
s_cmp_eq_u32 s99,36864
s_cmov_b32 s99,0
s_sub_u32 s46,s99,s90
v_add_u32 v246,s46,v246
v_add_u32 v247,s46,v247
v_add_u32 v248,s46,v248
v_add_u32 v249,s46,v249
v_mfma_f32_16x16x32_bf16 v[76:79],v[220:223],v[188:191],0
s_waitcnt lgkmcnt(9)
v_mfma_f32_16x16x32_bf16 v[32:35],v[224:227],v[184:187],0
v_mfma_f32_16x16x32_bf16 v[80:83],v[224:227],v[188:191],0
s_mov_b32 s45,6
W3_LOOP:
s_waitcnt vmcnt(6) lgkmcnt(0)
s_barrier
ds_read_b128 v[220:223],v244
ds_read_b128 v[224:227],v245
v_mfma_f32_16x16x32_bf16 v[36:39],v[204:207],v[184:187],v[36:39]
v_exp_f32 v176,v176
s_mov_b32 m0,s96
v_mfma_f32_16x16x32_bf16 v[84:87],v[204:207],v[188:191],v[84:87]
v_exp_f32 v177,v177
buffer_load_dwordx4 v251,s[64:67],s94 offen nt lds
v_mfma_f32_16x16x32_bf16 v[108:111],v[204:207],v[200:203],v[108:111]
ds_read_b128 v[204:207],v244 offset:1024
v_add_f32 v241,v241,v176
v_exp_f32 v178,v178
v_mfma_f32_16x16x32_bf16 v[40:43],v[208:211],v[184:187],v[40:43]
v_add_f32 v241,v241,v177
v_exp_f32 v179,v179
v_cvt_pk_bf16_f32 v194,v176,v177
v_mfma_f32_16x16x32_bf16 v[88:91],v[208:211],v[188:191],v[88:91]
ds_read_b128 v[208:211],v245 offset:1024
ds_read_b128 v[200:203],v250 offset:7168
v_add_f32 v241,v241,v178
v_exp_f32 v180,v180
s_add_u32 m0,s96,896
v_mfma_f32_16x16x32_bf16 v[44:47],v[212:215],v[184:187],v[44:47]
v_add_f32 v241,v241,v179
v_exp_f32 v181,v181
v_cvt_pk_bf16_f32 v195,v178,v179
buffer_load_dwordx4 v251,s[64:67],s94 offen offset:128 nt lds
v_mfma_f32_16x16x32_bf16 v[92:95],v[212:215],v[188:191],v[92:95]
ds_read_b128 v[212:215],v244 offset:2048
v_add_f32 v242,v242,v180
v_exp_f32 v182,v182
v_mfma_f32_16x16x32_bf16 v[48:51],v[216:219],v[184:187],v[48:51]
v_add_f32 v242,v242,v181
v_exp_f32 v183,v183
v_cvt_pk_bf16_f32 v198,v180,v181
v_mfma_f32_16x16x32_bf16 v[96:99],v[216:219],v[188:191],v[96:99]
ds_read_b128 v[216:219],v245 offset:2048
v_add_f32 v242,v242,v182
s_add_u32 m0,s96,1792
s_waitcnt lgkmcnt(6)
v_mfma_f32_16x16x32_bf16 v[168:171],v[220:223],v[112:115],v[160:163]
v_add_f32 v242,v242,v183
v_cvt_pk_bf16_f32 v199,v182,v183
buffer_load_dwordx4 v251,s[64:67],s94 offen offset:256 nt lds
v_mfma_f32_16x16x32_bf16 v[172:175],v[220:223],v[136:139],v[164:167]
ds_read_b128 v[220:223],v244 offset:6144
s_waitcnt lgkmcnt(6)
v_mfma_f32_16x16x32_bf16 v[168:171],v[224:227],v[116:119],v[168:171]
v_mfma_f32_16x16x32_bf16 v[172:175],v[224:227],v[140:143],v[172:175]
ds_read_b128 v[224:227],v245 offset:6144
s_mov_b32 m0,s97
s_waitcnt lgkmcnt(6)
v_mfma_f32_16x16x32_bf16 v[168:171],v[204:207],v[120:123],v[168:171]
buffer_load_dwordx4 v252,s[68:71],s95 offen nt lds
v_mfma_f32_16x16x32_bf16 v[172:175],v[204:207],v[144:147],v[172:175]
ds_read_b128 v[204:207],v244 offset:7168
s_waitcnt lgkmcnt(6)
v_mfma_f32_16x16x32_bf16 v[168:171],v[208:211],v[124:127],v[168:171]
)A"
R"A(s_waitcnt lgkmcnt(5)
v_dot2c_f32_bf16 v243,s55,v200
v_mfma_f32_16x16x32_bf16 v[172:175],v[208:211],v[148:151],v[172:175]
ds_read_b128 v[208:211],v245 offset:7168
s_add_u32 m0,s97,896
s_waitcnt lgkmcnt(5)
v_mfma_f32_16x16x32_bf16 v[168:171],v[212:215],v[128:131],v[168:171]
v_dot2c_f32_bf16 v243,s55,v201
buffer_load_dwordx4 v252,s[68:71],s95 offen offset:128 nt lds
v_mfma_f32_16x16x32_bf16 v[172:175],v[212:215],v[152:155],v[172:175]
ds_read_b128 v[212:215],v244 offset:8192
s_waitcnt lgkmcnt(5)
v_mfma_f32_16x16x32_bf16 v[168:171],v[216:219],v[132:135],v[168:171]
v_dot2c_f32_bf16 v243,s55,v202
v_mfma_f32_16x16x32_bf16 v[172:175],v[216:219],v[156:159],v[172:175]
ds_read_b128 v[216:219],v245 offset:8192
s_add_u32 m0,s97,1792
s_waitcnt lgkmcnt(5)
v_mfma_f32_16x16x32_bf16 v[176:179],v[220:223],v[112:115],v[160:163]
v_dot2c_f32_bf16 v243,s55,v203
buffer_load_dwordx4 v252,s[68:71],s95 offen offset:256 nt lds
v_mfma_f32_16x16x32_bf16 v[180:183],v[220:223],v[136:139],v[164:167]
ds_read_b64_tr_b16 v[220:221],v246
ds_read_b64_tr_b16 v[222:223],v246 offset:6144
s_waitcnt lgkmcnt(6)
v_mfma_f32_16x16x32_bf16 v[176:179],v[224:227],v[116:119],v[176:179]
v_mfma_f32_16x16x32_bf16 v[180:183],v[224:227],v[140:143],v[180:183]
ds_read_b64_tr_b16 v[224:225],v247
ds_read_b64_tr_b16 v[226:227],v247 offset:6144
v_exp_f32 v168,v168
s_mov_b32 s90,s98
s_add_u32 s98,s98,12288
s_cmp_eq_u32 s98,36864
s_cmov_b32 s98,0
s_sub_u32 s46,s98,s90
v_add_u32 v244,s46,v244
v_add_u32 v245,s46,v245
s_waitcnt lgkmcnt(7)
v_mfma_f32_16x16x32_bf16 v[176:179],v[204:207],v[120:123],v[176:179]
v_exp_f32 v169,v169
v_mfma_f32_16x16x32_bf16 v[180:183],v[204:207],v[144:147],v[180:183]
ds_read_b64_tr_b16 v[204:205],v248
ds_read_b64_tr_b16 v[206:207],v248 offset:6144
v_add_f32 v241,v241,v168
v_exp_f32 v170,v170
s_add_u32 s94,s94,12288
s_add_u32 s89,s48,36864
s_add_u32 s96,s96,12288
s_cmp_eq_u32 s96,s89
s_cselect_b32 s96,s48,s96
s_waitcnt lgkmcnt(8)
v_mfma_f32_16x16x32_bf16 v[176:179],v[208:211],v[124:127],v[176:179]
v_add_f32 v241,v241,v169
v_exp_f32 v171,v171
v_cvt_pk_bf16_f32 v184,v168,v169
v_mfma_f32_16x16x32_bf16 v[180:183],v[208:211],v[148:151],v[180:183]
ds_read_b64_tr_b16 v[208:209],v249
ds_read_b64_tr_b16 v[210:211],v249 offset:6144
v_add_f32 v241,v241,v170
v_exp_f32 v172,v172
s_add_u32 s95,s95,12288
s_add_u32 s89,s49,36864
s_add_u32 s97,s97,12288
s_cmp_eq_u32 s97,s89
s_cselect_b32 s97,s49,s97
s_waitcnt lgkmcnt(9)
v_mfma_f32_16x16x32_bf16 v[176:179],v[212:215],v[128:131],v[176:179]
v_add_f32 v241,v241,v171
v_exp_f32 v173,v173
v_cvt_pk_bf16_f32 v185,v170,v171
v_mfma_f32_16x16x32_bf16 v[180:183],v[212:215],v[152:155],v[180:183]
ds_read_b64_tr_b16 v[212:213],v246 offset:1024
ds_read_b64_tr_b16 v[214:215],v246 offset:7168
v_add_f32 v242,v242,v172
v_exp_f32 v174,v174
s_sub_u32 s45,s45,1
s_waitcnt lgkmcnt(10)
v_mfma_f32_16x16x32_bf16 v[176:179],v[216:219],v[132:135],v[176:179]
v_add_f32 v242,v242,v173
v_exp_f32 v175,v175
v_cvt_pk_bf16_f32 v188,v172,v173
v_mfma_f32_16x16x32_bf16 v[180:183],v[216:219],v[156:159],v[180:183]
ds_read_b64_tr_b16 v[216:217],v247 offset:1024
ds_read_b64_tr_b16 v[218:219],v247 offset:7168
v_add_f32 v242,v242,v174
s_waitcnt lgkmcnt(10)
v_mfma_f32_16x16x32_bf16 v[4:7],v[220:223],v[192:195],v[4:7]
v_add_f32 v242,v242,v175
v_cvt_pk_bf16_f32 v189,v174,v175
v_mfma_f32_16x16x32_bf16 v[52:55],v[220:223],v[196:199],v[52:55]
v_mfma_f32_16x16x32_bf16 v[100:103],v[220:223],v[200:203],v[100:103]
ds_read_b64_tr_b16 v[220:221],v248 offset:1024
ds_read_b64_tr_b16 v[222:223],v248 offset:7168
s_waitcnt lgkmcnt(10)
v_mfma_f32_16x16x32_bf16 v[8:11],v[224:227],v[192:195],v[8:11]
v_mfma_f32_16x16x32_bf16 v[56:59],v[224:227],v[196:199],v[56:59]
ds_read_b64_tr_b16 v[224:225],v249 offset:1024
ds_read_b64_tr_b16 v[226:227],v249 offset:7168
s_waitcnt lgkmcnt(10)
v_mfma_f32_16x16x32_bf16 v[12:15],v[204:207],v[192:195],v[12:15]
v_mfma_f32_16x16x32_bf16 v[60:63],v[204:207],v[196:199],v[60:63]
ds_read_b64_tr_b16 v[204:205],v246 offset:2048
ds_read_b64_tr_b16 v[206:207],v246 offset:8192
s_waitcnt lgkmcnt(10)
v_mfma_f32_16x16x32_bf16 v[16:19],v[208:211],v[192:195],v[16:19]
v_mfma_f32_16x16x32_bf16 v[64:67],v[208:211],v[196:199],v[64:67]
ds_read_b64_tr_b16 v[208:209],v247 offset:2048
ds_read_b64_tr_b16 v[210:211],v247 offset:8192
s_waitcnt lgkmcnt(10)
v_mfma_f32_16x16x32_bf16 v[20:23],v[212:215],v[192:195],v[20:23]
v_mfma_f32_16x16x32_bf16 v[68:71],v[212:215],v[196:199],v[68:71]
v_mfma_f32_16x16x32_bf16 v[104:107],v[212:215],v[200:203],v[104:107]
ds_read_b64_tr_b16 v[212:213],v248 offset:2048
ds_read_b64_tr_b16 v[214:215],v248 offset:8192
s_waitcnt lgkmcnt(10)
v_mfma_f32_16x16x32_bf16 v[24:27],v[216:219],v[192:195],v[24:27]
v_mfma_f32_16x16x32_bf16 v[72:75],v[216:219],v[196:199],v[72:75]
ds_read_b64_tr_b16 v[216:217],v249 offset:2048
ds_read_b64_tr_b16 v[218:219],v249 offset:8192
s_waitcnt lgkmcnt(10)
v_mfma_f32_16x16x32_bf16 v[28:31],v[220:223],v[192:195],v[28:31]
s_mov_b32 s90,s99
s_add_u32 s99,s99,12288
s_cmp_eq_u32 s99,36864
s_cmov_b32 s99,0
s_sub_u32 s46,s99,s90
v_add_u32 v246,s46,v246
v_add_u32 v247,s46,v247
v_add_u32 v248,s46,v248
v_add_u32 v249,s46,v249
v_mfma_f32_16x16x32_bf16 v[76:79],v[220:223],v[196:199],v[76:79]
s_waitcnt lgkmcnt(8)
v_mfma_f32_16x16x32_bf16 v[32:35],v[224:227],v[192:195],v[32:35]
v_mfma_f32_16x16x32_bf16 v[80:83],v[224:227],v[196:199],v[80:83]
s_waitcnt vmcnt(6) lgkmcnt(0)
s_barrier
ds_read_b128 v[220:223],v244
ds_read_b128 v[224:227],v245
v_mfma_f32_16x16x32_bf16 v[36:39],v[204:207],v[192:195],v[36:39]
v_exp_f32 v176,v176
s_mov_b32 m0,s96
v_mfma_f32_16x16x32_bf16 v[84:87],v[204:207],v[196:199],v[84:87]
v_exp_f32 v177,v177
buffer_load_dwordx4 v251,s[64:67],s94 offen nt lds
v_mfma_f32_16x16x32_bf16 v[108:111],v[204:207],v[200:203],v[108:111]
ds_read_b128 v[204:207],v244 offset:1024
v_add_f32 v241,v241,v176
v_exp_f32 v178,v178
v_mfma_f32_16x16x32_bf16 v[40:43],v[208:211],v[192:195],v[40:43]
v_add_f32 v241,v241,v177
v_exp_f32 v179,v179
v_cvt_pk_bf16_f32 v186,v176,v177
v_mfma_f32_16x16x32_bf16 v[88:91],v[208:211],v[196:199],v[88:91]
ds_read_b128 v[208:211],v245 offset:1024
ds_read_b128 v[200:203],v250 offset:6144
v_add_f32 v241,v241,v178
v_exp_f32 v180,v180
s_add_u32 m0,s96,896
v_mfma_f32_16x16x32_bf16 v[44:47],v[212:215],v[192:195],v[44:47]
v_add_f32 v241,v241,v179
v_exp_f32 v181,v181
v_cvt_pk_bf16_f32 v187,v178,v179
buffer_load_dwordx4 v251,s[64:67],s94 offen offset:128 nt lds
v_mfma_f32_16x16x32_bf16 v[92:95],v[212:215],v[196:199],v[92:95]
ds_read_b128 v[212:215],v244 offset:2048
v_add_f32 v242,v242,v180
v_exp_f32 v182,v182
v_mfma_f32_16x16x32_bf16 v[48:51],v[216:219],v[192:195],v[48:51]
v_add_f32 v242,v242,v181
v_exp_f32 v183,v183
v_cvt_pk_bf16_f32 v190,v180,v181
v_mfma_f32_16x16x32_bf16 v[96:99],v[216:219],v[196:199],v[96:99]
ds_read_b128 v[216:219],v245 offset:2048
v_add_f32 v242,v242,v182
s_add_u32 m0,s96,1792
s_waitcnt lgkmcnt(6)
v_mfma_f32_16x16x32_bf16 v[168:171],v[220:223],v[112:115],v[160:163]
v_add_f32 v242,v242,v183
v_cvt_pk_bf16_f32 v191,v182,v183
buffer_load_dwordx4 v251,s[64:67],s94 offen offset:256 nt lds
v_mfma_f32_16x16x32_bf16 v[172:175],v[220:223],v[136:139],v[164:167]
ds_read_b128 v[220:223],v244 offset:6144
s_waitcnt lgkmcnt(6)
v_mfma_f32_16x16x32_bf16 v[168:171],v[224:227],v[116:119],v[168:171]
v_mfma_f32_16x16x32_bf16 v[172:175],v[224:227],v[140:143],v[172:175]
ds_read_b128 v[224:227],v245 offset:6144
s_mov_b32 m0,s97
s_waitcnt lgkmcnt(6)
v_mfma_f32_16x16x32_bf16 v[168:171],v[204:207],v[120:123],v[168:171]
buffer_load_dwordx4 v252,s[68:71],s95 offen nt lds
v_mfma_f32_16x16x32_bf16 v[172:175],v[204:207],v[144:147],v[172:175]
ds_read_b128 v[204:207],v244 offset:7168
ds_read_b128 v[228:231],v250
ds_read_b128 v[232:235],v250 offset:1024
s_waitcnt lgkmcnt(8)
v_mfma_f32_16x16x32_bf16 v[168:171],v[208:211],v[124:127],v[168:171]
s_waitcnt lgkmcnt(7)
v_dot2c_f32_bf16 v243,s55,v200
v_mfma_f32_16x16x32_bf16 v[172:175],v[208:211],v[148:151],v[172:175]
ds_read_b128 v[208:211],v245 offset:7168
s_add_u32 m0,s97,896
s_waitcnt lgkmcnt(7)
v_mfma_f32_16x16x32_bf16 v[168:171],v[212:215],v[128:131],v[168:171]
v_dot2c_f32_bf16 v243,s55,v201
v_mov_b32 v236,v240
v_mov_b32 v237,v240
v_mov_b32 v238,v240
v_mov_b32 v239,v240
buffer_load_dwordx4 v252,s[68:71],s95 offen offset:128 nt lds
v_mfma_f32_16x16x32_bf16 v[172:175],v[212:215],v[152:155],v[172:175]
ds_read_b128 v[212:215],v244 offset:8192
s_waitcnt lgkmcnt(7)
v_mfma_f32_16x16x32_bf16 v[168:171],v[216:219],v[132:135],v[168:171]
v_dot2c_f32_bf16 v243,s55,v202
v_mfma_f32_16x16x32_bf16 v[172:175],v[216:219],v[156:159],v[172:175]
ds_read_b128 v[216:219],v245 offset:8192
s_add_u32 m0,s97,1792
s_waitcnt lgkmcnt(7)
v_mfma_f32_16x16x32_bf16 v[176:179],v[220:223],v[112:115],v[160:163]
v_dot2c_f32_bf16 v243,s55,v203
buffer_load_dwordx4 v252,s[68:71],s95 offen offset:256 nt lds
v_mfma_f32_16x16x32_bf16 v[180:183],v[220:223],v[136:139],v[164:167]
s_waitcnt lgkmcnt(4)
v_mfma_f32_16x16x32_bf16 v[236:239],v[220:223],v[228:231],v[236:239]
ds_read_b64_tr_b16 v[220:221],v246
ds_read_b64_tr_b16 v[222:223],v246 offset:6144
ds_read_b128 v[228:231],v250 offset:2048
v_mfma_f32_16x16x32_bf16 v[176:179],v[224:227],v[116:119],v[176:179]
v_exp_f32 v168,v168
v_mfma_f32_16x16x32_bf16 v[180:183],v[224:227],v[140:143],v[180:183]
v_exp_f32 v169,v169
s_waitcnt lgkmcnt(6)
v_mfma_f32_16x16x32_bf16 v[236:239],v[224:227],v[232:235],v[236:239]
ds_read_b64_tr_b16 v[224:225],v247
ds_read_b64_tr_b16 v[226:227],v247 offset:6144
ds_read_b128 v[232:235],v250 offset:3072
v_add_f32 v241,v241,v168
v_exp_f32 v170,v170
s_add_u32 s94,s94,12288
s_add_u32 s89,s48,36864
s_add_u32 s96,s96,12288
s_cmp_eq_u32 s96,s89
s_cselect_b32 s96,s48,s96
s_mov_b32 s90,s98
s_add_u32 s98,s98,12288
s_cmp_eq_u32 s98,36864
s_cmov_b32 s98,0
s_sub_u32 s46,s98,s90
v_add_u32 v244,s46,v244
v_add_u32 v245,s46,v245
v_mfma_f32_16x16x32_bf16 v[176:179],v[204:207],v[120:123],v[176:179]
v_add_f32 v241,v241,v169
v_exp_f32 v171,v171
v_cvt_pk_bf16_f32 v192,v168,v169
v_mfma_f32_16x16x32_bf16 v[180:183],v[204:207],v[144:147],v[180:183]
v_add_f32 v241,v241,v170
v_exp_f32 v172,v172
s_add_u32 s95,s95,12288
s_add_u32 s89,s49,36864
s_add_u32 s97,s97,12288
s_cmp_eq_u32 s97,s89
s_cselect_b32 s97,s49,s97
s_waitcnt lgkmcnt(3)
v_mfma_f32_16x16x32_bf16 v[236:239],v[204:207],v[228:231],v[236:239]
ds_read_b64_tr_b16 v[204:205],v248
ds_read_b64_tr_b16 v[206:207],v248 offset:6144
ds_read_b128 v[228:231],v250 offset:4096
v_add_f32 v241,v241,v171
v_exp_f32 v173,v173
v_cvt_pk_bf16_f32 v193,v170,v171
v_mfma_f32_16x16x32_bf16 v[176:179],v[208:211],v[124:127],v[176:179]
v_add_f32 v242,v242,v172
v_exp_f32 v174,v174
v_mfma_f32_16x16x32_bf16 v[180:183],v[208:211],v[148:151],v[180:183]
v_add_f32 v242,v242,v173
v_exp_f32 v175,v175
v_cvt_pk_bf16_f32 v196,v172,v173
s_waitcnt lgkmcnt(3)
v_mfma_f32_16x16x32_bf16 v[236:239],v[208:211],v[232:235],v[236:239]
ds_read_b64_tr_b16 v[208:209],v249
ds_read_b64_tr_b16 v[210:211],v249 offset:6144
ds_read_b128 v[232:235],v250 offset:5120
v_add_f32 v242,v242,v174
v_mfma_f32_16x16x32_bf16 v[176:179],v[212:215],v[128:131],v[176:179]
v_add_f32 v242,v242,v175
v_cvt_pk_bf16_f32 v197,v174,v175
v_mfma_f32_16x16x32_bf16 v[180:183],v[212:215],v[152:155],v[180:183]
s_waitcnt lgkmcnt(3)
v_mfma_f32_16x16x32_bf16 v[236:239],v[212:215],v[228:231],v[236:239]
ds_read_b64_tr_b16 v[212:213],v246 offset:1024
ds_read_b64_tr_b16 v[214:215],v246 offset:7168
v_mfma_f32_16x16x32_bf16 v[176:179],v[216:219],v[132:135],v[176:179]
v_mfma_f32_16x16x32_bf16 v[180:183],v[216:219],v[156:159],v[180:183]
s_waitcnt lgkmcnt(2)
v_mfma_f32_16x16x32_bf16 v[236:239],v[216:219],v[232:235],v[236:239]
ds_read_b64_tr_b16 v[216:217],v247 offset:1024
ds_read_b64_tr_b16 v[218:219],v247 offset:7168
v_mfma_f32_16x16x32_bf16 v[4:7],v[220:223],v[184:187],v[4:7]
v_mfma_f32_16x16x32_bf16 v[52:55],v[220:223],v[188:191],v[52:55]
v_mfma_f32_16x16x32_bf16 v[100:103],v[220:223],v[200:203],v[100:103]
ds_read_b64_tr_b16 v[220:221],v248 offset:1024
ds_read_b64_tr_b16 v[222:223],v248 offset:7168
v_mfma_f32_16x16x32_bf16 v[8:11],v[224:227],v[184:187],v[8:11]
v_exp_f32 v236,v236
v_mfma_f32_16x16x32_bf16 v[56:59],v[224:227],v[188:191],v[56:59]
ds_read_b64_tr_b16 v[224:225],v249 offset:1024
ds_read_b64_tr_b16 v[226:227],v249 offset:7168
v_exp_f32 v237,v237
v_mfma_f32_16x16x32_bf16 v[12:15],v[204:207],v[184:187],v[12:15]
v_exp_f32 v238,v238
v_mfma_f32_16x16x32_bf16 v[60:63],v[204:207],v[188:191],v[60:63]
ds_read_b64_tr_b16 v[204:205],v246 offset:2048
ds_read_b64_tr_b16 v[206:207],v246 offset:8192
v_exp_f32 v239,v239
v_mfma_f32_16x16x32_bf16 v[16:19],v[208:211],v[184:187],v[16:19]
v_mfma_f32_16x16x32_bf16 v[64:67],v[208:211],v[188:191],v[64:67]
ds_read_b64_tr_b16 v[208:209],v247 offset:2048
ds_read_b64_tr_b16 v[210:211],v247 offset:8192
v_cvt_pk_bf16_f32 v0,v236,v237
v_cvt_pk_bf16_f32 v1,v238,v239
s_waitcnt lgkmcnt(10)
v_mfma_f32_16x16x32_bf16 v[20:23],v[212:215],v[184:187],v[20:23]
ds_write_b64 v250,v[0:1] offset:7176
v_mfma_f32_16x16x32_bf16 v[68:71],v[212:215],v[188:191],v[68:71]
v_mfma_f32_16x16x32_bf16 v[104:107],v[212:215],v[200:203],v[104:107]
ds_read_b64_tr_b16 v[212:213],v248 offset:2048
ds_read_b64_tr_b16 v[214:215],v248 offset:8192
s_waitcnt lgkmcnt(11)
v_mfma_f32_16x16x32_bf16 v[24:27],v[216:219],v[184:187],v[24:27]
v_mfma_f32_16x16x32_bf16 v[72:75],v[216:219],v[188:191],v[72:75]
ds_read_b64_tr_b16 v[216:217],v249 offset:2048
ds_read_b64_tr_b16 v[218:219],v249 offset:8192
s_waitcnt lgkmcnt(11)
v_mfma_f32_16x16x32_bf16 v[28:31],v[220:223],v[184:187],v[28:31]
s_mov_b32 s90,s99
s_add_u32 s99,s99,12288
s_cmp_eq_u32 s99,36864
s_cmov_b32 s99,0
s_sub_u32 s46,s99,s90
v_add_u32 v246,s46,v246
v_add_u32 v247,s46,v247
v_add_u32 v248,s46,v248
v_add_u32 v249,s46,v249
v_mfma_f32_16x16x32_bf16 v[76:79],v[220:223],v[188:191],v[76:79]
s_waitcnt lgkmcnt(9)
v_mfma_f32_16x16x32_bf16 v[32:35],v[224:227],v[184:187],v[32:35]
v_mfma_f32_16x16x32_bf16 v[80:83],v[224:227],v[188:191],v[80:83]
s_cmp_lg_u32 s45,0
s_cbranch_scc1 W3_LOOP
s_cmp_eq_u32 s47,0
s_cselect_b32 s94,5,3
s_mul_i32 s94,s94,393216
s_waitcnt vmcnt(6) lgkmcnt(0)
s_barrier
ds_read_b128 v[220:223],v244
ds_read_b128 v[224:227],v245
v_mfma_f32_16x16x32_bf16 v[36:39],v[204:207],v[184:187],v[36:39]
v_exp_f32 v176,v176
s_mov_b32 m0,s96
v_mfma_f32_16x16x32_bf16 v[84:87],v[204:207],v[188:191],v[84:87]
v_exp_f32 v177,v177
s_cmp_lg_u32 s34,1
s_cbranch_scc1 LG56
buffer_load_dwordx4 v253,s[80:83],s94 offen lds
LG56:
v_mfma_f32_16x16x32_bf16 v[108:111],v[204:207],v[200:203],v[108:111]
ds_read_b128 v[204:207],v244 offset:1024
v_add_f32 v241,v241,v176
v_exp_f32 v178,v178
v_mfma_f32_16x16x32_bf16 v[40:43],v[208:211],v[184:187],v[40:43]
v_add_f32 v241,v241,v177
v_exp_f32 v179,v179
v_cvt_pk_bf16_f32 v194,v176,v177
v_mfma_f32_16x16x32_bf16 v[88:91],v[208:211],v[188:191],v[88:91]
ds_read_b128 v[208:211],v245 offset:1024
ds_read_b128 v[200:203],v250 offset:7168
v_add_f32 v241,v241,v178
v_exp_f32 v180,v180
s_add_u32 m0,s96,896
v_mfma_f32_16x16x32_bf16 v[44:47],v[212:215],v[184:187],v[44:47]
v_add_f32 v241,v241,v179
v_exp_f32 v181,v181
v_cvt_pk_bf16_f32 v195,v178,v179
s_cmp_lg_u32 s34,1
s_cbranch_scc1 LG57
buffer_load_dwordx4 v253,s[80:83],s94 offen offset:128 lds
LG57:
v_mfma_f32_16x16x32_bf16 v[92:95],v[212:215],v[188:191],v[92:95]
ds_read_b128 v[212:215],v244 offset:2048
v_add_f32 v242,v242,v180
v_exp_f32 v182,v182
v_mfma_f32_16x16x32_bf16 v[48:51],v[216:219],v[184:187],v[48:51]
v_add_f32 v242,v242,v181
v_exp_f32 v183,v183
v_cvt_pk_bf16_f32 v198,v180,v181
v_mfma_f32_16x16x32_bf16 v[96:99],v[216:219],v[188:191],v[96:99]
ds_read_b128 v[216:219],v245 offset:2048
v_add_f32 v242,v242,v182
s_add_u32 m0,s96,1792
s_waitcnt lgkmcnt(6)
)A"
R"A(v_mfma_f32_16x16x32_bf16 v[168:171],v[220:223],v[112:115],v[160:163]
v_add_f32 v242,v242,v183
v_cvt_pk_bf16_f32 v199,v182,v183
s_cmp_lg_u32 s34,1
s_cbranch_scc1 LG58
buffer_load_dwordx4 v253,s[80:83],s94 offen offset:256 lds
LG58:
v_mfma_f32_16x16x32_bf16 v[172:175],v[220:223],v[136:139],v[164:167]
ds_read_b128 v[220:223],v244 offset:6144
s_waitcnt lgkmcnt(6)
v_mfma_f32_16x16x32_bf16 v[168:171],v[224:227],v[116:119],v[168:171]
v_mfma_f32_16x16x32_bf16 v[172:175],v[224:227],v[140:143],v[172:175]
ds_read_b128 v[224:227],v245 offset:6144
s_mov_b32 m0,s97
s_waitcnt lgkmcnt(6)
v_mfma_f32_16x16x32_bf16 v[168:171],v[204:207],v[120:123],v[168:171]
buffer_load_dwordx4 v252,s[68:71],s95 offen nt lds
v_mfma_f32_16x16x32_bf16 v[172:175],v[204:207],v[144:147],v[172:175]
ds_read_b128 v[204:207],v244 offset:7168
s_waitcnt lgkmcnt(6)
v_mfma_f32_16x16x32_bf16 v[168:171],v[208:211],v[124:127],v[168:171]
s_waitcnt lgkmcnt(5)
v_dot2c_f32_bf16 v243,s55,v200
v_mfma_f32_16x16x32_bf16 v[172:175],v[208:211],v[148:151],v[172:175]
ds_read_b128 v[208:211],v245 offset:7168
s_add_u32 m0,s97,896
s_waitcnt lgkmcnt(5)
v_mfma_f32_16x16x32_bf16 v[168:171],v[212:215],v[128:131],v[168:171]
v_dot2c_f32_bf16 v243,s55,v201
buffer_load_dwordx4 v252,s[68:71],s95 offen offset:128 nt lds
v_mfma_f32_16x16x32_bf16 v[172:175],v[212:215],v[152:155],v[172:175]
ds_read_b128 v[212:215],v244 offset:8192
s_waitcnt lgkmcnt(5)
v_mfma_f32_16x16x32_bf16 v[168:171],v[216:219],v[132:135],v[168:171]
v_dot2c_f32_bf16 v243,s55,v202
v_mfma_f32_16x16x32_bf16 v[172:175],v[216:219],v[156:159],v[172:175]
ds_read_b128 v[216:219],v245 offset:8192
s_add_u32 m0,s97,1792
s_waitcnt lgkmcnt(5)
v_mfma_f32_16x16x32_bf16 v[176:179],v[220:223],v[112:115],v[160:163]
v_dot2c_f32_bf16 v243,s55,v203
buffer_load_dwordx4 v252,s[68:71],s95 offen offset:256 nt lds
v_mfma_f32_16x16x32_bf16 v[180:183],v[220:223],v[136:139],v[164:167]
ds_read_b64_tr_b16 v[220:221],v246
ds_read_b64_tr_b16 v[222:223],v246 offset:6144
s_waitcnt lgkmcnt(6)
v_mfma_f32_16x16x32_bf16 v[176:179],v[224:227],v[116:119],v[176:179]
v_mfma_f32_16x16x32_bf16 v[180:183],v[224:227],v[140:143],v[180:183]
ds_read_b64_tr_b16 v[224:225],v247
ds_read_b64_tr_b16 v[226:227],v247 offset:6144
v_exp_f32 v168,v168
s_mov_b32 s90,s98
s_add_u32 s98,s98,12288
s_cmp_eq_u32 s98,36864
s_cmov_b32 s98,0
s_sub_u32 s46,s98,s90
v_add_u32 v244,s46,v244
v_add_u32 v245,s46,v245
s_waitcnt lgkmcnt(7)
v_mfma_f32_16x16x32_bf16 v[176:179],v[204:207],v[120:123],v[176:179]
v_exp_f32 v169,v169
v_mfma_f32_16x16x32_bf16 v[180:183],v[204:207],v[144:147],v[180:183]
ds_read_b64_tr_b16 v[204:205],v248
ds_read_b64_tr_b16 v[206:207],v248 offset:6144
v_add_f32 v241,v241,v168
v_exp_f32 v170,v170
s_add_u32 s89,s48,36864
s_add_u32 s96,s96,12288
s_cmp_eq_u32 s96,s89
s_cselect_b32 s96,s48,s96
s_waitcnt lgkmcnt(8)
v_mfma_f32_16x16x32_bf16 v[176:179],v[208:211],v[124:127],v[176:179]
v_add_f32 v241,v241,v169
v_exp_f32 v171,v171
v_cvt_pk_bf16_f32 v184,v168,v169
v_mfma_f32_16x16x32_bf16 v[180:183],v[208:211],v[148:151],v[180:183]
ds_read_b64_tr_b16 v[208:209],v249
ds_read_b64_tr_b16 v[210:211],v249 offset:6144
v_add_f32 v241,v241,v170
v_exp_f32 v172,v172
s_cmp_eq_u32 s47,0
s_cselect_b32 s94,0,1
s_mul_i32 s94,s94,393216
s_waitcnt lgkmcnt(9)
v_mfma_f32_16x16x32_bf16 v[176:179],v[212:215],v[128:131],v[176:179]
v_add_f32 v241,v241,v171
v_exp_f32 v173,v173
v_cvt_pk_bf16_f32 v185,v170,v171
v_mfma_f32_16x16x32_bf16 v[180:183],v[212:215],v[152:155],v[180:183]
ds_read_b64_tr_b16 v[212:213],v246 offset:1024
ds_read_b64_tr_b16 v[214:215],v246 offset:7168
v_add_f32 v242,v242,v172
v_exp_f32 v174,v174
s_waitcnt lgkmcnt(10)
v_mfma_f32_16x16x32_bf16 v[176:179],v[216:219],v[132:135],v[176:179]
v_add_f32 v242,v242,v173
v_exp_f32 v175,v175
v_cvt_pk_bf16_f32 v188,v172,v173
v_mfma_f32_16x16x32_bf16 v[180:183],v[216:219],v[156:159],v[180:183]
ds_read_b64_tr_b16 v[216:217],v247 offset:1024
ds_read_b64_tr_b16 v[218:219],v247 offset:7168
v_add_f32 v242,v242,v174
s_waitcnt lgkmcnt(10)
v_mfma_f32_16x16x32_bf16 v[4:7],v[220:223],v[192:195],v[4:7]
v_add_f32 v242,v242,v175
v_cvt_pk_bf16_f32 v189,v174,v175
v_mfma_f32_16x16x32_bf16 v[52:55],v[220:223],v[196:199],v[52:55]
v_mfma_f32_16x16x32_bf16 v[100:103],v[220:223],v[200:203],v[100:103]
ds_read_b64_tr_b16 v[220:221],v248 offset:1024
ds_read_b64_tr_b16 v[222:223],v248 offset:7168
s_waitcnt lgkmcnt(10)
v_mfma_f32_16x16x32_bf16 v[8:11],v[224:227],v[192:195],v[8:11]
v_mfma_f32_16x16x32_bf16 v[56:59],v[224:227],v[196:199],v[56:59]
ds_read_b64_tr_b16 v[224:225],v249 offset:1024
ds_read_b64_tr_b16 v[226:227],v249 offset:7168
s_waitcnt lgkmcnt(10)
v_mfma_f32_16x16x32_bf16 v[12:15],v[204:207],v[192:195],v[12:15]
v_mfma_f32_16x16x32_bf16 v[60:63],v[204:207],v[196:199],v[60:63]
ds_read_b64_tr_b16 v[204:205],v246 offset:2048
ds_read_b64_tr_b16 v[206:207],v246 offset:8192
s_waitcnt lgkmcnt(10)
v_mfma_f32_16x16x32_bf16 v[16:19],v[208:211],v[192:195],v[16:19]
v_mfma_f32_16x16x32_bf16 v[64:67],v[208:211],v[196:199],v[64:67]
ds_read_b64_tr_b16 v[208:209],v247 offset:2048
ds_read_b64_tr_b16 v[210:211],v247 offset:8192
s_waitcnt lgkmcnt(10)
v_mfma_f32_16x16x32_bf16 v[20:23],v[212:215],v[192:195],v[20:23]
v_mfma_f32_16x16x32_bf16 v[68:71],v[212:215],v[196:199],v[68:71]
v_mfma_f32_16x16x32_bf16 v[104:107],v[212:215],v[200:203],v[104:107]
ds_read_b64_tr_b16 v[212:213],v248 offset:2048
ds_read_b64_tr_b16 v[214:215],v248 offset:8192
s_waitcnt lgkmcnt(10)
v_mfma_f32_16x16x32_bf16 v[24:27],v[216:219],v[192:195],v[24:27]
v_mfma_f32_16x16x32_bf16 v[72:75],v[216:219],v[196:199],v[72:75]
ds_read_b64_tr_b16 v[216:217],v249 offset:2048
ds_read_b64_tr_b16 v[218:219],v249 offset:8192
s_waitcnt lgkmcnt(10)
v_mfma_f32_16x16x32_bf16 v[28:31],v[220:223],v[192:195],v[28:31]
s_mov_b32 s90,s99
s_add_u32 s99,s99,12288
s_cmp_eq_u32 s99,36864
s_cmov_b32 s99,0
s_sub_u32 s46,s99,s90
v_add_u32 v246,s46,v246
v_add_u32 v247,s46,v247
v_add_u32 v248,s46,v248
v_add_u32 v249,s46,v249
v_mfma_f32_16x16x32_bf16 v[76:79],v[220:223],v[196:199],v[76:79]
s_waitcnt lgkmcnt(8)
v_mfma_f32_16x16x32_bf16 v[32:35],v[224:227],v[192:195],v[32:35]
v_mfma_f32_16x16x32_bf16 v[80:83],v[224:227],v[196:199],v[80:83]
s_waitcnt vmcnt(3) lgkmcnt(0)
s_barrier
ds_read_b128 v[220:223],v244
ds_read_b128 v[224:227],v245
v_mfma_f32_16x16x32_bf16 v[36:39],v[204:207],v[192:195],v[36:39]
v_exp_f32 v176,v176
s_mov_b32 m0,s96
v_mfma_f32_16x16x32_bf16 v[84:87],v[204:207],v[196:199],v[84:87]
v_exp_f32 v177,v177
s_cmp_lg_u32 s34,1
s_cbranch_scc1 LG59
buffer_load_dwordx4 v253,s[80:83],s94 offen lds
LG59:
v_mfma_f32_16x16x32_bf16 v[108:111],v[204:207],v[200:203],v[108:111]
ds_read_b128 v[204:207],v244 offset:1024
v_add_f32 v241,v241,v176
v_exp_f32 v178,v178
v_mfma_f32_16x16x32_bf16 v[40:43],v[208:211],v[192:195],v[40:43]
v_add_f32 v241,v241,v177
v_exp_f32 v179,v179
v_cvt_pk_bf16_f32 v186,v176,v177
v_mfma_f32_16x16x32_bf16 v[88:91],v[208:211],v[196:199],v[88:91]
ds_read_b128 v[208:211],v245 offset:1024
ds_read_b128 v[200:203],v250 offset:6144
v_add_f32 v241,v241,v178
v_exp_f32 v180,v180
s_add_u32 m0,s96,896
v_mfma_f32_16x16x32_bf16 v[44:47],v[212:215],v[192:195],v[44:47]
v_add_f32 v241,v241,v179
v_exp_f32 v181,v181
v_cvt_pk_bf16_f32 v187,v178,v179
s_cmp_lg_u32 s34,1
s_cbranch_scc1 LG60
buffer_load_dwordx4 v253,s[80:83],s94 offen offset:128 lds
LG60:
v_mfma_f32_16x16x32_bf16 v[92:95],v[212:215],v[196:199],v[92:95]
ds_read_b128 v[212:215],v244 offset:2048
v_add_f32 v242,v242,v180
v_exp_f32 v182,v182
v_mfma_f32_16x16x32_bf16 v[48:51],v[216:219],v[192:195],v[48:51]
v_add_f32 v242,v242,v181
v_exp_f32 v183,v183
v_cvt_pk_bf16_f32 v190,v180,v181
v_mfma_f32_16x16x32_bf16 v[96:99],v[216:219],v[196:199],v[96:99]
ds_read_b128 v[216:219],v245 offset:2048
v_add_f32 v242,v242,v182
s_add_u32 m0,s96,1792
s_waitcnt lgkmcnt(6)
v_mfma_f32_16x16x32_bf16 v[168:171],v[220:223],v[112:115],v[160:163]
v_add_f32 v242,v242,v183
v_cvt_pk_bf16_f32 v191,v182,v183
s_cmp_lg_u32 s34,1
s_cbranch_scc1 LG61
buffer_load_dwordx4 v253,s[80:83],s94 offen offset:256 lds
LG61:
v_mfma_f32_16x16x32_bf16 v[172:175],v[220:223],v[136:139],v[164:167]
ds_read_b128 v[220:223],v244 offset:6144
s_waitcnt lgkmcnt(6)
v_mfma_f32_16x16x32_bf16 v[168:171],v[224:227],v[116:119],v[168:171]
v_mfma_f32_16x16x32_bf16 v[172:175],v[224:227],v[140:143],v[172:175]
ds_read_b128 v[224:227],v245 offset:6144
s_waitcnt lgkmcnt(6)
v_mfma_f32_16x16x32_bf16 v[168:171],v[204:207],v[120:123],v[168:171]
v_mfma_f32_16x16x32_bf16 v[172:175],v[204:207],v[144:147],v[172:175]
ds_read_b128 v[204:207],v244 offset:7168
ds_read_b128 v[228:231],v250
ds_read_b128 v[232:235],v250 offset:1024
s_add_u32 s89,s48,36864
s_add_u32 s96,s96,12288
s_cmp_eq_u32 s96,s89
s_cselect_b32 s96,s48,s96
s_waitcnt lgkmcnt(8)
v_mfma_f32_16x16x32_bf16 v[168:171],v[208:211],v[124:127],v[168:171]
s_waitcnt lgkmcnt(7)
v_dot2c_f32_bf16 v243,s55,v200
v_mfma_f32_16x16x32_bf16 v[172:175],v[208:211],v[148:151],v[172:175]
ds_read_b128 v[208:211],v245 offset:7168
s_cmp_eq_u32 s47,0
s_cselect_b32 s94,4,2
s_mul_i32 s94,s94,393216
s_waitcnt lgkmcnt(7)
v_mfma_f32_16x16x32_bf16 v[168:171],v[212:215],v[128:131],v[168:171]
v_dot2c_f32_bf16 v243,s55,v201
v_mov_b32 v236,v240
v_mov_b32 v237,v240
v_mov_b32 v238,v240
v_mov_b32 v239,v240
v_mfma_f32_16x16x32_bf16 v[172:175],v[212:215],v[152:155],v[172:175]
ds_read_b128 v[212:215],v244 offset:8192
s_xor_b32 s90,s33,1
s_mul_i32 s90,s90,18432
s_lshl_b32 s97,s84,10
s_add_u32 s97,s97,s51
s_add_u32 s97,s97,73728
s_add_u32 s97,s97,s90
s_mov_b32 s95,0
s_waitcnt lgkmcnt(7)
v_mfma_f32_16x16x32_bf16 v[168:171],v[216:219],v[132:135],v[168:171]
v_dot2c_f32_bf16 v243,s55,v202
v_mfma_f32_16x16x32_bf16 v[172:175],v[216:219],v[156:159],v[172:175]
ds_read_b128 v[216:219],v245 offset:8192
s_waitcnt lgkmcnt(7)
v_mfma_f32_16x16x32_bf16 v[176:179],v[220:223],v[112:115],v[160:163]
v_dot2c_f32_bf16 v243,s55,v203
v_mfma_f32_16x16x32_bf16 v[180:183],v[220:223],v[136:139],v[164:167]
s_waitcnt lgkmcnt(4)
v_mfma_f32_16x16x32_bf16 v[236:239],v[220:223],v[228:231],v[236:239]
ds_read_b64_tr_b16 v[220:221],v246
ds_read_b64_tr_b16 v[222:223],v246 offset:6144
ds_read_b128 v[228:231],v250 offset:2048
v_mfma_f32_16x16x32_bf16 v[176:179],v[224:227],v[116:119],v[176:179]
v_exp_f32 v168,v168
v_mfma_f32_16x16x32_bf16 v[180:183],v[224:227],v[140:143],v[180:183]
v_exp_f32 v169,v169
s_waitcnt lgkmcnt(6)
v_mfma_f32_16x16x32_bf16 v[236:239],v[224:227],v[232:235],v[236:239]
ds_read_b64_tr_b16 v[224:225],v247
ds_read_b64_tr_b16 v[226:227],v247 offset:6144
ds_read_b128 v[232:235],v250 offset:3072
v_add_f32 v241,v241,v168
v_exp_f32 v170,v170
s_mov_b32 s90,s98
s_add_u32 s98,s98,12288
s_cmp_eq_u32 s98,36864
s_cmov_b32 s98,0
s_sub_u32 s46,s98,s90
v_add_u32 v244,s46,v244
v_add_u32 v245,s46,v245
v_mfma_f32_16x16x32_bf16 v[176:179],v[204:207],v[120:123],v[176:179]
v_add_f32 v241,v241,v169
v_exp_f32 v171,v171
v_cvt_pk_bf16_f32 v192,v168,v169
v_mfma_f32_16x16x32_bf16 v[180:183],v[204:207],v[144:147],v[180:183]
v_add_f32 v241,v241,v170
v_exp_f32 v172,v172
s_waitcnt lgkmcnt(3)
v_mfma_f32_16x16x32_bf16 v[236:239],v[204:207],v[228:231],v[236:239]
ds_read_b64_tr_b16 v[204:205],v248
ds_read_b64_tr_b16 v[206:207],v248 offset:6144
ds_read_b128 v[228:231],v250 offset:4096
v_add_f32 v241,v241,v171
v_exp_f32 v173,v173
v_cvt_pk_bf16_f32 v193,v170,v171
v_mfma_f32_16x16x32_bf16 v[176:179],v[208:211],v[124:127],v[176:179]
v_add_f32 v242,v242,v172
v_exp_f32 v174,v174
v_mfma_f32_16x16x32_bf16 v[180:183],v[208:211],v[148:151],v[180:183]
v_add_f32 v242,v242,v173
v_exp_f32 v175,v175
v_cvt_pk_bf16_f32 v196,v172,v173
s_waitcnt lgkmcnt(3)
v_mfma_f32_16x16x32_bf16 v[236:239],v[208:211],v[232:235],v[236:239]
ds_read_b64_tr_b16 v[208:209],v249
ds_read_b64_tr_b16 v[210:211],v249 offset:6144
ds_read_b128 v[232:235],v250 offset:5120
v_add_f32 v242,v242,v174
v_mfma_f32_16x16x32_bf16 v[176:179],v[212:215],v[128:131],v[176:179]
v_add_f32 v242,v242,v175
v_cvt_pk_bf16_f32 v197,v174,v175
v_mfma_f32_16x16x32_bf16 v[180:183],v[212:215],v[152:155],v[180:183]
s_waitcnt lgkmcnt(3)
v_mfma_f32_16x16x32_bf16 v[236:239],v[212:215],v[228:231],v[236:239]
ds_read_b64_tr_b16 v[212:213],v246 offset:1024
ds_read_b64_tr_b16 v[214:215],v246 offset:7168
v_mfma_f32_16x16x32_bf16 v[176:179],v[216:219],v[132:135],v[176:179]
v_mfma_f32_16x16x32_bf16 v[180:183],v[216:219],v[156:159],v[180:183]
s_waitcnt lgkmcnt(2)
v_mfma_f32_16x16x32_bf16 v[236:239],v[216:219],v[232:235],v[236:239]
ds_read_b64_tr_b16 v[216:217],v247 offset:1024
ds_read_b64_tr_b16 v[218:219],v247 offset:7168
v_mfma_f32_16x16x32_bf16 v[4:7],v[220:223],v[184:187],v[4:7]
v_mfma_f32_16x16x32_bf16 v[52:55],v[220:223],v[188:191],v[52:55]
v_mfma_f32_16x16x32_bf16 v[100:103],v[220:223],v[200:203],v[100:103]
ds_read_b64_tr_b16 v[220:221],v248 offset:1024
ds_read_b64_tr_b16 v[222:223],v248 offset:7168
v_mfma_f32_16x16x32_bf16 v[8:11],v[224:227],v[184:187],v[8:11]
v_exp_f32 v236,v236
v_mfma_f32_16x16x32_bf16 v[56:59],v[224:227],v[188:191],v[56:59]
ds_read_b64_tr_b16 v[224:225],v249 offset:1024
ds_read_b64_tr_b16 v[226:227],v249 offset:7168
v_exp_f32 v237,v237
v_mfma_f32_16x16x32_bf16 v[12:15],v[204:207],v[184:187],v[12:15]
v_exp_f32 v238,v238
v_mfma_f32_16x16x32_bf16 v[60:63],v[204:207],v[188:191],v[60:63]
ds_read_b64_tr_b16 v[204:205],v246 offset:2048
ds_read_b64_tr_b16 v[206:207],v246 offset:8192
v_exp_f32 v239,v239
v_mfma_f32_16x16x32_bf16 v[16:19],v[208:211],v[184:187],v[16:19]
v_mfma_f32_16x16x32_bf16 v[64:67],v[208:211],v[188:191],v[64:67]
ds_read_b64_tr_b16 v[208:209],v247 offset:2048
ds_read_b64_tr_b16 v[210:211],v247 offset:8192
v_cvt_pk_bf16_f32 v0,v236,v237
v_cvt_pk_bf16_f32 v1,v238,v239
s_waitcnt lgkmcnt(10)
v_mfma_f32_16x16x32_bf16 v[20:23],v[212:215],v[184:187],v[20:23]
ds_write_b64 v250,v[0:1] offset:7176
v_mfma_f32_16x16x32_bf16 v[68:71],v[212:215],v[188:191],v[68:71]
v_mfma_f32_16x16x32_bf16 v[104:107],v[212:215],v[200:203],v[104:107]
ds_read_b64_tr_b16 v[212:213],v248 offset:2048
ds_read_b64_tr_b16 v[214:215],v248 offset:8192
s_waitcnt lgkmcnt(11)
v_mfma_f32_16x16x32_bf16 v[24:27],v[216:219],v[184:187],v[24:27]
v_mfma_f32_16x16x32_bf16 v[72:75],v[216:219],v[188:191],v[72:75]
ds_read_b64_tr_b16 v[216:217],v249 offset:2048
ds_read_b64_tr_b16 v[218:219],v249 offset:8192
s_waitcnt lgkmcnt(11)
v_mfma_f32_16x16x32_bf16 v[28:31],v[220:223],v[184:187],v[28:31]
s_mov_b32 s90,s99
s_add_u32 s99,s99,12288
s_cmp_eq_u32 s99,36864
s_cmov_b32 s99,0
s_sub_u32 s46,s99,s90
v_add_u32 v246,s46,v246
v_add_u32 v247,s46,v247
v_add_u32 v248,s46,v248
v_add_u32 v249,s46,v249
v_mfma_f32_16x16x32_bf16 v[76:79],v[220:223],v[188:191],v[76:79]
s_waitcnt lgkmcnt(9)
v_mfma_f32_16x16x32_bf16 v[32:35],v[224:227],v[184:187],v[32:35]
v_mfma_f32_16x16x32_bf16 v[80:83],v[224:227],v[188:191],v[80:83]
s_waitcnt vmcnt(0) lgkmcnt(0)
s_barrier
ds_read_b64_tr_b16 v[220:221],v246
ds_read_b64_tr_b16 v[222:223],v246 offset:6144
ds_read_b64_tr_b16 v[224:225],v247
ds_read_b64_tr_b16 v[226:227],v247 offset:6144
v_mfma_f32_16x16x32_bf16 v[36:39],v[204:207],v[184:187],v[36:39]
v_exp_f32 v176,v176
s_mov_b32 m0,s96
v_mfma_f32_16x16x32_bf16 v[84:87],v[204:207],v[188:191],v[84:87]
v_exp_f32 v177,v177
s_cmp_lg_u32 s34,1
s_cbranch_scc1 LG62
buffer_load_dwordx4 v253,s[80:83],s94 offen lds
LG62:
v_mfma_f32_16x16x32_bf16 v[108:111],v[204:207],v[200:203],v[108:111]
ds_read_b64_tr_b16 v[204:205],v248
ds_read_b64_tr_b16 v[206:207],v248 offset:6144
v_add_f32 v241,v241,v176
v_exp_f32 v178,v178
s_add_u32 m0,s96,896
v_mfma_f32_16x16x32_bf16 v[40:43],v[208:211],v[184:187],v[40:43]
v_add_f32 v241,v241,v177
v_exp_f32 v179,v179
v_cvt_pk_bf16_f32 v194,v176,v177
s_cmp_lg_u32 s34,1
s_cbranch_scc1 LG63
buffer_load_dwordx4 v253,s[80:83],s94 offen offset:128 lds
LG63:
)A"
R"A(v_mfma_f32_16x16x32_bf16 v[88:91],v[208:211],v[188:191],v[88:91]
ds_read_b64_tr_b16 v[208:209],v249
ds_read_b64_tr_b16 v[210:211],v249 offset:6144
ds_read_b128 v[200:203],v250 offset:7168
v_add_f32 v241,v241,v178
v_exp_f32 v180,v180
s_add_u32 m0,s96,1792
v_mfma_f32_16x16x32_bf16 v[44:47],v[212:215],v[184:187],v[44:47]
v_add_f32 v241,v241,v179
v_exp_f32 v181,v181
v_cvt_pk_bf16_f32 v195,v178,v179
s_cmp_lg_u32 s34,1
s_cbranch_scc1 LG64
buffer_load_dwordx4 v253,s[80:83],s94 offen offset:256 lds
LG64:
v_mfma_f32_16x16x32_bf16 v[92:95],v[212:215],v[188:191],v[92:95]
ds_read_b64_tr_b16 v[212:213],v246 offset:1024
ds_read_b64_tr_b16 v[214:215],v246 offset:7168
v_add_f32 v242,v242,v180
v_exp_f32 v182,v182
s_mov_b32 m0,s97
s_mov_b32 s88,s95
v_mfma_f32_16x16x32_bf16 v[48:51],v[216:219],v[184:187],v[48:51]
v_add_f32 v242,v242,v181
v_exp_f32 v183,v183
v_cvt_pk_bf16_f32 v198,v180,v181
s_cmp_lg_u32 s34,1
s_cbranch_scc1 LG65
buffer_load_dwordx4 v254,s[76:79],s88 offen lds
LG65:
v_mfma_f32_16x16x32_bf16 v[96:99],v[216:219],v[188:191],v[96:99]
ds_read_b64_tr_b16 v[216:217],v247 offset:1024
ds_read_b64_tr_b16 v[218:219],v247 offset:7168
v_add_f32 v242,v242,v182
v_add_f32 v242,v242,v183
v_cvt_pk_bf16_f32 v199,v182,v183
s_add_u32 m0,s97,4096
s_add_u32 s88,s95,40960
s_waitcnt lgkmcnt(11)
v_mfma_f32_16x16x32_bf16 v[4:7],v[220:223],v[192:195],v[4:7]
s_cmp_lg_u32 s34,1
s_cbranch_scc1 LG66
buffer_load_dwordx4 v254,s[76:79],s88 offen lds
LG66:
v_mfma_f32_16x16x32_bf16 v[52:55],v[220:223],v[196:199],v[52:55]
s_add_u32 m0,s97,8192
s_add_u32 s88,s95,81920
s_waitcnt lgkmcnt(4)
v_mfma_f32_16x16x32_bf16 v[100:103],v[220:223],v[200:203],v[100:103]
ds_read_b64_tr_b16 v[220:221],v248 offset:1024
ds_read_b64_tr_b16 v[222:223],v248 offset:7168
s_cmp_lg_u32 s34,1
s_cbranch_scc1 LG67
buffer_load_dwordx4 v254,s[76:79],s88 offen lds
LG67:
v_mfma_f32_16x16x32_bf16 v[8:11],v[224:227],v[192:195],v[8:11]
s_add_u32 m0,s97,12288
s_add_u32 s88,s95,122880
v_mov_b32 v164,v241
s_nop 1
v_permlane32_swap_b32 v241,v164
v_add_f32 v241,v241,v164
v_mov_b32 v164,v241
s_nop 1
v_permlane16_swap_b32 v241,v164
v_add_f32 v241,v241,v164
v_rcp_f32 v0,v241
v_mfma_f32_16x16x32_bf16 v[56:59],v[224:227],v[196:199],v[56:59]
ds_read_b64_tr_b16 v[224:225],v249 offset:1024
ds_read_b64_tr_b16 v[226:227],v249 offset:7168
s_cmp_lg_u32 s34,1
s_cbranch_scc1 LG68
buffer_load_dwordx4 v254,s[76:79],s88 offen lds
LG68:
v_mov_b32 v165,v242
s_nop 1
v_permlane32_swap_b32 v242,v165
v_add_f32 v242,v242,v165
v_mov_b32 v165,v242
s_nop 1
v_permlane16_swap_b32 v242,v165
v_add_f32 v242,v242,v165
v_rcp_f32 v2,v242
v_mfma_f32_16x16x32_bf16 v[12:15],v[204:207],v[192:195],v[12:15]
s_add_u32 m0,s97,16384
s_add_u32 s88,s95,163840
v_mfma_f32_16x16x32_bf16 v[60:63],v[204:207],v[196:199],v[60:63]
ds_read_b64_tr_b16 v[204:205],v246 offset:2048
ds_read_b64_tr_b16 v[206:207],v246 offset:8192
s_cmp_lg_u32 s34,1
s_cbranch_scc1 LG69
s_cmp_ge_u32 s84,2
s_cbranch_scc1 LX70
buffer_load_dwordx4 v254,s[76:79],s88 offen lds
LX70:
LG69:
v_mfma_f32_16x16x32_bf16 v[16:19],v[208:211],v[192:195],v[16:19]
v_dot2c_f32_bf16 v243,s55,v200
v_mfma_f32_16x16x32_bf16 v[64:67],v[208:211],v[196:199],v[64:67]
ds_read_b64_tr_b16 v[208:209],v247 offset:2048
ds_read_b64_tr_b16 v[210:211],v247 offset:8192
s_waitcnt lgkmcnt(10)
v_mfma_f32_16x16x32_bf16 v[20:23],v[212:215],v[192:195],v[20:23]
v_dot2c_f32_bf16 v243,s55,v201
v_mfma_f32_16x16x32_bf16 v[68:71],v[212:215],v[196:199],v[68:71]
v_mfma_f32_16x16x32_bf16 v[104:107],v[212:215],v[200:203],v[104:107]
ds_read_b64_tr_b16 v[212:213],v248 offset:2048
ds_read_b64_tr_b16 v[214:215],v248 offset:8192
s_waitcnt lgkmcnt(10)
v_mfma_f32_16x16x32_bf16 v[24:27],v[216:219],v[192:195],v[24:27]
v_dot2c_f32_bf16 v243,s55,v202
v_mfma_f32_16x16x32_bf16 v[72:75],v[216:219],v[196:199],v[72:75]
ds_read_b64_tr_b16 v[216:217],v249 offset:2048
ds_read_b64_tr_b16 v[218:219],v249 offset:8192
s_waitcnt lgkmcnt(10)
v_mfma_f32_16x16x32_bf16 v[28:31],v[220:223],v[192:195],v[28:31]
v_dot2c_f32_bf16 v243,s55,v203
v_mfma_f32_16x16x32_bf16 v[76:79],v[220:223],v[196:199],v[76:79]
s_waitcnt lgkmcnt(8)
v_mfma_f32_16x16x32_bf16 v[32:35],v[224:227],v[192:195],v[32:35]
v_mfma_f32_16x16x32_bf16 v[80:83],v[224:227],v[196:199],v[80:83]
v_mov_b32 v166,v243
s_nop 1
v_permlane32_swap_b32 v243,v166
v_add_f32 v243,v243,v166
v_mov_b32 v166,v243
s_nop 1
v_permlane16_swap_b32 v243,v166
v_add_f32 v243,v243,v166
v_rcp_f32 v160,v243
s_waitcnt lgkmcnt(6)
v_mfma_f32_16x16x32_bf16 v[36:39],v[204:207],v[192:195],v[36:39]
s_mov_b32 s90,0x5d800000
v_cmp_ngt_f32 s[92:93],s90,v241
v_cmp_ngt_f32 vcc,s90,v242
s_nop 0
s_or_b64 s[92:93],s[92:93],vcc
v_cmp_ngt_f32 vcc,s90,v243
s_nop 0
s_or_b64 s[92:93],s[92:93],vcc
s_cselect_b32 s90,1,0
v_mov_b32 v164,s90
s_lshl_b32 s91,s47,4
s_add_u32 s91,s91,153612
v_mov_b32 v165,s91
ds_write_b32 v165,v164
v_mbcnt_lo_u32_b32 v167,-1,0
v_mbcnt_hi_u32_b32 v167,-1,v167
v_lshlrev_b32 v167,4,v167
s_cmp_eq_u32 s33,0
s_movk_i32 s91,18432
s_cselect_b32 s91,12288,s91
s_add_u32 s91,s91,s51
s_add_u32 s91,s91,73728
v_mbcnt_lo_u32_b32 v162,-1,0
v_mbcnt_hi_u32_b32 v162,-1,v162
v_lshlrev_b32 v162,3,v162
v_add_u32 v162,s91,v162
v_mfma_f32_16x16x32_bf16 v[84:87],v[204:207],v[196:199],v[84:87]
v_pk_mul_f32 v[8:9],v[8:9],v[0:1] op_sel_hi:[1,0]
v_pk_mul_f32 v[10:11],v[10:11],v[0:1] op_sel_hi:[1,0]
v_pk_mul_f32 v[4:5],v[4:5],v[0:1] op_sel_hi:[1,0]
v_pk_mul_f32 v[6:7],v[6:7],v[0:1] op_sel_hi:[1,0]
v_cvt_pk_bf16_f32 v8,v8,v9
v_cvt_pk_bf16_f32 v9,v10,v11
v_cvt_pk_bf16_f32 v10,v4,v5
v_cvt_pk_bf16_f32 v11,v6,v7
s_mov_b32 s91,0x120400
buffer_store_dwordx4 v[8:11],v167,s[72:75],s91 offen sc1
v_mfma_f32_16x16x32_bf16 v[108:111],v[204:207],v[200:203],v[108:111]
v_pk_mul_f32 v[56:57],v[56:57],v[2:3] op_sel_hi:[1,0]
v_pk_mul_f32 v[58:59],v[58:59],v[2:3] op_sel_hi:[1,0]
v_pk_mul_f32 v[52:53],v[52:53],v[2:3] op_sel_hi:[1,0]
v_pk_mul_f32 v[54:55],v[54:55],v[2:3] op_sel_hi:[1,0]
v_cvt_pk_bf16_f32 v56,v56,v57
v_cvt_pk_bf16_f32 v57,v58,v59
v_cvt_pk_bf16_f32 v58,v52,v53
v_cvt_pk_bf16_f32 v59,v54,v55
s_mov_b32 s91,0x150400
buffer_store_dwordx4 v[56:59],v167,s[72:75],s91 offen sc1
s_waitcnt lgkmcnt(5)
v_mfma_f32_16x16x32_bf16 v[40:43],v[208:211],v[192:195],v[40:43]
v_pk_mul_f32 v[16:17],v[16:17],v[0:1] op_sel_hi:[1,0]
v_pk_mul_f32 v[18:19],v[18:19],v[0:1] op_sel_hi:[1,0]
v_pk_mul_f32 v[12:13],v[12:13],v[0:1] op_sel_hi:[1,0]
v_pk_mul_f32 v[14:15],v[14:15],v[0:1] op_sel_hi:[1,0]
v_cvt_pk_bf16_f32 v16,v16,v17
v_cvt_pk_bf16_f32 v17,v18,v19
v_cvt_pk_bf16_f32 v18,v12,v13
v_cvt_pk_bf16_f32 v19,v14,v15
s_mov_b32 s91,0x120000
buffer_store_dwordx4 v[16:19],v167,s[72:75],s91 offen sc1
v_mfma_f32_16x16x32_bf16 v[88:91],v[208:211],v[196:199],v[88:91]
v_pk_mul_f32 v[64:65],v[64:65],v[2:3] op_sel_hi:[1,0]
v_pk_mul_f32 v[66:67],v[66:67],v[2:3] op_sel_hi:[1,0]
v_pk_mul_f32 v[60:61],v[60:61],v[2:3] op_sel_hi:[1,0]
v_pk_mul_f32 v[62:63],v[62:63],v[2:3] op_sel_hi:[1,0]
v_cvt_pk_bf16_f32 v64,v64,v65
v_cvt_pk_bf16_f32 v65,v66,v67
v_cvt_pk_bf16_f32 v66,v60,v61
v_cvt_pk_bf16_f32 v67,v62,v63
s_mov_b32 s91,0x150000
buffer_store_dwordx4 v[64:67],v167,s[72:75],s91 offen sc1
s_waitcnt lgkmcnt(3)
v_mfma_f32_16x16x32_bf16 v[44:47],v[212:215],v[192:195],v[44:47]
v_pk_mul_f32 v[100:101],v[100:101],v[160:161] op_sel_hi:[1,0]
v_pk_mul_f32 v[102:103],v[102:103],v[160:161] op_sel_hi:[1,0]
v_cvt_pk_bf16_f32 v100,v100,v101
v_cvt_pk_bf16_f32 v101,v102,v103
ds_write_b64 v162,v[100:101] offset:1536
v_mfma_f32_16x16x32_bf16 v[92:95],v[212:215],v[196:199],v[92:95]
v_pk_mul_f32 v[24:25],v[24:25],v[0:1] op_sel_hi:[1,0]
v_pk_mul_f32 v[26:27],v[26:27],v[0:1] op_sel_hi:[1,0]
v_pk_mul_f32 v[20:21],v[20:21],v[0:1] op_sel_hi:[1,0]
v_pk_mul_f32 v[22:23],v[22:23],v[0:1] op_sel_hi:[1,0]
v_cvt_pk_bf16_f32 v24,v24,v25
v_cvt_pk_bf16_f32 v25,v26,v27
v_cvt_pk_bf16_f32 v26,v20,v21
v_cvt_pk_bf16_f32 v27,v22,v23
s_mov_b32 s91,0x120c00
buffer_store_dwordx4 v[24:27],v167,s[72:75],s91 offen sc1
s_waitcnt lgkmcnt(2)
v_mfma_f32_16x16x32_bf16 v[48:51],v[216:219],v[192:195],v[48:51]
v_pk_mul_f32 v[72:73],v[72:73],v[2:3] op_sel_hi:[1,0]
v_pk_mul_f32 v[74:75],v[74:75],v[2:3] op_sel_hi:[1,0]
v_pk_mul_f32 v[68:69],v[68:69],v[2:3] op_sel_hi:[1,0]
v_pk_mul_f32 v[70:71],v[70:71],v[2:3] op_sel_hi:[1,0]
v_cvt_pk_bf16_f32 v72,v72,v73
v_cvt_pk_bf16_f32 v73,v74,v75
v_cvt_pk_bf16_f32 v74,v68,v69
v_cvt_pk_bf16_f32 v75,v70,v71
s_mov_b32 s91,0x150c00
buffer_store_dwordx4 v[72:75],v167,s[72:75],s91 offen sc1
v_mfma_f32_16x16x32_bf16 v[96:99],v[216:219],v[196:199],v[96:99]
v_pk_mul_f32 v[32:33],v[32:33],v[0:1] op_sel_hi:[1,0]
v_pk_mul_f32 v[34:35],v[34:35],v[0:1] op_sel_hi:[1,0]
v_pk_mul_f32 v[28:29],v[28:29],v[0:1] op_sel_hi:[1,0]
v_pk_mul_f32 v[30:31],v[30:31],v[0:1] op_sel_hi:[1,0]
v_cvt_pk_bf16_f32 v32,v32,v33
v_cvt_pk_bf16_f32 v33,v34,v35
v_cvt_pk_bf16_f32 v34,v28,v29
v_cvt_pk_bf16_f32 v35,v30,v31
s_mov_b32 s91,0x120800
buffer_store_dwordx4 v[32:35],v167,s[72:75],s91 offen sc1
v_pk_mul_f32 v[80:81],v[80:81],v[2:3] op_sel_hi:[1,0]
v_pk_mul_f32 v[82:83],v[82:83],v[2:3] op_sel_hi:[1,0]
v_pk_mul_f32 v[76:77],v[76:77],v[2:3] op_sel_hi:[1,0]
v_pk_mul_f32 v[78:79],v[78:79],v[2:3] op_sel_hi:[1,0]
v_cvt_pk_bf16_f32 v80,v80,v81
v_cvt_pk_bf16_f32 v81,v82,v83
v_cvt_pk_bf16_f32 v82,v76,v77
v_cvt_pk_bf16_f32 v83,v78,v79
s_mov_b32 s91,0x150800
buffer_store_dwordx4 v[80:83],v167,s[72:75],s91 offen sc1
v_pk_mul_f32 v[104:105],v[104:105],v[160:161] op_sel_hi:[1,0]
v_pk_mul_f32 v[106:107],v[106:107],v[160:161] op_sel_hi:[1,0]
v_cvt_pk_bf16_f32 v104,v104,v105
v_cvt_pk_bf16_f32 v105,v106,v107
ds_write_b64 v162,v[104:105] offset:2048
v_pk_mul_f32 v[40:41],v[40:41],v[0:1] op_sel_hi:[1,0]
v_pk_mul_f32 v[42:43],v[42:43],v[0:1] op_sel_hi:[1,0]
v_pk_mul_f32 v[36:37],v[36:37],v[0:1] op_sel_hi:[1,0]
v_pk_mul_f32 v[38:39],v[38:39],v[0:1] op_sel_hi:[1,0]
v_cvt_pk_bf16_f32 v40,v40,v41
v_cvt_pk_bf16_f32 v41,v42,v43
v_cvt_pk_bf16_f32 v42,v36,v37
v_cvt_pk_bf16_f32 v43,v38,v39
s_mov_b32 s91,0x121400
buffer_store_dwordx4 v[40:43],v167,s[72:75],s91 offen sc1
v_pk_mul_f32 v[88:89],v[88:89],v[2:3] op_sel_hi:[1,0]
v_pk_mul_f32 v[90:91],v[90:91],v[2:3] op_sel_hi:[1,0]
v_pk_mul_f32 v[84:85],v[84:85],v[2:3] op_sel_hi:[1,0]
v_pk_mul_f32 v[86:87],v[86:87],v[2:3] op_sel_hi:[1,0]
v_cvt_pk_bf16_f32 v88,v88,v89
v_cvt_pk_bf16_f32 v89,v90,v91
v_cvt_pk_bf16_f32 v90,v84,v85
v_cvt_pk_bf16_f32 v91,v86,v87
s_mov_b32 s91,0x151400
buffer_store_dwordx4 v[88:91],v167,s[72:75],s91 offen sc1
v_pk_mul_f32 v[48:49],v[48:49],v[0:1] op_sel_hi:[1,0]
v_pk_mul_f32 v[50:51],v[50:51],v[0:1] op_sel_hi:[1,0]
v_pk_mul_f32 v[44:45],v[44:45],v[0:1] op_sel_hi:[1,0]
v_pk_mul_f32 v[46:47],v[46:47],v[0:1] op_sel_hi:[1,0]
v_cvt_pk_bf16_f32 v48,v48,v49
v_cvt_pk_bf16_f32 v49,v50,v51
v_cvt_pk_bf16_f32 v50,v44,v45
v_cvt_pk_bf16_f32 v51,v46,v47
s_mov_b32 s91,0x121000
buffer_store_dwordx4 v[48:51],v167,s[72:75],s91 offen sc1
v_pk_mul_f32 v[96:97],v[96:97],v[2:3] op_sel_hi:[1,0]
v_pk_mul_f32 v[98:99],v[98:99],v[2:3] op_sel_hi:[1,0]
v_pk_mul_f32 v[92:93],v[92:93],v[2:3] op_sel_hi:[1,0]
v_pk_mul_f32 v[94:95],v[94:95],v[2:3] op_sel_hi:[1,0]
v_cvt_pk_bf16_f32 v96,v96,v97
v_cvt_pk_bf16_f32 v97,v98,v99
v_cvt_pk_bf16_f32 v98,v92,v93
v_cvt_pk_bf16_f32 v99,v94,v95
s_mov_b32 s91,0x151000
buffer_store_dwordx4 v[96:99],v167,s[72:75],s91 offen sc1
v_pk_mul_f32 v[108:109],v[108:109],v[160:161] op_sel_hi:[1,0]
v_pk_mul_f32 v[110:111],v[110:111],v[160:161] op_sel_hi:[1,0]
v_cvt_pk_bf16_f32 v108,v108,v109
v_cvt_pk_bf16_f32 v109,v110,v111
ds_write_b64 v162,v[108:109] offset:2560
s_waitcnt lgkmcnt(0)
s_barrier
v_mov_b32 v1,0x25800
ds_read_b128 v[168:171],v1
ds_read_b128 v[172:175],v1 offset:16
s_waitcnt lgkmcnt(1)
v_or_b32 v0,v168,v169
v_or3_b32 v0,v0,v170,v171
s_waitcnt lgkmcnt(0)
v_or3_b32 v0,v0,v172,v173
v_or3_b32 v0,v0,v174,v175
s_nop 0
v_readfirstlane_b32 s90,v0
s_nop 0
s_andn2_b32 s90,s90,s44
s_cbranch_scc0 W3_EPI
s_mov_b32 s44,1
v_mov_b32 v0,0xff800000
v_mov_b32 v1,0xff800000
v_mov_b32 v2,0xff800000
s_mov_b32 s98,0
v_mbcnt_lo_u32_b32 v168,-1,0
v_mbcnt_hi_u32_b32 v168,-1,v168
v_and_b32 v169,15,v168
v_lshrrev_b32 v170,4,v168
v_lshrrev_b32 v168,3,v169
s_movk_i32 s88,3072
v_mul_u32_u24 v244,s88,v168
v_and_b32 v168,7,v169
v_lshl_add_u32 v244,v168,7,v244
v_lshrrev_b32 v168,1,v169
v_xor_b32 v168,v168,v170
v_lshl_add_u32 v244,v168,4,v244
v_xor_b32 v245,64,v244
s_add_u32 s88,s51,s98
v_add_u32 v244,s88,v244
v_add_u32 v245,s88,v245
s_mov_b32 s94,0
s_mov_b32 s96,s48
W3_MAX:
s_waitcnt vmcnt(0) lgkmcnt(0)
s_barrier
s_mov_b32 m0,s96
s_nop 0
buffer_load_dwordx4 v251,s[64:67],s94 offen nt lds
s_add_u32 m0,s96,896
s_nop 0
buffer_load_dwordx4 v251,s[64:67],s94 offen offset:128 nt lds
s_add_u32 m0,s96,1792
s_nop 0
buffer_load_dwordx4 v251,s[64:67],s94 offen offset:256 nt lds
s_waitcnt vmcnt(0)
s_barrier
ds_read_b128 v[204:207],v244
ds_read_b128 v[208:211],v245
ds_read_b128 v[212:215],v244 offset:1024
ds_read_b128 v[216:219],v245 offset:1024
ds_read_b128 v[220:223],v244 offset:2048
ds_read_b128 v[224:227],v245 offset:2048
ds_read_b128 v[228:231],v250
s_waitcnt lgkmcnt(6)
v_mfma_f32_16x16x32_bf16 v[168:171],v[204:207],v[112:115],0
v_mfma_f32_16x16x32_bf16 v[172:175],v[204:207],v[136:139],0
s_waitcnt lgkmcnt(0)
v_mfma_f32_16x16x32_bf16 v[236:239],v[204:207],v[228:231],0
ds_read_b128 v[232:235],v250 offset:1024
v_mfma_f32_16x16x32_bf16 v[168:171],v[208:211],v[116:119],v[168:171]
v_mfma_f32_16x16x32_bf16 v[172:175],v[208:211],v[140:143],v[172:175]
s_waitcnt lgkmcnt(0)
v_mfma_f32_16x16x32_bf16 v[236:239],v[208:211],v[232:235],v[236:239]
ds_read_b128 v[228:231],v250 offset:2048
v_mfma_f32_16x16x32_bf16 v[168:171],v[212:215],v[120:123],v[168:171]
v_mfma_f32_16x16x32_bf16 v[172:175],v[212:215],v[144:147],v[172:175]
s_waitcnt lgkmcnt(0)
v_mfma_f32_16x16x32_bf16 v[236:239],v[212:215],v[228:231],v[236:239]
ds_read_b128 v[232:235],v250 offset:3072
v_mfma_f32_16x16x32_bf16 v[168:171],v[216:219],v[124:127],v[168:171]
v_mfma_f32_16x16x32_bf16 v[172:175],v[216:219],v[148:151],v[172:175]
s_waitcnt lgkmcnt(0)
v_mfma_f32_16x16x32_bf16 v[236:239],v[216:219],v[232:235],v[236:239]
ds_read_b128 v[228:231],v250 offset:4096
v_mfma_f32_16x16x32_bf16 v[168:171],v[220:223],v[128:131],v[168:171]
v_mfma_f32_16x16x32_bf16 v[172:175],v[220:223],v[152:155],v[172:175]
s_waitcnt lgkmcnt(0)
v_mfma_f32_16x16x32_bf16 v[236:239],v[220:223],v[228:231],v[236:239]
ds_read_b128 v[232:235],v250 offset:5120
v_mfma_f32_16x16x32_bf16 v[168:171],v[224:227],v[132:135],v[168:171]
v_mfma_f32_16x16x32_bf16 v[172:175],v[224:227],v[156:159],v[172:175]
s_waitcnt lgkmcnt(0)
v_mfma_f32_16x16x32_bf16 v[236:239],v[224:227],v[232:235],v[236:239]
s_nop 4
v_max3_f32 v0,v0,v168,v169
v_max3_f32 v0,v0,v170,v171
v_max3_f32 v1,v1,v172,v173
v_max3_f32 v1,v1,v174,v175
v_max3_f32 v2,v2,v236,v237
)A"
R"A(v_max3_f32 v2,v2,v238,v239
ds_read_b128 v[204:207],v244 offset:6144
ds_read_b128 v[208:211],v245 offset:6144
ds_read_b128 v[212:215],v244 offset:7168
ds_read_b128 v[216:219],v245 offset:7168
ds_read_b128 v[220:223],v244 offset:8192
ds_read_b128 v[224:227],v245 offset:8192
ds_read_b128 v[228:231],v250
s_waitcnt lgkmcnt(6)
v_mfma_f32_16x16x32_bf16 v[168:171],v[204:207],v[112:115],0
v_mfma_f32_16x16x32_bf16 v[172:175],v[204:207],v[136:139],0
s_waitcnt lgkmcnt(0)
v_mfma_f32_16x16x32_bf16 v[236:239],v[204:207],v[228:231],0
ds_read_b128 v[232:235],v250 offset:1024
v_mfma_f32_16x16x32_bf16 v[168:171],v[208:211],v[116:119],v[168:171]
v_mfma_f32_16x16x32_bf16 v[172:175],v[208:211],v[140:143],v[172:175]
s_waitcnt lgkmcnt(0)
v_mfma_f32_16x16x32_bf16 v[236:239],v[208:211],v[232:235],v[236:239]
ds_read_b128 v[228:231],v250 offset:2048
v_mfma_f32_16x16x32_bf16 v[168:171],v[212:215],v[120:123],v[168:171]
v_mfma_f32_16x16x32_bf16 v[172:175],v[212:215],v[144:147],v[172:175]
s_waitcnt lgkmcnt(0)
v_mfma_f32_16x16x32_bf16 v[236:239],v[212:215],v[228:231],v[236:239]
ds_read_b128 v[232:235],v250 offset:3072
v_mfma_f32_16x16x32_bf16 v[168:171],v[216:219],v[124:127],v[168:171]
v_mfma_f32_16x16x32_bf16 v[172:175],v[216:219],v[148:151],v[172:175]
s_waitcnt lgkmcnt(0)
v_mfma_f32_16x16x32_bf16 v[236:239],v[216:219],v[232:235],v[236:239]
ds_read_b128 v[228:231],v250 offset:4096
v_mfma_f32_16x16x32_bf16 v[168:171],v[220:223],v[128:131],v[168:171]
v_mfma_f32_16x16x32_bf16 v[172:175],v[220:223],v[152:155],v[172:175]
s_waitcnt lgkmcnt(0)
v_mfma_f32_16x16x32_bf16 v[236:239],v[220:223],v[228:231],v[236:239]
ds_read_b128 v[232:235],v250 offset:5120
v_mfma_f32_16x16x32_bf16 v[168:171],v[224:227],v[132:135],v[168:171]
v_mfma_f32_16x16x32_bf16 v[172:175],v[224:227],v[156:159],v[172:175]
s_waitcnt lgkmcnt(0)
v_mfma_f32_16x16x32_bf16 v[236:239],v[224:227],v[232:235],v[236:239]
s_nop 4
v_max3_f32 v0,v0,v168,v169
v_max3_f32 v0,v0,v170,v171
v_max3_f32 v1,v1,v172,v173
v_max3_f32 v1,v1,v174,v175
v_max3_f32 v2,v2,v236,v237
v_max3_f32 v2,v2,v238,v239
s_add_u32 s94,s94,12288
s_cmp_lt_u32 s94,196608
s_cbranch_scc1 W3_MAX
v_mov_b32 v3,v0
s_nop 1
v_permlane32_swap_b32 v0,v3
v_max_f32 v0,v0,v3
v_mov_b32 v3,v0
s_nop 1
v_permlane16_swap_b32 v0,v3
v_max_f32 v0,v0,v3
v_mov_b32 v3,v1
s_nop 1
v_permlane32_swap_b32 v1,v3
v_max_f32 v1,v1,v3
v_mov_b32 v3,v1
s_nop 1
v_permlane16_swap_b32 v1,v3
v_max_f32 v1,v1,v3
v_mov_b32 v3,v2
s_nop 1
v_permlane32_swap_b32 v2,v3
v_max_f32 v2,v2,v3
v_mov_b32 v3,v2
s_nop 1
v_permlane16_swap_b32 v2,v3
v_max_f32 v2,v2,v3
v_xor_b32 v160,0x80000000,v0
v_xor_b32 v164,0x80000000,v1
v_xor_b32 v161,0x80000000,v0
v_xor_b32 v165,0x80000000,v1
v_xor_b32 v162,0x80000000,v0
v_xor_b32 v166,0x80000000,v1
v_xor_b32 v163,0x80000000,v0
v_xor_b32 v167,0x80000000,v1
v_xor_b32 v240,0x80000000,v2
s_waitcnt vmcnt(0) lgkmcnt(0)
s_barrier
s_mul_i32 s98,s47,12288
s_add_u32 s96,s48,s98
s_mul_i32 s88,s35,12288
s_add_u32 s97,s88,s49
s_mov_b32 s94,0
s_mov_b32 s95,0
s_mov_b32 m0,s96
s_nop 0
buffer_load_dwordx4 v251,s[64:67],s94 offen nt lds
s_add_u32 m0,s96,896
s_nop 0
buffer_load_dwordx4 v251,s[64:67],s94 offen offset:128 nt lds
s_add_u32 m0,s96,1792
s_nop 0
buffer_load_dwordx4 v251,s[64:67],s94 offen offset:256 nt lds
s_mov_b32 m0,s97
s_nop 0
buffer_load_dwordx4 v252,s[68:71],s95 offen nt lds
s_add_u32 m0,s97,896
s_nop 0
buffer_load_dwordx4 v252,s[68:71],s95 offen offset:128 nt lds
s_add_u32 m0,s97,1792
s_nop 0
buffer_load_dwordx4 v252,s[68:71],s95 offen offset:256 nt lds
s_add_u32 s94,s94,12288
s_add_u32 s89,s48,36864
s_add_u32 s96,s96,12288
s_cmp_eq_u32 s96,s89
s_cselect_b32 s96,s48,s96
s_add_u32 s95,s95,12288
s_add_u32 s89,s49,36864
s_add_u32 s97,s97,12288
s_cmp_eq_u32 s97,s89
s_cselect_b32 s97,s49,s97
s_mov_b32 m0,s96
s_nop 0
buffer_load_dwordx4 v251,s[64:67],s94 offen nt lds
s_add_u32 m0,s96,896
s_nop 0
buffer_load_dwordx4 v251,s[64:67],s94 offen offset:128 nt lds
s_add_u32 m0,s96,1792
s_nop 0
buffer_load_dwordx4 v251,s[64:67],s94 offen offset:256 nt lds
s_add_u32 s94,s94,12288
s_add_u32 s89,s48,36864
s_add_u32 s96,s96,12288
s_cmp_eq_u32 s96,s89
s_cselect_b32 s96,s48,s96
v_mbcnt_lo_u32_b32 v0,-1,0
v_mbcnt_hi_u32_b32 v0,-1,v0
v_and_b32 v1,15,v0
v_lshrrev_b32 v2,4,v0
v_lshrrev_b32 v0,3,v1
s_movk_i32 s88,3072
v_mul_u32_u24 v244,s88,v0
v_and_b32 v0,7,v1
v_lshl_add_u32 v244,v0,7,v244
v_lshrrev_b32 v0,1,v1
v_xor_b32 v0,v0,v2
v_lshl_add_u32 v244,v0,4,v244
v_xor_b32 v245,64,v244
s_add_u32 s88,s51,s98
v_add_u32 v244,s88,v244
v_add_u32 v245,s88,v245
v_mbcnt_lo_u32_b32 v0,-1,0
v_mbcnt_hi_u32_b32 v0,-1,v0
v_and_b32 v1,15,v0
v_lshrrev_b32 v2,4,v0
v_lshlrev_b32 v250,4,v0
v_add_u32 v250,s50,v250
s_movk_i32 s88,3072
v_lshrrev_b32 v3,1,v2
v_mul_u32_u24 v246,s88,v3
v_and_b32 v2,1,v2
v_lshrrev_b32 v1,2,v1
v_lshl_add_u32 v3,v2,2,v1
v_lshl_add_u32 v246,v3,7,v246
v_lshrrev_b32 v1,1,v1
v_lshl_or_b32 v1,v2,1,v1
v_xor_b32 v1,3,v1
v_lshl_add_u32 v246,v1,5,v246
v_and_b32 v0,3,v0
v_lshl_add_u32 v246,v0,3,v246
v_xor_b32 v247,32,v246
v_xor_b32 v248,64,v246
v_xor_b32 v249,0x60,v246
s_mul_i32 s99,s35,12288
s_add_u32 s88,s99,s51
s_add_u32 s88,s88,73728
v_add_u32 v246,s88,v246
v_add_u32 v247,s88,v247
v_add_u32 v248,s88,v248
v_add_u32 v249,s88,v249
s_waitcnt vmcnt(6) lgkmcnt(0)
s_barrier
s_branch W3_ATT
W3_EPI:
s_nop 3
v_mbcnt_lo_u32_b32 v0,-1,0
v_mbcnt_hi_u32_b32 v0,-1,v0
v_lshlrev_b32 v0,4,v0
s_branch LNEXT
LNEXT:
s_cmp_eq_u32 s87,1
s_cbranch_scc1 LEND
s_mov_b32 s87,1
s_xor_b32 s85,s85,2
s_mov_b32 s33,1
s_mov_b32 s35,2
s_mov_b32 s34,0
s_mul_i32 s88,s85,184320
s_add_u32 s56,s36,s88
s_addc_u32 s57,s37,0
s_and_b32 s57,s57,0xffff
s_mov_b32 s58,0x2d000
s_mov_b32 s59,0x20000
s_mul_i32 s88,s86,384
s_sub_u32 s89,0x780000,s88
s_add_u32 s60,s42,s88
s_addc_u32 s61,s43,0
s_and_b32 s61,s61,0xffff
s_mov_b32 s62,s89
s_mov_b32 s63,0x20000
s_lshl_b32 s88,s85,5
s_add_u32 s88,s88,s86
s_mul_i32 s88,s88,196608
s_add_u32 s64,s38,s88
s_addc_u32 s65,s39,0
s_and_b32 s65,s65,0xffff
s_mov_b32 s66,0x30000
s_mov_b32 s67,0x20000
s_add_u32 s68,s40,s88
s_addc_u32 s69,s41,0
s_and_b32 s69,s69,0xffff
s_mov_b32 s70,0x30000
s_mov_b32 s71,0x20000
s_mul_i32 s88,s85,1728
s_mul_i32 s89,s86,6
s_add_u32 s88,s88,s89
s_lshl_b32 s88,s88,10
s_add_u32 s72,s52,s88
s_addc_u32 s73,s53,0
s_and_b32 s73,s73,0xffff
s_mov_b32 s74,0x181800
s_mov_b32 s75,0x20000
s_branch LITEM
LEND:
s_waitcnt vmcnt(0) lgkmcnt(0)
s_barrier
s_or_b32 s88,s84,s47
s_cbranch_scc1 LNOSIG
s_lshl_b32 s88,s54,6
s_load_dwordx2 s[90:91],%0,88
s_waitcnt lgkmcnt(0)
s_add_u32 s76,s90,s88
s_addc_u32 s77,s91,0
s_and_b32 s77,s77,0xffff
s_mov_b32 s78,0x40
s_mov_b32 s79,0x20000
v_mov_b32 v0,s30
v_mov_b32 v1,4
s_mov_b64 exec,1
buffer_store_dword v0,v1,s[76:79],0 offen sc1
s_mov_b64 exec,-1
LNOSIG:
s_lshl_b32 s92,s47,1
s_xor_b32 s92,s92,s84
s_lshl_b32 s93,s47,2
s_add_u32 s45,s92,s93
s_load_dwordx2 s[34:35],%0,64
s_load_dwordx2 s[36:37],%0,72
s_load_dwordx2 s[64:65],%0,80
s_load_dwordx2 s[42:43],%0,88
s_load_dwordx2 s[38:39],%0,96
s_load_dwordx2 s[40:41],%0,104
s_mov_b32 s44,%1
s_waitcnt lgkmcnt(0)
s_and_b32 s68,s44,7
s_lshr_b32 s69,s44,3
s_and_b32 s48,s68,1
s_lshr_b32 s80,s68,1
s_and_b32 s80,s80,1
s_lshr_b32 s82,s69,2
s_lshr_b32 s70,s68,2
s_lshl_b32 s70,s70,3
s_add_u32 s81,s70,s82
s_and_b32 s49,s69,3
s_lshl_b32 s50,s80,4
s_add_u32 s50,s50,s81
s_cmp_eq_u32 s80,1
s_cselect_b64 s[36:37],s[64:65],s[36:37]
s_lshr_b32 s46,s45,2
s_and_b32 s47,s45,3
s_mul_i32 s68,s50,1769472
s_add_u32 s52,s34,s68
s_addc_u32 s53,s35,0
s_and_b32 s53,s53,0xffff
s_mov_b32 s54,0x1b0000
s_mov_b32 s55,0x20000
s_mul_i32 s68,s49,320
s_sub_u32 s69,0x780000,s68
s_add_u32 s56,s36,s68
s_addc_u32 s57,s37,0
s_and_b32 s57,s57,0xffff
s_mov_b32 s58,s69
s_mov_b32 s59,0x20000
v_mbcnt_lo_u32_b32 v249,-1,0
v_mbcnt_hi_u32_b32 v249,-1,v249
v_lshlrev_b32 v240,4,v249
v_add_u32 v250,0,v249
s_movk_i32 s68,3277
v_mul_u32_u24 v251,s68,v250
v_lshrrev_b32 v251,16,v251
v_mul_u32_u24 v254,20,v251
v_sub_u32 v250,v250,v254
v_lshrrev_b32 v254,1,v251
v_and_b32 v254,2,v254
v_xor_b32 v250,v250,v254
v_lshlrev_b32 v250,4,v250
s_movk_i32 s68,1280
v_mad_u32_u24 v241,v251,s68,v250
v_add_u32 v250,64,v249
s_movk_i32 s68,3277
v_mul_u32_u24 v251,s68,v250
v_lshrrev_b32 v251,16,v251
v_mul_u32_u24 v254,20,v251
v_sub_u32 v250,v250,v254
v_lshrrev_b32 v254,1,v251
v_and_b32 v254,2,v254
v_xor_b32 v250,v250,v254
v_lshlrev_b32 v250,4,v250
s_movk_i32 s68,1280
v_mad_u32_u24 v242,v251,s68,v250
v_add_u32 v250,0x80,v249
s_movk_i32 s68,3277
v_mul_u32_u24 v251,s68,v250
v_lshrrev_b32 v251,16,v251
v_mul_u32_u24 v254,20,v251
v_sub_u32 v250,v250,v254
v_lshrrev_b32 v254,1,v251
v_and_b32 v254,2,v254
v_xor_b32 v250,v250,v254
v_lshlrev_b32 v250,4,v250
s_movk_i32 s68,1280
v_mad_u32_u24 v243,v251,s68,v250
v_add_u32 v250,0xc0,v249
s_movk_i32 s68,3277
v_mul_u32_u24 v251,s68,v250
v_lshrrev_b32 v251,16,v251
v_mul_u32_u24 v254,20,v251
v_sub_u32 v250,v250,v254
v_lshrrev_b32 v254,1,v251
v_and_b32 v254,2,v254
v_xor_b32 v250,v250,v254
v_lshlrev_b32 v250,4,v250
s_movk_i32 s68,1280
v_mad_u32_u24 v244,v251,s68,v250
v_add_u32 v250,0x100,v249
s_movk_i32 s68,3277
v_mul_u32_u24 v251,s68,v250
v_lshrrev_b32 v251,16,v251
v_mul_u32_u24 v254,20,v251
v_sub_u32 v250,v250,v254
v_lshrrev_b32 v254,1,v251
v_and_b32 v254,2,v254
v_xor_b32 v250,v250,v254
v_lshlrev_b32 v250,4,v250
s_movk_i32 s68,1280
v_mad_u32_u24 v245,v251,s68,v250
s_mul_i32 s68,s48,96
s_add_u32 s68,s68,s47
s_mul_i32 s69,s46,960
s_add_u32 s69,s69,s68
s_lshl_b32 s74,s69,10
s_lshl_b32 s69,s68,5
s_lshl_b32 s70,s46,4
s_add_u32 s69,s69,s70
s_mul_i32 s75,s69,1280
s_mul_i32 s76,s47,9216
s_mul_i32 s68,s46,5120
s_add_u32 s76,s76,s68
s_mul_i32 s77,s47,10240
s_add_u32 s77,s77,s68
s_add_u32 s77,s77,36864
s_lshr_b32 s90,s81,1
s_and_b32 s90,s90,1
s_mul_i32 s90,s90,12
s_mul_i32 s68,s90,4096
s_add_u32 s74,s74,s68
s_mul_i32 s68,s90,163840
s_add_u32 s75,s75,s68
s_add_u32 m0,s77,0
s_nop 0
buffer_load_dwordx4 v241,s[56:59],s75 offen lds
s_add_u32 m0,s77,1024
s_nop 0
buffer_load_dwordx4 v242,s[56:59],s75 offen lds
s_add_u32 m0,s77,2048
s_nop 0
buffer_load_dwordx4 v243,s[56:59],s75 offen lds
s_add_u32 m0,s77,3072
s_nop 0
buffer_load_dwordx4 v244,s[56:59],s75 offen lds
s_add_u32 m0,s77,4096
s_nop 0
buffer_load_dwordx4 v245,s[56:59],s75 offen lds
s_cmp_lg_u32 s45,0
s_cbranch_scc1 K2D0_RDY
v_mbcnt_lo_u32_b32 v250,-1,0
v_mbcnt_hi_u32_b32 v250,-1,v250
v_and_b32 v251,7,v250
v_lshlrev_b32 v251,5,v251
s_lshr_b32 s68,s81,2
s_lshl_b32 s68,s68,3
s_lshl_b32 s69,s80,2
s_add_u32 s68,s68,s69
s_lshl_b32 s69,s48,1
)A"
R"A(s_add_u32 s68,s68,s69
s_lshr_b32 s69,s81,1
s_and_b32 s69,s69,1
s_add_u32 s68,s68,s69
v_add_u32 v251,s68,v251
v_lshlrev_b32 v251,6,v251
v_add_u32 v251,0,v251
s_mov_b32 s68,0
s_add_u32 s60,s42,s68
s_addc_u32 s61,s43,0
s_and_b32 s61,s61,0xffff
s_mov_b32 s62,0x4000
s_mov_b32 s63,0x20000
s_mov_b32 s70,0x4000
K2D0_WAIT:
buffer_load_dword v254,v251,s[60:63],0 offen sc1
s_waitcnt vmcnt(0)
v_cmp_eq_u32 vcc,s30,v254
s_nop 1
s_and_b32 s92,vcc_lo,0xff
s_cmp_eq_u32 s92,0xff
s_cbranch_scc1 K2D0_GOT
s_sleep 2
s_sub_u32 s70,s70,1
s_cmp_lg_u32 s70,0
s_cbranch_scc1 K2D0_WAIT
K2D0_GOT:
buffer_inv sc1
s_waitcnt vmcnt(0)
K2D0_RDY:
s_barrier
s_add_u32 m0,s76,0
s_add_u32 s68,s74,0
buffer_load_dwordx4 v240,s[52:55],s68 offen lds
s_add_u32 m0,s76,1024
s_add_u32 s68,s74,196608
buffer_load_dwordx4 v240,s[52:55],s68 offen lds
s_add_u32 m0,s76,2048
s_add_u32 s68,s74,393216
buffer_load_dwordx4 v240,s[52:55],s68 offen lds
s_add_u32 m0,s76,3072
s_add_u32 s68,s74,589824
buffer_load_dwordx4 v240,s[52:55],s68 offen lds
s_add_u32 m0,s76,4096
s_add_u32 s68,s74,786432
s_cmp_eq_u32 s46,1
s_cbranch_scc1 K2O71
buffer_load_dwordx4 v240,s[52:55],s68 offen lds
K2O71:
s_add_u32 s90,s90,1
s_cmp_eq_u32 s90,24
s_cselect_b32 s69,98304,0
s_sub_u32 s74,s74,s69
s_cmp_eq_u32 s90,24
s_cselect_b32 s69,3932160,0
s_sub_u32 s75,s75,s69
s_cmp_eq_u32 s90,24
s_cmov_b32 s90,0
s_add_u32 s74,s74,4096
s_add_u32 s75,s75,163840
s_mov_b32 s78,77824
s_add_u32 s76,s76,77824
s_add_u32 s77,s77,77824
v_and_b32 v250,15,v249
v_lshrrev_b32 v251,4,v249
v_lshrrev_b32 v250,2,v250
v_lshl_add_u32 v250,v251,2,v250
s_movk_i32 s68,320
v_mul_u32_u24 v246,s68,v250
v_and_b32 v250,3,v249
v_lshl_add_u32 v246,v250,3,v246
s_mul_i32 s68,s47,10240
s_mul_i32 s69,s46,160
s_add_u32 s68,s68,s69
s_add_u32 s68,s68,36864
v_add_u32 v246,s68,v246
v_and_b32 v251,1,v251
v_lshlrev_b32 v251,5,v251
v_sub_u32 v254,0,v251
s_cmp_eq_u32 s46,0
s_cselect_b64 vcc,-1,0
v_cndmask_b32 v250,v254,v251,vcc
v_sub_u32 v247,v246,v250
v_add_u32 v246,v246,v250
s_mul_i32 s68,s47,9216
v_add_u32 v248,s68,v240
v_mov_b32 v140,0
v_mov_b32 v141,0
v_mov_b32 v142,0
v_mov_b32 v143,0
v_mov_b32 v144,0
v_mov_b32 v145,0
v_mov_b32 v146,0
v_mov_b32 v147,0
v_mov_b32 v148,0
v_mov_b32 v149,0
v_mov_b32 v150,0
v_mov_b32 v151,0
v_mov_b32 v152,0
v_mov_b32 v153,0
v_mov_b32 v154,0
v_mov_b32 v155,0
v_mov_b32 v156,0
v_mov_b32 v157,0
v_mov_b32 v158,0
v_mov_b32 v159,0
v_mov_b32 v160,0
v_mov_b32 v161,0
v_mov_b32 v162,0
v_mov_b32 v163,0
v_mov_b32 v164,0
v_mov_b32 v165,0
v_mov_b32 v166,0
v_mov_b32 v167,0
v_mov_b32 v168,0
v_mov_b32 v169,0
v_mov_b32 v170,0
v_mov_b32 v171,0
v_mov_b32 v172,0
v_mov_b32 v173,0
v_mov_b32 v174,0
v_mov_b32 v175,0
v_mov_b32 v176,0
v_mov_b32 v177,0
v_mov_b32 v178,0
v_mov_b32 v179,0
s_waitcnt vmcnt(0) lgkmcnt(0)
s_barrier
ds_read_b64_tr_b16 v[180:181],v246
ds_read_b64_tr_b16 v[182:183],v246 offset:5120
ds_read_b128 v[220:223],v248
ds_read_b64_tr_b16 v[184:185],v247 offset:32
ds_read_b64_tr_b16 v[186:187],v247 offset:5152
ds_read_b64_tr_b16 v[188:189],v246 offset:64
ds_read_b64_tr_b16 v[190:191],v246 offset:5184
ds_read_b64_tr_b16 v[192:193],v247 offset:96
ds_read_b64_tr_b16 v[194:195],v247 offset:5216
ds_read_b64_tr_b16 v[196:197],v246 offset:128
ds_read_b64_tr_b16 v[198:199],v246 offset:5248
ds_read_b128 v[224:227],v248 offset:1024
ds_read_b128 v[228:231],v248 offset:2048
s_waitcnt lgkmcnt(10)
v_mfma_f32_16x16x32_bf16 v[0:3],v[180:183],v[220:223],0
s_add_u32 m0,s77,0
s_waitcnt lgkmcnt(8)
v_mfma_f32_16x16x32_bf16 v[4:7],v[184:187],v[220:223],0
buffer_load_dwordx4 v241,s[56:59],s75 offen lds
s_waitcnt lgkmcnt(6)
v_mfma_f32_16x16x32_bf16 v[8:11],v[188:191],v[220:223],0
s_add_u32 m0,s77,1024
s_waitcnt lgkmcnt(4)
v_mfma_f32_16x16x32_bf16 v[12:15],v[192:195],v[220:223],0
buffer_load_dwordx4 v242,s[56:59],s75 offen lds
s_waitcnt lgkmcnt(2)
v_mfma_f32_16x16x32_bf16 v[16:19],v[196:199],v[220:223],0
ds_read_b128 v[220:223],v248 offset:3072
s_add_u32 m0,s77,2048
s_waitcnt lgkmcnt(2)
v_mfma_f32_16x16x32_bf16 v[20:23],v[180:183],v[224:227],0
buffer_load_dwordx4 v243,s[56:59],s75 offen lds
v_mfma_f32_16x16x32_bf16 v[24:27],v[184:187],v[224:227],0
s_add_u32 m0,s77,3072
v_mfma_f32_16x16x32_bf16 v[28:31],v[188:191],v[224:227],0
buffer_load_dwordx4 v244,s[56:59],s75 offen lds
v_mfma_f32_16x16x32_bf16 v[32:35],v[192:195],v[224:227],0
s_add_u32 m0,s77,4096
v_mfma_f32_16x16x32_bf16 v[36:39],v[196:199],v[224:227],0
ds_read_b128 v[224:227],v248 offset:4096
buffer_load_dwordx4 v245,s[56:59],s75 offen lds
s_waitcnt lgkmcnt(2)
v_mfma_f32_16x16x32_bf16 v[40:43],v[180:183],v[228:231],0
s_add_u32 m0,s76,0
s_add_u32 s68,s74,0
v_mfma_f32_16x16x32_bf16 v[44:47],v[184:187],v[228:231],0
buffer_load_dwordx4 v240,s[52:55],s68 offen lds
v_mfma_f32_16x16x32_bf16 v[48:51],v[188:191],v[228:231],0
s_add_u32 m0,s76,1024
s_add_u32 s68,s74,196608
v_mfma_f32_16x16x32_bf16 v[52:55],v[192:195],v[228:231],0
buffer_load_dwordx4 v240,s[52:55],s68 offen lds
v_mfma_f32_16x16x32_bf16 v[56:59],v[196:199],v[228:231],0
ds_read_b128 v[228:231],v248 offset:5120
s_add_u32 m0,s76,2048
s_add_u32 s68,s74,393216
s_waitcnt lgkmcnt(2)
v_mfma_f32_16x16x32_bf16 v[60:63],v[180:183],v[220:223],0
ds_read_b128 v[232:235],v248 offset:7168
buffer_load_dwordx4 v240,s[52:55],s68 offen lds
v_mfma_f32_16x16x32_bf16 v[64:67],v[184:187],v[220:223],0
s_add_u32 m0,s76,3072
s_add_u32 s68,s74,589824
v_mfma_f32_16x16x32_bf16 v[68:71],v[188:191],v[220:223],0
buffer_load_dwordx4 v240,s[52:55],s68 offen lds
v_mfma_f32_16x16x32_bf16 v[72:75],v[192:195],v[220:223],0
s_add_u32 m0,s76,4096
s_add_u32 s68,s74,786432
v_mfma_f32_16x16x32_bf16 v[76:79],v[196:199],v[220:223],0
ds_read_b128 v[220:223],v248 offset:6144
s_cmp_eq_u32 s46,1
s_cbranch_scc1 K2O72
buffer_load_dwordx4 v240,s[52:55],s68 offen lds
K2O72:
s_waitcnt lgkmcnt(3)
v_mfma_f32_16x16x32_bf16 v[80:83],v[180:183],v[224:227],0
ds_read_b128 v[236:239],v248 offset:8192
v_mfma_f32_16x16x32_bf16 v[84:87],v[184:187],v[224:227],0
s_add_u32 s90,s90,1
s_cmp_eq_u32 s90,24
s_cselect_b32 s69,98304,0
s_sub_u32 s74,s74,s69
s_cmp_eq_u32 s90,24
s_cselect_b32 s69,3932160,0
s_sub_u32 s75,s75,s69
s_cmp_eq_u32 s90,24
s_cmov_b32 s90,0
s_add_u32 s74,s74,4096
s_add_u32 s75,s75,163840
v_mfma_f32_16x16x32_bf16 v[88:91],v[188:191],v[224:227],0
v_add_u32 v246,s78,v246
v_add_u32 v247,s78,v247
v_add_u32 v248,s78,v248
v_mfma_f32_16x16x32_bf16 v[92:95],v[192:195],v[224:227],0
s_sub_u32 s76,s76,s78
s_sub_u32 s77,s77,s78
s_sub_u32 s78,0,s78
v_mfma_f32_16x16x32_bf16 v[96:99],v[196:199],v[224:227],0
s_waitcnt lgkmcnt(3)
v_mfma_f32_16x16x32_bf16 v[100:103],v[180:183],v[228:231],0
v_mfma_f32_16x16x32_bf16 v[104:107],v[184:187],v[228:231],0
v_mfma_f32_16x16x32_bf16 v[108:111],v[188:191],v[228:231],0
v_mfma_f32_16x16x32_bf16 v[112:115],v[192:195],v[228:231],0
v_mfma_f32_16x16x32_bf16 v[116:119],v[196:199],v[228:231],0
s_waitcnt lgkmcnt(1)
v_mfma_f32_16x16x32_bf16 v[120:123],v[180:183],v[220:223],0
v_mfma_f32_16x16x32_bf16 v[124:127],v[184:187],v[220:223],0
v_mfma_f32_16x16x32_bf16 v[128:131],v[188:191],v[220:223],0
v_mfma_f32_16x16x32_bf16 v[132:135],v[192:195],v[220:223],0
v_mfma_f32_16x16x32_bf16 v[136:139],v[196:199],v[220:223],0
s_mov_b32 s79,11
K2_LOOP:
s_waitcnt vmcnt(0) lgkmcnt(0)
s_barrier
ds_read_b64_tr_b16 v[200:201],v246
ds_read_b64_tr_b16 v[202:203],v246 offset:5120
ds_read_b128 v[220:223],v248
v_mfma_f32_16x16x32_bf16 v[140:143],v[180:183],v[232:235],v[140:143]
ds_read_b64_tr_b16 v[204:205],v247 offset:32
s_add_u32 m0,s77,0
v_mfma_f32_16x16x32_bf16 v[144:147],v[184:187],v[232:235],v[144:147]
ds_read_b64_tr_b16 v[206:207],v247 offset:5152
buffer_load_dwordx4 v241,s[56:59],s75 offen lds
v_mfma_f32_16x16x32_bf16 v[148:151],v[188:191],v[232:235],v[148:151]
ds_read_b64_tr_b16 v[208:209],v246 offset:64
s_add_u32 m0,s77,1024
v_mfma_f32_16x16x32_bf16 v[152:155],v[192:195],v[232:235],v[152:155]
ds_read_b64_tr_b16 v[210:211],v246 offset:5184
buffer_load_dwordx4 v242,s[56:59],s75 offen lds
v_mfma_f32_16x16x32_bf16 v[156:159],v[196:199],v[232:235],v[156:159]
ds_read_b64_tr_b16 v[212:213],v247 offset:96
s_add_u32 m0,s77,2048
v_mfma_f32_16x16x32_bf16 v[160:163],v[180:183],v[236:239],v[160:163]
ds_read_b64_tr_b16 v[214:215],v247 offset:5216
buffer_load_dwordx4 v243,s[56:59],s75 offen lds
v_mfma_f32_16x16x32_bf16 v[164:167],v[184:187],v[236:239],v[164:167]
ds_read_b64_tr_b16 v[216:217],v246 offset:128
s_add_u32 m0,s77,3072
v_mfma_f32_16x16x32_bf16 v[168:171],v[188:191],v[236:239],v[168:171]
ds_read_b64_tr_b16 v[218:219],v246 offset:5248
buffer_load_dwordx4 v244,s[56:59],s75 offen lds
v_mfma_f32_16x16x32_bf16 v[172:175],v[192:195],v[236:239],v[172:175]
ds_read_b128 v[224:227],v248 offset:1024
s_add_u32 m0,s77,4096
v_mfma_f32_16x16x32_bf16 v[176:179],v[196:199],v[236:239],v[176:179]
ds_read_b128 v[228:231],v248 offset:2048
buffer_load_dwordx4 v245,s[56:59],s75 offen lds
s_waitcnt lgkmcnt(10)
v_mfma_f32_16x16x32_bf16 v[0:3],v[200:203],v[220:223],v[0:3]
s_add_u32 m0,s76,0
s_add_u32 s68,s74,0
s_waitcnt lgkmcnt(8)
v_mfma_f32_16x16x32_bf16 v[4:7],v[204:207],v[220:223],v[4:7]
buffer_load_dwordx4 v240,s[52:55],s68 offen lds
s_waitcnt lgkmcnt(6)
v_mfma_f32_16x16x32_bf16 v[8:11],v[208:211],v[220:223],v[8:11]
s_add_u32 m0,s76,1024
s_add_u32 s68,s74,196608
s_waitcnt lgkmcnt(4)
v_mfma_f32_16x16x32_bf16 v[12:15],v[212:215],v[220:223],v[12:15]
buffer_load_dwordx4 v240,s[52:55],s68 offen lds
s_waitcnt lgkmcnt(2)
v_mfma_f32_16x16x32_bf16 v[16:19],v[216:219],v[220:223],v[16:19]
ds_read_b128 v[220:223],v248 offset:3072
s_add_u32 m0,s76,2048
s_add_u32 s68,s74,393216
s_waitcnt lgkmcnt(2)
v_mfma_f32_16x16x32_bf16 v[20:23],v[200:203],v[224:227],v[20:23]
buffer_load_dwordx4 v240,s[52:55],s68 offen lds
v_mfma_f32_16x16x32_bf16 v[24:27],v[204:207],v[224:227],v[24:27]
s_add_u32 m0,s76,3072
s_add_u32 s68,s74,589824
v_mfma_f32_16x16x32_bf16 v[28:31],v[208:211],v[224:227],v[28:31]
buffer_load_dwordx4 v240,s[52:55],s68 offen lds
v_mfma_f32_16x16x32_bf16 v[32:35],v[212:215],v[224:227],v[32:35]
s_add_u32 m0,s76,4096
s_add_u32 s68,s74,786432
v_mfma_f32_16x16x32_bf16 v[36:39],v[216:219],v[224:227],v[36:39]
ds_read_b128 v[224:227],v248 offset:4096
s_cmp_eq_u32 s46,1
s_cbranch_scc1 K2O73
buffer_load_dwordx4 v240,s[52:55],s68 offen lds
K2O73:
s_waitcnt lgkmcnt(2)
v_mfma_f32_16x16x32_bf16 v[40:43],v[200:203],v[228:231],v[40:43]
v_mfma_f32_16x16x32_bf16 v[44:47],v[204:207],v[228:231],v[44:47]
v_mfma_f32_16x16x32_bf16 v[48:51],v[208:211],v[228:231],v[48:51]
v_mfma_f32_16x16x32_bf16 v[52:55],v[212:215],v[228:231],v[52:55]
v_mfma_f32_16x16x32_bf16 v[56:59],v[216:219],v[228:231],v[56:59]
ds_read_b128 v[228:231],v248 offset:5120
s_waitcnt lgkmcnt(2)
v_mfma_f32_16x16x32_bf16 v[60:63],v[200:203],v[220:223],v[60:63]
ds_read_b128 v[232:235],v248 offset:7168
v_mfma_f32_16x16x32_bf16 v[64:67],v[204:207],v[220:223],v[64:67]
v_mfma_f32_16x16x32_bf16 v[68:71],v[208:211],v[220:223],v[68:71]
v_mfma_f32_16x16x32_bf16 v[72:75],v[212:215],v[220:223],v[72:75]
v_mfma_f32_16x16x32_bf16 v[76:79],v[216:219],v[220:223],v[76:79]
ds_read_b128 v[220:223],v248 offset:6144
s_waitcnt lgkmcnt(3)
v_mfma_f32_16x16x32_bf16 v[80:83],v[200:203],v[224:227],v[80:83]
ds_read_b128 v[236:239],v248 offset:8192
v_mfma_f32_16x16x32_bf16 v[84:87],v[204:207],v[224:227],v[84:87]
s_add_u32 s90,s90,1
s_cmp_eq_u32 s90,24
s_cselect_b32 s69,98304,0
s_sub_u32 s74,s74,s69
s_cmp_eq_u32 s90,24
s_cselect_b32 s69,3932160,0
s_sub_u32 s75,s75,s69
s_cmp_eq_u32 s90,24
s_cmov_b32 s90,0
s_add_u32 s74,s74,4096
s_add_u32 s75,s75,163840
v_mfma_f32_16x16x32_bf16 v[88:91],v[208:211],v[224:227],v[88:91]
v_add_u32 v246,s78,v246
v_add_u32 v247,s78,v247
v_add_u32 v248,s78,v248
v_mfma_f32_16x16x32_bf16 v[92:95],v[212:215],v[224:227],v[92:95]
s_sub_u32 s76,s76,s78
s_sub_u32 s77,s77,s78
s_sub_u32 s78,0,s78
v_mfma_f32_16x16x32_bf16 v[96:99],v[216:219],v[224:227],v[96:99]
s_waitcnt lgkmcnt(3)
v_mfma_f32_16x16x32_bf16 v[100:103],v[200:203],v[228:231],v[100:103]
v_mfma_f32_16x16x32_bf16 v[104:107],v[204:207],v[228:231],v[104:107]
v_mfma_f32_16x16x32_bf16 v[108:111],v[208:211],v[228:231],v[108:111]
v_mfma_f32_16x16x32_bf16 v[112:115],v[212:215],v[228:231],v[112:115]
v_mfma_f32_16x16x32_bf16 v[116:119],v[216:219],v[228:231],v[116:119]
s_waitcnt lgkmcnt(1)
v_mfma_f32_16x16x32_bf16 v[120:123],v[200:203],v[220:223],v[120:123]
v_mfma_f32_16x16x32_bf16 v[124:127],v[204:207],v[220:223],v[124:127]
v_mfma_f32_16x16x32_bf16 v[128:131],v[208:211],v[220:223],v[128:131]
v_mfma_f32_16x16x32_bf16 v[132:135],v[212:215],v[220:223],v[132:135]
v_mfma_f32_16x16x32_bf16 v[136:139],v[216:219],v[220:223],v[136:139]
s_waitcnt vmcnt(0) lgkmcnt(0)
s_barrier
ds_read_b64_tr_b16 v[180:181],v246
ds_read_b64_tr_b16 v[182:183],v246 offset:5120
ds_read_b128 v[220:223],v248
v_mfma_f32_16x16x32_bf16 v[140:143],v[200:203],v[232:235],v[140:143]
ds_read_b64_tr_b16 v[184:185],v247 offset:32
s_add_u32 m0,s77,0
v_mfma_f32_16x16x32_bf16 v[144:147],v[204:207],v[232:235],v[144:147]
ds_read_b64_tr_b16 v[186:187],v247 offset:5152
buffer_load_dwordx4 v241,s[56:59],s75 offen lds
v_mfma_f32_16x16x32_bf16 v[148:151],v[208:211],v[232:235],v[148:151]
ds_read_b64_tr_b16 v[188:189],v246 offset:64
s_add_u32 m0,s77,1024
v_mfma_f32_16x16x32_bf16 v[152:155],v[212:215],v[232:235],v[152:155]
ds_read_b64_tr_b16 v[190:191],v246 offset:5184
buffer_load_dwordx4 v242,s[56:59],s75 offen lds
v_mfma_f32_16x16x32_bf16 v[156:159],v[216:219],v[232:235],v[156:159]
ds_read_b64_tr_b16 v[192:193],v247 offset:96
s_add_u32 m0,s77,2048
v_mfma_f32_16x16x32_bf16 v[160:163],v[200:203],v[236:239],v[160:163]
ds_read_b64_tr_b16 v[194:195],v247 offset:5216
buffer_load_dwordx4 v243,s[56:59],s75 offen lds
v_mfma_f32_16x16x32_bf16 v[164:167],v[204:207],v[236:239],v[164:167]
ds_read_b64_tr_b16 v[196:197],v246 offset:128
s_add_u32 m0,s77,3072
v_mfma_f32_16x16x32_bf16 v[168:171],v[208:211],v[236:239],v[168:171]
ds_read_b64_tr_b16 v[198:199],v246 offset:5248
buffer_load_dwordx4 v244,s[56:59],s75 offen lds
v_mfma_f32_16x16x32_bf16 v[172:175],v[212:215],v[236:239],v[172:175]
ds_read_b128 v[224:227],v248 offset:1024
s_add_u32 m0,s77,4096
v_mfma_f32_16x16x32_bf16 v[176:179],v[216:219],v[236:239],v[176:179]
ds_read_b128 v[228:231],v248 offset:2048
buffer_load_dwordx4 v245,s[56:59],s75 offen lds
s_waitcnt lgkmcnt(10)
v_mfma_f32_16x16x32_bf16 v[0:3],v[180:183],v[220:223],v[0:3]
s_add_u32 m0,s76,0
)A"
R"A(s_add_u32 s68,s74,0
s_waitcnt lgkmcnt(8)
v_mfma_f32_16x16x32_bf16 v[4:7],v[184:187],v[220:223],v[4:7]
buffer_load_dwordx4 v240,s[52:55],s68 offen lds
s_waitcnt lgkmcnt(6)
v_mfma_f32_16x16x32_bf16 v[8:11],v[188:191],v[220:223],v[8:11]
s_add_u32 m0,s76,1024
s_add_u32 s68,s74,196608
s_waitcnt lgkmcnt(4)
v_mfma_f32_16x16x32_bf16 v[12:15],v[192:195],v[220:223],v[12:15]
buffer_load_dwordx4 v240,s[52:55],s68 offen lds
s_waitcnt lgkmcnt(2)
v_mfma_f32_16x16x32_bf16 v[16:19],v[196:199],v[220:223],v[16:19]
ds_read_b128 v[220:223],v248 offset:3072
s_add_u32 m0,s76,2048
s_add_u32 s68,s74,393216
s_waitcnt lgkmcnt(2)
v_mfma_f32_16x16x32_bf16 v[20:23],v[180:183],v[224:227],v[20:23]
buffer_load_dwordx4 v240,s[52:55],s68 offen lds
v_mfma_f32_16x16x32_bf16 v[24:27],v[184:187],v[224:227],v[24:27]
s_add_u32 m0,s76,3072
s_add_u32 s68,s74,589824
v_mfma_f32_16x16x32_bf16 v[28:31],v[188:191],v[224:227],v[28:31]
buffer_load_dwordx4 v240,s[52:55],s68 offen lds
v_mfma_f32_16x16x32_bf16 v[32:35],v[192:195],v[224:227],v[32:35]
s_add_u32 m0,s76,4096
s_add_u32 s68,s74,786432
v_mfma_f32_16x16x32_bf16 v[36:39],v[196:199],v[224:227],v[36:39]
ds_read_b128 v[224:227],v248 offset:4096
s_cmp_eq_u32 s46,1
s_cbranch_scc1 K2O74
buffer_load_dwordx4 v240,s[52:55],s68 offen lds
K2O74:
s_waitcnt lgkmcnt(2)
v_mfma_f32_16x16x32_bf16 v[40:43],v[180:183],v[228:231],v[40:43]
v_mfma_f32_16x16x32_bf16 v[44:47],v[184:187],v[228:231],v[44:47]
v_mfma_f32_16x16x32_bf16 v[48:51],v[188:191],v[228:231],v[48:51]
v_mfma_f32_16x16x32_bf16 v[52:55],v[192:195],v[228:231],v[52:55]
v_mfma_f32_16x16x32_bf16 v[56:59],v[196:199],v[228:231],v[56:59]
ds_read_b128 v[228:231],v248 offset:5120
s_waitcnt lgkmcnt(2)
v_mfma_f32_16x16x32_bf16 v[60:63],v[180:183],v[220:223],v[60:63]
ds_read_b128 v[232:235],v248 offset:7168
v_mfma_f32_16x16x32_bf16 v[64:67],v[184:187],v[220:223],v[64:67]
v_mfma_f32_16x16x32_bf16 v[68:71],v[188:191],v[220:223],v[68:71]
v_mfma_f32_16x16x32_bf16 v[72:75],v[192:195],v[220:223],v[72:75]
v_mfma_f32_16x16x32_bf16 v[76:79],v[196:199],v[220:223],v[76:79]
ds_read_b128 v[220:223],v248 offset:6144
s_waitcnt lgkmcnt(3)
v_mfma_f32_16x16x32_bf16 v[80:83],v[180:183],v[224:227],v[80:83]
ds_read_b128 v[236:239],v248 offset:8192
v_mfma_f32_16x16x32_bf16 v[84:87],v[184:187],v[224:227],v[84:87]
s_add_u32 s90,s90,1
s_cmp_eq_u32 s90,24
s_cselect_b32 s69,98304,0
s_sub_u32 s74,s74,s69
s_cmp_eq_u32 s90,24
s_cselect_b32 s69,3932160,0
s_sub_u32 s75,s75,s69
s_cmp_eq_u32 s90,24
s_cmov_b32 s90,0
s_add_u32 s74,s74,4096
s_add_u32 s75,s75,163840
v_mfma_f32_16x16x32_bf16 v[88:91],v[188:191],v[224:227],v[88:91]
v_add_u32 v246,s78,v246
v_add_u32 v247,s78,v247
v_add_u32 v248,s78,v248
v_mfma_f32_16x16x32_bf16 v[92:95],v[192:195],v[224:227],v[92:95]
s_sub_u32 s76,s76,s78
s_sub_u32 s77,s77,s78
s_sub_u32 s78,0,s78
s_cmp_lg_u32 s79,7
s_cbranch_scc1 K2D1_75_SKIP
s_cmp_lg_u32 s45,0
s_cbranch_scc1 K2D1_75_RDY
v_mbcnt_lo_u32_b32 v250,-1,0
v_mbcnt_hi_u32_b32 v250,-1,v250
v_and_b32 v251,7,v250
v_lshlrev_b32 v251,5,v251
s_lshr_b32 s68,s81,2
s_lshl_b32 s68,s68,3
s_lshl_b32 s69,s80,2
s_add_u32 s68,s68,s69
s_lshl_b32 s69,s48,1
s_add_u32 s68,s68,s69
s_lshr_b32 s69,s81,1
s_and_b32 s69,s69,1
s_xor_b32 s69,s69,1
s_add_u32 s68,s68,s69
v_add_u32 v251,s68,v251
v_lshlrev_b32 v251,6,v251
v_add_u32 v251,4,v251
s_mov_b32 s68,0
s_add_u32 s60,s42,s68
s_addc_u32 s61,s43,0
s_and_b32 s61,s61,0xffff
s_mov_b32 s62,0x4000
s_mov_b32 s63,0x20000
s_mov_b32 s70,0x4000
K2D1_75_WAIT:
buffer_load_dword v254,v251,s[60:63],0 offen sc1
s_waitcnt vmcnt(0)
v_cmp_eq_u32 vcc,s30,v254
s_nop 1
s_and_b32 s92,vcc_lo,0xff
s_cmp_eq_u32 s92,0xff
s_cbranch_scc1 K2D1_75_GOT
s_sleep 2
s_sub_u32 s70,s70,1
s_cmp_lg_u32 s70,0
s_cbranch_scc1 K2D1_75_WAIT
K2D1_75_GOT:
buffer_inv sc1
s_waitcnt vmcnt(0)
K2D1_75_RDY:
K2D1_75_SKIP:
v_mfma_f32_16x16x32_bf16 v[96:99],v[196:199],v[224:227],v[96:99]
s_sub_u32 s79,s79,1
s_waitcnt lgkmcnt(3)
v_mfma_f32_16x16x32_bf16 v[100:103],v[180:183],v[228:231],v[100:103]
v_mfma_f32_16x16x32_bf16 v[104:107],v[184:187],v[228:231],v[104:107]
v_mfma_f32_16x16x32_bf16 v[108:111],v[188:191],v[228:231],v[108:111]
v_mfma_f32_16x16x32_bf16 v[112:115],v[192:195],v[228:231],v[112:115]
v_mfma_f32_16x16x32_bf16 v[116:119],v[196:199],v[228:231],v[116:119]
s_waitcnt lgkmcnt(1)
v_mfma_f32_16x16x32_bf16 v[120:123],v[180:183],v[220:223],v[120:123]
v_mfma_f32_16x16x32_bf16 v[124:127],v[184:187],v[220:223],v[124:127]
v_mfma_f32_16x16x32_bf16 v[128:131],v[188:191],v[220:223],v[128:131]
v_mfma_f32_16x16x32_bf16 v[132:135],v[192:195],v[220:223],v[132:135]
v_mfma_f32_16x16x32_bf16 v[136:139],v[196:199],v[220:223],v[136:139]
s_cmp_lg_u32 s79,0
s_cbranch_scc1 K2_LOOP
s_waitcnt vmcnt(0) lgkmcnt(0)
s_barrier
ds_read_b64_tr_b16 v[200:201],v246
ds_read_b64_tr_b16 v[202:203],v246 offset:5120
ds_read_b128 v[220:223],v248
v_mfma_f32_16x16x32_bf16 v[140:143],v[180:183],v[232:235],v[140:143]
ds_read_b64_tr_b16 v[204:205],v247 offset:32
v_mfma_f32_16x16x32_bf16 v[144:147],v[184:187],v[232:235],v[144:147]
ds_read_b64_tr_b16 v[206:207],v247 offset:5152
v_mfma_f32_16x16x32_bf16 v[148:151],v[188:191],v[232:235],v[148:151]
ds_read_b64_tr_b16 v[208:209],v246 offset:64
v_mfma_f32_16x16x32_bf16 v[152:155],v[192:195],v[232:235],v[152:155]
ds_read_b64_tr_b16 v[210:211],v246 offset:5184
v_mfma_f32_16x16x32_bf16 v[156:159],v[196:199],v[232:235],v[156:159]
ds_read_b64_tr_b16 v[212:213],v247 offset:96
v_mfma_f32_16x16x32_bf16 v[160:163],v[180:183],v[236:239],v[160:163]
ds_read_b64_tr_b16 v[214:215],v247 offset:5216
v_mfma_f32_16x16x32_bf16 v[164:167],v[184:187],v[236:239],v[164:167]
ds_read_b64_tr_b16 v[216:217],v246 offset:128
v_mfma_f32_16x16x32_bf16 v[168:171],v[188:191],v[236:239],v[168:171]
ds_read_b64_tr_b16 v[218:219],v246 offset:5248
v_mfma_f32_16x16x32_bf16 v[172:175],v[192:195],v[236:239],v[172:175]
ds_read_b128 v[224:227],v248 offset:1024
v_mfma_f32_16x16x32_bf16 v[176:179],v[196:199],v[236:239],v[176:179]
ds_read_b128 v[228:231],v248 offset:2048
s_waitcnt lgkmcnt(10)
v_mfma_f32_16x16x32_bf16 v[0:3],v[200:203],v[220:223],v[0:3]
s_waitcnt lgkmcnt(8)
v_mfma_f32_16x16x32_bf16 v[4:7],v[204:207],v[220:223],v[4:7]
s_waitcnt lgkmcnt(6)
v_mfma_f32_16x16x32_bf16 v[8:11],v[208:211],v[220:223],v[8:11]
s_waitcnt lgkmcnt(4)
v_mfma_f32_16x16x32_bf16 v[12:15],v[212:215],v[220:223],v[12:15]
s_waitcnt lgkmcnt(2)
v_mfma_f32_16x16x32_bf16 v[16:19],v[216:219],v[220:223],v[16:19]
ds_read_b128 v[220:223],v248 offset:3072
s_waitcnt lgkmcnt(2)
v_mfma_f32_16x16x32_bf16 v[20:23],v[200:203],v[224:227],v[20:23]
v_mfma_f32_16x16x32_bf16 v[24:27],v[204:207],v[224:227],v[24:27]
v_mfma_f32_16x16x32_bf16 v[28:31],v[208:211],v[224:227],v[28:31]
v_mfma_f32_16x16x32_bf16 v[32:35],v[212:215],v[224:227],v[32:35]
v_mfma_f32_16x16x32_bf16 v[36:39],v[216:219],v[224:227],v[36:39]
ds_read_b128 v[224:227],v248 offset:4096
s_waitcnt lgkmcnt(2)
v_mfma_f32_16x16x32_bf16 v[40:43],v[200:203],v[228:231],v[40:43]
v_mfma_f32_16x16x32_bf16 v[44:47],v[204:207],v[228:231],v[44:47]
v_mfma_f32_16x16x32_bf16 v[48:51],v[208:211],v[228:231],v[48:51]
v_mfma_f32_16x16x32_bf16 v[52:55],v[212:215],v[228:231],v[52:55]
v_mfma_f32_16x16x32_bf16 v[56:59],v[216:219],v[228:231],v[56:59]
ds_read_b128 v[228:231],v248 offset:5120
s_waitcnt lgkmcnt(2)
v_mfma_f32_16x16x32_bf16 v[60:63],v[200:203],v[220:223],v[60:63]
ds_read_b128 v[232:235],v248 offset:7168
v_mfma_f32_16x16x32_bf16 v[64:67],v[204:207],v[220:223],v[64:67]
v_mfma_f32_16x16x32_bf16 v[68:71],v[208:211],v[220:223],v[68:71]
v_mfma_f32_16x16x32_bf16 v[72:75],v[212:215],v[220:223],v[72:75]
v_mfma_f32_16x16x32_bf16 v[76:79],v[216:219],v[220:223],v[76:79]
ds_read_b128 v[220:223],v248 offset:6144
s_waitcnt lgkmcnt(3)
v_mfma_f32_16x16x32_bf16 v[80:83],v[200:203],v[224:227],v[80:83]
ds_read_b128 v[236:239],v248 offset:8192
v_mfma_f32_16x16x32_bf16 v[84:87],v[204:207],v[224:227],v[84:87]
v_mfma_f32_16x16x32_bf16 v[88:91],v[208:211],v[224:227],v[88:91]
v_mfma_f32_16x16x32_bf16 v[92:95],v[212:215],v[224:227],v[92:95]
v_mfma_f32_16x16x32_bf16 v[96:99],v[216:219],v[224:227],v[96:99]
s_waitcnt lgkmcnt(3)
v_mfma_f32_16x16x32_bf16 v[100:103],v[200:203],v[228:231],v[100:103]
v_mfma_f32_16x16x32_bf16 v[104:107],v[204:207],v[228:231],v[104:107]
v_mfma_f32_16x16x32_bf16 v[108:111],v[208:211],v[228:231],v[108:111]
v_mfma_f32_16x16x32_bf16 v[112:115],v[212:215],v[228:231],v[112:115]
v_mfma_f32_16x16x32_bf16 v[116:119],v[216:219],v[228:231],v[116:119]
s_waitcnt lgkmcnt(1)
v_mfma_f32_16x16x32_bf16 v[120:123],v[200:203],v[220:223],v[120:123]
v_mfma_f32_16x16x32_bf16 v[124:127],v[204:207],v[220:223],v[124:127]
v_mfma_f32_16x16x32_bf16 v[128:131],v[208:211],v[220:223],v[128:131]
v_mfma_f32_16x16x32_bf16 v[132:135],v[212:215],v[220:223],v[132:135]
v_mfma_f32_16x16x32_bf16 v[136:139],v[216:219],v[220:223],v[136:139]
v_mfma_f32_16x16x32_bf16 v[140:143],v[200:203],v[232:235],v[140:143]
v_mfma_f32_16x16x32_bf16 v[144:147],v[204:207],v[232:235],v[144:147]
v_mfma_f32_16x16x32_bf16 v[148:151],v[208:211],v[232:235],v[148:151]
v_mfma_f32_16x16x32_bf16 v[152:155],v[212:215],v[232:235],v[152:155]
v_mfma_f32_16x16x32_bf16 v[156:159],v[216:219],v[232:235],v[156:159]
s_waitcnt lgkmcnt(0)
v_mfma_f32_16x16x32_bf16 v[160:163],v[200:203],v[236:239],v[160:163]
v_mfma_f32_16x16x32_bf16 v[164:167],v[204:207],v[236:239],v[164:167]
v_mfma_f32_16x16x32_bf16 v[168:171],v[208:211],v[236:239],v[168:171]
v_mfma_f32_16x16x32_bf16 v[172:175],v[212:215],v[236:239],v[172:175]
v_mfma_f32_16x16x32_bf16 v[176:179],v[216:219],v[236:239],v[176:179]
s_waitcnt vmcnt(0)
s_cmp_eq_u32 s47,1
s_cbranch_scc1 K2_E1
s_cmp_eq_u32 s47,2
s_cbranch_scc1 K2_E2
s_cmp_eq_u32 s47,3
s_cbranch_scc1 K2_E3
K2_E0:
s_nop 7
s_waitcnt lgkmcnt(0)
s_barrier
s_mul_i32 s68,s46,76800
s_add_u32 s69,s68,15360
v_add_u32 v250,s69,v240
ds_write_b128 v250,v[20:23]
ds_write_b128 v250,v[24:27] offset:1024
ds_write_b128 v250,v[28:31] offset:2048
ds_write_b128 v250,v[32:35] offset:3072
ds_write_b128 v250,v[36:39] offset:4096
s_add_u32 s69,s68,30720
v_add_u32 v249,s69,v240
ds_write_b128 v249,v[40:43]
ds_write_b128 v249,v[44:47] offset:1024
ds_write_b128 v249,v[48:51] offset:2048
ds_write_b128 v249,v[52:55] offset:3072
ds_write_b128 v249,v[56:59] offset:4096
s_add_u32 s69,s68,46080
v_add_u32 v250,s69,v240
ds_write_b128 v250,v[60:63]
ds_write_b128 v250,v[64:67] offset:1024
ds_write_b128 v250,v[68:71] offset:2048
ds_write_b128 v250,v[72:75] offset:3072
ds_write_b128 v250,v[76:79] offset:4096
s_waitcnt lgkmcnt(0)
s_barrier
s_add_u32 s69,s68,0
v_add_u32 v249,s69,v240
ds_read_b128 v[180:183],v249
ds_read_b128 v[184:187],v249 offset:1024
ds_read_b128 v[188:191],v249 offset:2048
ds_read_b128 v[192:195],v249 offset:3072
ds_read_b128 v[196:199],v249 offset:4096
ds_read_b128 v[200:203],v249 offset:5120
ds_read_b128 v[204:207],v249 offset:6144
s_waitcnt lgkmcnt(6)
v_pk_add_f32 v[0:1],v[0:1],v[180:181]
v_pk_add_f32 v[2:3],v[2:3],v[182:183]
ds_read_b128 v[208:211],v249 offset:7168
s_waitcnt lgkmcnt(6)
v_pk_add_f32 v[4:5],v[4:5],v[184:185]
v_pk_add_f32 v[6:7],v[6:7],v[186:187]
ds_read_b128 v[212:215],v249 offset:8192
s_waitcnt lgkmcnt(6)
v_pk_add_f32 v[8:9],v[8:9],v[188:189]
v_pk_add_f32 v[10:11],v[10:11],v[190:191]
ds_read_b128 v[216:219],v249 offset:9216
s_waitcnt lgkmcnt(6)
v_pk_add_f32 v[12:13],v[12:13],v[192:193]
v_pk_add_f32 v[14:15],v[14:15],v[194:195]
ds_read_b128 v[180:183],v249 offset:10240
s_waitcnt lgkmcnt(6)
v_pk_add_f32 v[16:17],v[16:17],v[196:197]
v_pk_add_f32 v[18:19],v[18:19],v[198:199]
ds_read_b128 v[184:187],v249 offset:11264
s_waitcnt lgkmcnt(6)
v_pk_add_f32 v[0:1],v[0:1],v[200:201]
v_pk_add_f32 v[2:3],v[2:3],v[202:203]
ds_read_b128 v[188:191],v249 offset:12288
s_waitcnt lgkmcnt(6)
v_pk_add_f32 v[4:5],v[4:5],v[204:205]
v_pk_add_f32 v[6:7],v[6:7],v[206:207]
ds_read_b128 v[192:195],v249 offset:13312
s_waitcnt lgkmcnt(6)
v_pk_add_f32 v[8:9],v[8:9],v[208:209]
v_pk_add_f32 v[10:11],v[10:11],v[210:211]
ds_read_b128 v[196:199],v249 offset:14336
s_waitcnt lgkmcnt(6)
v_pk_add_f32 v[12:13],v[12:13],v[212:213]
v_pk_add_f32 v[14:15],v[14:15],v[214:215]
s_add_u32 s69,s68,61440
v_add_u32 v249,s69,v240
ds_read_b128 v[200:203],v249
s_waitcnt lgkmcnt(6)
v_pk_add_f32 v[16:17],v[16:17],v[216:217]
v_pk_add_f32 v[18:19],v[18:19],v[218:219]
ds_read_b128 v[204:207],v249 offset:1024
s_waitcnt lgkmcnt(6)
v_pk_add_f32 v[0:1],v[0:1],v[180:181]
v_pk_add_f32 v[2:3],v[2:3],v[182:183]
ds_read_b128 v[208:211],v249 offset:2048
s_waitcnt lgkmcnt(6)
v_pk_add_f32 v[4:5],v[4:5],v[184:185]
v_pk_add_f32 v[6:7],v[6:7],v[186:187]
ds_read_b128 v[212:215],v249 offset:3072
s_waitcnt lgkmcnt(6)
v_pk_add_f32 v[8:9],v[8:9],v[188:189]
v_pk_add_f32 v[10:11],v[10:11],v[190:191]
ds_read_b128 v[216:219],v249 offset:4096
s_waitcnt lgkmcnt(6)
v_pk_add_f32 v[12:13],v[12:13],v[192:193]
v_pk_add_f32 v[14:15],v[14:15],v[194:195]
ds_read_b128 v[180:183],v249 offset:5120
s_waitcnt lgkmcnt(6)
v_pk_add_f32 v[16:17],v[16:17],v[196:197]
v_pk_add_f32 v[18:19],v[18:19],v[198:199]
ds_read_b128 v[184:187],v249 offset:6144
s_waitcnt lgkmcnt(6)
v_pk_add_f32 v[80:81],v[80:81],v[200:201]
v_pk_add_f32 v[82:83],v[82:83],v[202:203]
ds_read_b128 v[188:191],v249 offset:7168
s_waitcnt lgkmcnt(6)
v_pk_add_f32 v[84:85],v[84:85],v[204:205]
v_pk_add_f32 v[86:87],v[86:87],v[206:207]
ds_read_b128 v[192:195],v249 offset:8192
s_waitcnt lgkmcnt(6)
v_pk_add_f32 v[88:89],v[88:89],v[208:209]
v_pk_add_f32 v[90:91],v[90:91],v[210:211]
ds_read_b128 v[196:199],v249 offset:9216
s_waitcnt lgkmcnt(6)
v_pk_add_f32 v[92:93],v[92:93],v[212:213]
v_pk_add_f32 v[94:95],v[94:95],v[214:215]
ds_read_b128 v[200:203],v249 offset:10240
s_waitcnt lgkmcnt(6)
v_pk_add_f32 v[96:97],v[96:97],v[216:217]
v_pk_add_f32 v[98:99],v[98:99],v[218:219]
ds_read_b128 v[204:207],v249 offset:11264
s_waitcnt lgkmcnt(6)
v_pk_add_f32 v[80:81],v[80:81],v[180:181]
v_pk_add_f32 v[82:83],v[82:83],v[182:183]
ds_read_b128 v[208:211],v249 offset:12288
s_waitcnt lgkmcnt(6)
v_pk_add_f32 v[84:85],v[84:85],v[184:185]
v_pk_add_f32 v[86:87],v[86:87],v[186:187]
ds_read_b128 v[212:215],v249 offset:13312
s_waitcnt lgkmcnt(6)
v_pk_add_f32 v[88:89],v[88:89],v[188:189]
v_pk_add_f32 v[90:91],v[90:91],v[190:191]
ds_read_b128 v[216:219],v249 offset:14336
s_waitcnt lgkmcnt(6)
v_pk_add_f32 v[92:93],v[92:93],v[192:193]
v_pk_add_f32 v[94:95],v[94:95],v[194:195]
s_waitcnt lgkmcnt(5)
v_pk_add_f32 v[96:97],v[96:97],v[196:197]
v_pk_add_f32 v[98:99],v[98:99],v[198:199]
s_waitcnt lgkmcnt(4)
v_pk_add_f32 v[80:81],v[80:81],v[200:201]
v_pk_add_f32 v[82:83],v[82:83],v[202:203]
s_waitcnt lgkmcnt(3)
v_pk_add_f32 v[84:85],v[84:85],v[204:205]
v_pk_add_f32 v[86:87],v[86:87],v[206:207]
s_waitcnt lgkmcnt(2)
v_pk_add_f32 v[88:89],v[88:89],v[208:209]
v_pk_add_f32 v[90:91],v[90:91],v[210:211]
s_waitcnt lgkmcnt(1)
v_pk_add_f32 v[92:93],v[92:93],v[212:213]
v_pk_add_f32 v[94:95],v[94:95],v[214:215]
s_waitcnt lgkmcnt(0)
v_pk_add_f32 v[96:97],v[96:97],v[216:217]
v_pk_add_f32 v[98:99],v[98:99],v[218:219]
s_waitcnt lgkmcnt(0)
s_barrier
s_mul_i32 s68,s46,61440
s_add_u32 s69,s68,0
v_add_u32 v249,s69,v240
ds_write_b128 v249,v[100:103]
ds_write_b128 v249,v[104:107] offset:1024
ds_write_b128 v249,v[108:111] offset:2048
ds_write_b128 v249,v[112:115] offset:3072
ds_write_b128 v249,v[116:119] offset:4096
s_add_u32 s69,s68,15360
v_add_u32 v250,s69,v240
ds_write_b128 v250,v[120:123]
ds_write_b128 v250,v[124:127] offset:1024
ds_write_b128 v250,v[128:131] offset:2048
ds_write_b128 v250,v[132:135] offset:3072
ds_write_b128 v250,v[136:139] offset:4096
s_add_u32 s69,s68,30720
v_add_u32 v249,s69,v240
ds_write_b128 v249,v[140:143]
ds_write_b128 v249,v[144:147] offset:1024
)A"
R"A(ds_write_b128 v249,v[148:151] offset:2048
ds_write_b128 v249,v[152:155] offset:3072
ds_write_b128 v249,v[156:159] offset:4096
s_waitcnt lgkmcnt(0)
s_barrier
s_add_u32 s69,s68,46080
v_add_u32 v250,s69,v240
ds_read_b128 v[180:183],v250
ds_read_b128 v[184:187],v250 offset:1024
ds_read_b128 v[188:191],v250 offset:2048
ds_read_b128 v[192:195],v250 offset:3072
ds_read_b128 v[196:199],v250 offset:4096
ds_read_b128 v[200:203],v250 offset:5120
ds_read_b128 v[204:207],v250 offset:6144
s_waitcnt lgkmcnt(6)
v_pk_add_f32 v[160:161],v[160:161],v[180:181]
v_pk_add_f32 v[162:163],v[162:163],v[182:183]
ds_read_b128 v[208:211],v250 offset:7168
s_waitcnt lgkmcnt(6)
v_pk_add_f32 v[164:165],v[164:165],v[184:185]
v_pk_add_f32 v[166:167],v[166:167],v[186:187]
ds_read_b128 v[212:215],v250 offset:8192
s_waitcnt lgkmcnt(6)
v_pk_add_f32 v[168:169],v[168:169],v[188:189]
v_pk_add_f32 v[170:171],v[170:171],v[190:191]
ds_read_b128 v[216:219],v250 offset:9216
s_waitcnt lgkmcnt(6)
v_pk_add_f32 v[172:173],v[172:173],v[192:193]
v_pk_add_f32 v[174:175],v[174:175],v[194:195]
ds_read_b128 v[180:183],v250 offset:10240
s_waitcnt lgkmcnt(6)
v_pk_add_f32 v[176:177],v[176:177],v[196:197]
v_pk_add_f32 v[178:179],v[178:179],v[198:199]
ds_read_b128 v[184:187],v250 offset:11264
s_waitcnt lgkmcnt(6)
v_pk_add_f32 v[160:161],v[160:161],v[200:201]
v_pk_add_f32 v[162:163],v[162:163],v[202:203]
ds_read_b128 v[188:191],v250 offset:12288
s_waitcnt lgkmcnt(6)
v_pk_add_f32 v[164:165],v[164:165],v[204:205]
v_pk_add_f32 v[166:167],v[166:167],v[206:207]
ds_read_b128 v[192:195],v250 offset:13312
s_waitcnt lgkmcnt(6)
v_pk_add_f32 v[168:169],v[168:169],v[208:209]
v_pk_add_f32 v[170:171],v[170:171],v[210:211]
ds_read_b128 v[196:199],v250 offset:14336
s_waitcnt lgkmcnt(6)
v_pk_add_f32 v[172:173],v[172:173],v[212:213]
v_pk_add_f32 v[174:175],v[174:175],v[214:215]
s_waitcnt lgkmcnt(5)
v_pk_add_f32 v[176:177],v[176:177],v[216:217]
v_pk_add_f32 v[178:179],v[178:179],v[218:219]
s_waitcnt lgkmcnt(4)
v_pk_add_f32 v[160:161],v[160:161],v[180:181]
v_pk_add_f32 v[162:163],v[162:163],v[182:183]
s_waitcnt lgkmcnt(3)
v_pk_add_f32 v[164:165],v[164:165],v[184:185]
v_pk_add_f32 v[166:167],v[166:167],v[186:187]
s_waitcnt lgkmcnt(2)
v_pk_add_f32 v[168:169],v[168:169],v[188:189]
v_pk_add_f32 v[170:171],v[170:171],v[190:191]
s_waitcnt lgkmcnt(1)
v_pk_add_f32 v[172:173],v[172:173],v[192:193]
v_pk_add_f32 v[174:175],v[174:175],v[194:195]
s_waitcnt lgkmcnt(0)
v_pk_add_f32 v[176:177],v[176:177],v[196:197]
v_pk_add_f32 v[178:179],v[178:179],v[198:199]
s_lshl_b32 s68,s50,2
s_add_u32 s68,s68,s49
s_mul_i32 s69,s68,92160
s_add_u32 s60,s40,s69
s_addc_u32 s61,s41,0
s_and_b32 s61,s61,0xffff
s_mov_b32 s62,0x16800
s_mov_b32 s63,0x20000
s_lshl_b32 s69,s68,7
s_add_u32 s69,s69,16384
s_add_u32 s64,s42,s69
s_addc_u32 s65,s43,0
s_and_b32 s65,s65,0xffff
s_mov_b32 s66,0x80
s_mov_b32 s67,0x20000
s_cmp_eq_u32 s46,s48
s_cbranch_scc1 K2_E0_RECV
s_mul_i32 s69,s48,46080
s_add_u32 s70,s69,0
buffer_store_dwordx4 v[0:3],v240,s[60:63],s70 offen sc1
buffer_store_dwordx4 v[4:7],v240,s[60:63],s70 offen offset:1024 sc1
buffer_store_dwordx4 v[8:11],v240,s[60:63],s70 offen offset:2048 sc1
buffer_store_dwordx4 v[12:15],v240,s[60:63],s70 offen offset:3072 sc1
s_add_u32 s70,s70,4096
buffer_store_dwordx4 v[16:19],v240,s[60:63],s70 offen sc1
s_add_u32 s70,s69,20480
buffer_store_dwordx4 v[80:83],v240,s[60:63],s70 offen sc1
buffer_store_dwordx4 v[84:87],v240,s[60:63],s70 offen offset:1024 sc1
buffer_store_dwordx4 v[88:91],v240,s[60:63],s70 offen offset:2048 sc1
buffer_store_dwordx4 v[92:95],v240,s[60:63],s70 offen offset:3072 sc1
s_add_u32 s70,s70,4096
buffer_store_dwordx4 v[96:99],v240,s[60:63],s70 offen sc1
s_add_u32 s70,s69,40960
buffer_store_dwordx4 v[160:163],v240,s[60:63],s70 offen sc1
buffer_store_dwordx4 v[164:167],v240,s[60:63],s70 offen offset:1024 sc1
buffer_store_dwordx4 v[168:171],v240,s[60:63],s70 offen offset:2048 sc1
buffer_store_dwordx4 v[172:175],v240,s[60:63],s70 offen offset:3072 sc1
s_add_u32 s70,s70,4096
buffer_store_dwordx4 v[176:179],v240,s[60:63],s70 offen sc1
s_waitcnt vmcnt(0)
s_barrier
v_mov_b32 v249,s30
s_lshl_b32 s69,s48,6
v_mov_b32 v250,0
s_mov_b64 exec,1
buffer_store_dword v249,v250,s[64:67],s69 offen sc1
s_mov_b64 exec,-1
s_branch K2_END
K2_E0_RECV:
s_barrier
s_xor_b32 s69,s48,1
s_lshl_b32 s70,s69,6
v_mov_b32 v250,0
s_mov_b32 s79,0x4000
K2_E0_POLL:
buffer_load_dword v249,v250,s[64:67],s70 offen sc1
s_waitcnt vmcnt(0)
v_readfirstlane_b32 s71,v249
s_cmp_eq_u32 s71,s30
s_cbranch_scc1 K2_E0_GO
s_sleep 2
s_sub_u32 s79,s79,1
s_cmp_lg_u32 s79,0
s_cbranch_scc1 K2_E0_POLL
K2_E0_GO:
s_mul_i32 s69,s69,46080
s_add_u32 s70,s69,0
buffer_load_dwordx4 v[180:183],v240,s[60:63],s70 offen sc1
s_add_u32 s70,s69,1024
buffer_load_dwordx4 v[184:187],v240,s[60:63],s70 offen sc1
s_add_u32 s70,s69,2048
buffer_load_dwordx4 v[188:191],v240,s[60:63],s70 offen sc1
s_add_u32 s70,s69,3072
buffer_load_dwordx4 v[192:195],v240,s[60:63],s70 offen sc1
s_add_u32 s70,s69,4096
buffer_load_dwordx4 v[196:199],v240,s[60:63],s70 offen sc1
s_add_u32 s70,s69,20480
buffer_load_dwordx4 v[200:203],v240,s[60:63],s70 offen sc1
s_add_u32 s70,s69,21504
buffer_load_dwordx4 v[204:207],v240,s[60:63],s70 offen sc1
s_add_u32 s70,s69,22528
buffer_load_dwordx4 v[208:211],v240,s[60:63],s70 offen sc1
s_add_u32 s70,s69,23552
buffer_load_dwordx4 v[212:215],v240,s[60:63],s70 offen sc1
s_add_u32 s70,s69,24576
buffer_load_dwordx4 v[216:219],v240,s[60:63],s70 offen sc1
s_add_u32 s70,s69,40960
buffer_load_dwordx4 v[220:223],v240,s[60:63],s70 offen sc1
s_add_u32 s70,s69,41984
buffer_load_dwordx4 v[224:227],v240,s[60:63],s70 offen sc1
s_add_u32 s70,s69,43008
buffer_load_dwordx4 v[228:231],v240,s[60:63],s70 offen sc1
s_add_u32 s70,s69,44032
buffer_load_dwordx4 v[232:235],v240,s[60:63],s70 offen sc1
s_add_u32 s70,s69,45056
buffer_load_dwordx4 v[236:239],v240,s[60:63],s70 offen sc1
s_mul_i32 s69,s81,368640
s_mul_i32 s70,s80,1280
s_add_u32 s69,s69,s70
s_mul_i32 s70,s49,320
s_add_u32 s69,s69,s70
s_mul_i32 s70,s46,160
s_add_u32 s69,s69,s70
s_sub_u32 s70,0x5a0000,s69
s_add_u32 s52,s38,s69
s_addc_u32 s53,s39,0
s_and_b32 s53,s53,0xffff
s_mov_b32 s54,s70
s_mov_b32 s55,0x20000
v_mbcnt_lo_u32_b32 v249,-1,0
v_mbcnt_hi_u32_b32 v249,-1,v249
v_and_b32 v250,15,v249
v_lshrrev_b32 v251,4,v249
s_movk_i32 s69,2560
v_mul_u32_u24 v250,s69,v250
v_lshl_add_u32 v250,v251,3,v250
s_waitcnt vmcnt(0)
v_pk_add_f32 v[0:1],v[0:1],v[180:181]
v_pk_add_f32 v[2:3],v[2:3],v[182:183]
v_cvt_pk_bf16_f32 v0,v0,v1
v_cvt_pk_bf16_f32 v1,v2,v3
s_mov_b32 s70,0
buffer_store_dwordx2 v[0:1],v250,s[52:55],s70 offen
v_pk_add_f32 v[4:5],v[4:5],v[184:185]
v_pk_add_f32 v[6:7],v[6:7],v[186:187]
v_cvt_pk_bf16_f32 v4,v4,v5
v_cvt_pk_bf16_f32 v5,v6,v7
s_mov_b32 s70,0
buffer_store_dwordx2 v[4:5],v250,s[52:55],s70 offen offset:32
v_pk_add_f32 v[8:9],v[8:9],v[188:189]
v_pk_add_f32 v[10:11],v[10:11],v[190:191]
v_cvt_pk_bf16_f32 v8,v8,v9
v_cvt_pk_bf16_f32 v9,v10,v11
s_mov_b32 s70,0
buffer_store_dwordx2 v[8:9],v250,s[52:55],s70 offen offset:64
v_pk_add_f32 v[12:13],v[12:13],v[192:193]
v_pk_add_f32 v[14:15],v[14:15],v[194:195]
v_cvt_pk_bf16_f32 v12,v12,v13
v_cvt_pk_bf16_f32 v13,v14,v15
s_mov_b32 s70,0
buffer_store_dwordx2 v[12:13],v250,s[52:55],s70 offen offset:96
v_pk_add_f32 v[16:17],v[16:17],v[196:197]
v_pk_add_f32 v[18:19],v[18:19],v[198:199]
v_cvt_pk_bf16_f32 v16,v16,v17
v_cvt_pk_bf16_f32 v17,v18,v19
s_mov_b32 s70,0
buffer_store_dwordx2 v[16:17],v250,s[52:55],s70 offen offset:128
v_pk_add_f32 v[80:81],v[80:81],v[200:201]
v_pk_add_f32 v[82:83],v[82:83],v[202:203]
v_cvt_pk_bf16_f32 v80,v80,v81
v_cvt_pk_bf16_f32 v81,v82,v83
s_mov_b32 s70,163840
buffer_store_dwordx2 v[80:81],v250,s[52:55],s70 offen
v_pk_add_f32 v[84:85],v[84:85],v[204:205]
v_pk_add_f32 v[86:87],v[86:87],v[206:207]
v_cvt_pk_bf16_f32 v84,v84,v85
v_cvt_pk_bf16_f32 v85,v86,v87
s_mov_b32 s70,163840
buffer_store_dwordx2 v[84:85],v250,s[52:55],s70 offen offset:32
v_pk_add_f32 v[88:89],v[88:89],v[208:209]
v_pk_add_f32 v[90:91],v[90:91],v[210:211]
v_cvt_pk_bf16_f32 v88,v88,v89
v_cvt_pk_bf16_f32 v89,v90,v91
s_mov_b32 s70,163840
buffer_store_dwordx2 v[88:89],v250,s[52:55],s70 offen offset:64
v_pk_add_f32 v[92:93],v[92:93],v[212:213]
v_pk_add_f32 v[94:95],v[94:95],v[214:215]
v_cvt_pk_bf16_f32 v92,v92,v93
v_cvt_pk_bf16_f32 v93,v94,v95
s_mov_b32 s70,163840
buffer_store_dwordx2 v[92:93],v250,s[52:55],s70 offen offset:96
v_pk_add_f32 v[96:97],v[96:97],v[216:217]
v_pk_add_f32 v[98:99],v[98:99],v[218:219]
v_cvt_pk_bf16_f32 v96,v96,v97
v_cvt_pk_bf16_f32 v97,v98,v99
s_mov_b32 s70,163840
buffer_store_dwordx2 v[96:97],v250,s[52:55],s70 offen offset:128
v_pk_add_f32 v[160:161],v[160:161],v[220:221]
v_pk_add_f32 v[162:163],v[162:163],v[222:223]
v_cvt_pk_bf16_f32 v160,v160,v161
v_cvt_pk_bf16_f32 v161,v162,v163
s_mov_b32 s70,327680
buffer_store_dwordx2 v[160:161],v250,s[52:55],s70 offen
v_pk_add_f32 v[164:165],v[164:165],v[224:225]
v_pk_add_f32 v[166:167],v[166:167],v[226:227]
v_cvt_pk_bf16_f32 v164,v164,v165
v_cvt_pk_bf16_f32 v165,v166,v167
s_mov_b32 s70,327680
buffer_store_dwordx2 v[164:165],v250,s[52:55],s70 offen offset:32
v_pk_add_f32 v[168:169],v[168:169],v[228:229]
v_pk_add_f32 v[170:171],v[170:171],v[230:231]
v_cvt_pk_bf16_f32 v168,v168,v169
v_cvt_pk_bf16_f32 v169,v170,v171
s_mov_b32 s70,327680
buffer_store_dwordx2 v[168:169],v250,s[52:55],s70 offen offset:64
v_pk_add_f32 v[172:173],v[172:173],v[232:233]
v_pk_add_f32 v[174:175],v[174:175],v[234:235]
v_cvt_pk_bf16_f32 v172,v172,v173
v_cvt_pk_bf16_f32 v173,v174,v175
s_mov_b32 s70,327680
buffer_store_dwordx2 v[172:173],v250,s[52:55],s70 offen offset:96
v_pk_add_f32 v[176:177],v[176:177],v[236:237]
v_pk_add_f32 v[178:179],v[178:179],v[238:239]
v_cvt_pk_bf16_f32 v176,v176,v177
v_cvt_pk_bf16_f32 v177,v178,v179
s_mov_b32 s70,327680
buffer_store_dwordx2 v[176:177],v250,s[52:55],s70 offen offset:128
s_branch K2_END
K2_E1:
s_nop 7
s_waitcnt lgkmcnt(0)
s_barrier
s_mul_i32 s68,s46,76800
s_add_u32 s69,s68,0
v_add_u32 v249,s69,v240
ds_write_b128 v249,v[0:3]
ds_write_b128 v249,v[4:7] offset:1024
ds_write_b128 v249,v[8:11] offset:2048
ds_write_b128 v249,v[12:15] offset:3072
ds_write_b128 v249,v[16:19] offset:4096
s_add_u32 s69,s68,35840
v_add_u32 v249,s69,v240
ds_write_b128 v249,v[40:43]
ds_write_b128 v249,v[44:47] offset:1024
ds_write_b128 v249,v[48:51] offset:2048
ds_write_b128 v249,v[52:55] offset:3072
ds_write_b128 v249,v[56:59] offset:4096
s_add_u32 s69,s68,51200
v_add_u32 v250,s69,v240
ds_write_b128 v250,v[60:63]
ds_write_b128 v250,v[64:67] offset:1024
ds_write_b128 v250,v[68:71] offset:2048
ds_write_b128 v250,v[72:75] offset:3072
ds_write_b128 v250,v[76:79] offset:4096
s_add_u32 s69,s68,61440
v_add_u32 v249,s69,v240
ds_write_b128 v249,v[80:83]
ds_write_b128 v249,v[84:87] offset:1024
ds_write_b128 v249,v[88:91] offset:2048
ds_write_b128 v249,v[92:95] offset:3072
ds_write_b128 v249,v[96:99] offset:4096
s_waitcnt lgkmcnt(0)
s_barrier
s_add_u32 s69,s68,15360
v_add_u32 v250,s69,v240
ds_read_b128 v[180:183],v250
ds_read_b128 v[184:187],v250 offset:1024
ds_read_b128 v[188:191],v250 offset:2048
ds_read_b128 v[192:195],v250 offset:3072
ds_read_b128 v[196:199],v250 offset:4096
ds_read_b128 v[200:203],v250 offset:5120
ds_read_b128 v[204:207],v250 offset:6144
s_waitcnt lgkmcnt(6)
v_pk_add_f32 v[20:21],v[20:21],v[180:181]
v_pk_add_f32 v[22:23],v[22:23],v[182:183]
ds_read_b128 v[208:211],v250 offset:7168
s_waitcnt lgkmcnt(6)
v_pk_add_f32 v[24:25],v[24:25],v[184:185]
v_pk_add_f32 v[26:27],v[26:27],v[186:187]
ds_read_b128 v[212:215],v250 offset:8192
s_waitcnt lgkmcnt(6)
v_pk_add_f32 v[28:29],v[28:29],v[188:189]
v_pk_add_f32 v[30:31],v[30:31],v[190:191]
ds_read_b128 v[216:219],v250 offset:9216
s_waitcnt lgkmcnt(6)
v_pk_add_f32 v[32:33],v[32:33],v[192:193]
v_pk_add_f32 v[34:35],v[34:35],v[194:195]
ds_read_b128 v[180:183],v250 offset:10240
s_waitcnt lgkmcnt(6)
v_pk_add_f32 v[36:37],v[36:37],v[196:197]
v_pk_add_f32 v[38:39],v[38:39],v[198:199]
ds_read_b128 v[184:187],v250 offset:11264
s_waitcnt lgkmcnt(6)
v_pk_add_f32 v[20:21],v[20:21],v[200:201]
v_pk_add_f32 v[22:23],v[22:23],v[202:203]
ds_read_b128 v[188:191],v250 offset:12288
s_waitcnt lgkmcnt(6)
v_pk_add_f32 v[24:25],v[24:25],v[204:205]
v_pk_add_f32 v[26:27],v[26:27],v[206:207]
ds_read_b128 v[192:195],v250 offset:13312
s_waitcnt lgkmcnt(6)
v_pk_add_f32 v[28:29],v[28:29],v[208:209]
v_pk_add_f32 v[30:31],v[30:31],v[210:211]
ds_read_b128 v[196:199],v250 offset:14336
s_waitcnt lgkmcnt(6)
v_pk_add_f32 v[32:33],v[32:33],v[212:213]
v_pk_add_f32 v[34:35],v[34:35],v[214:215]
s_waitcnt lgkmcnt(5)
v_pk_add_f32 v[36:37],v[36:37],v[216:217]
v_pk_add_f32 v[38:39],v[38:39],v[218:219]
s_waitcnt lgkmcnt(4)
v_pk_add_f32 v[20:21],v[20:21],v[180:181]
v_pk_add_f32 v[22:23],v[22:23],v[182:183]
s_waitcnt lgkmcnt(3)
v_pk_add_f32 v[24:25],v[24:25],v[184:185]
v_pk_add_f32 v[26:27],v[26:27],v[186:187]
s_waitcnt lgkmcnt(2)
v_pk_add_f32 v[28:29],v[28:29],v[188:189]
v_pk_add_f32 v[30:31],v[30:31],v[190:191]
s_waitcnt lgkmcnt(1)
v_pk_add_f32 v[32:33],v[32:33],v[192:193]
v_pk_add_f32 v[34:35],v[34:35],v[194:195]
s_waitcnt lgkmcnt(0)
v_pk_add_f32 v[36:37],v[36:37],v[196:197]
v_pk_add_f32 v[38:39],v[38:39],v[198:199]
s_waitcnt lgkmcnt(0)
s_barrier
s_mul_i32 s68,s46,61440
s_add_u32 s69,s68,20480
v_add_u32 v250,s69,v240
ds_write_b128 v250,v[120:123]
ds_write_b128 v250,v[124:127] offset:1024
ds_write_b128 v250,v[128:131] offset:2048
ds_write_b128 v250,v[132:135] offset:3072
ds_write_b128 v250,v[136:139] offset:4096
s_add_u32 s69,s68,35840
v_add_u32 v249,s69,v240
ds_write_b128 v249,v[140:143]
ds_write_b128 v249,v[144:147] offset:1024
ds_write_b128 v249,v[148:151] offset:2048
ds_write_b128 v249,v[152:155] offset:3072
ds_write_b128 v249,v[156:159] offset:4096
s_add_u32 s69,s68,46080
v_add_u32 v250,s69,v240
ds_write_b128 v250,v[160:163]
ds_write_b128 v250,v[164:167] offset:1024
ds_write_b128 v250,v[168:171] offset:2048
ds_write_b128 v250,v[172:175] offset:3072
ds_write_b128 v250,v[176:179] offset:4096
s_waitcnt lgkmcnt(0)
s_barrier
s_add_u32 s69,s68,0
v_add_u32 v249,s69,v240
)A"
R"A(ds_read_b128 v[180:183],v249
ds_read_b128 v[184:187],v249 offset:1024
ds_read_b128 v[188:191],v249 offset:2048
ds_read_b128 v[192:195],v249 offset:3072
ds_read_b128 v[196:199],v249 offset:4096
ds_read_b128 v[200:203],v249 offset:5120
ds_read_b128 v[204:207],v249 offset:6144
s_waitcnt lgkmcnt(6)
v_pk_add_f32 v[100:101],v[100:101],v[180:181]
v_pk_add_f32 v[102:103],v[102:103],v[182:183]
ds_read_b128 v[208:211],v249 offset:7168
s_waitcnt lgkmcnt(6)
v_pk_add_f32 v[104:105],v[104:105],v[184:185]
v_pk_add_f32 v[106:107],v[106:107],v[186:187]
ds_read_b128 v[212:215],v249 offset:8192
s_waitcnt lgkmcnt(6)
v_pk_add_f32 v[108:109],v[108:109],v[188:189]
v_pk_add_f32 v[110:111],v[110:111],v[190:191]
ds_read_b128 v[216:219],v249 offset:9216
s_waitcnt lgkmcnt(6)
v_pk_add_f32 v[112:113],v[112:113],v[192:193]
v_pk_add_f32 v[114:115],v[114:115],v[194:195]
ds_read_b128 v[180:183],v249 offset:10240
s_waitcnt lgkmcnt(6)
v_pk_add_f32 v[116:117],v[116:117],v[196:197]
v_pk_add_f32 v[118:119],v[118:119],v[198:199]
ds_read_b128 v[184:187],v249 offset:11264
s_waitcnt lgkmcnt(6)
v_pk_add_f32 v[100:101],v[100:101],v[200:201]
v_pk_add_f32 v[102:103],v[102:103],v[202:203]
ds_read_b128 v[188:191],v249 offset:12288
s_waitcnt lgkmcnt(6)
v_pk_add_f32 v[104:105],v[104:105],v[204:205]
v_pk_add_f32 v[106:107],v[106:107],v[206:207]
ds_read_b128 v[192:195],v249 offset:13312
s_waitcnt lgkmcnt(6)
v_pk_add_f32 v[108:109],v[108:109],v[208:209]
v_pk_add_f32 v[110:111],v[110:111],v[210:211]
ds_read_b128 v[196:199],v249 offset:14336
s_waitcnt lgkmcnt(6)
v_pk_add_f32 v[112:113],v[112:113],v[212:213]
v_pk_add_f32 v[114:115],v[114:115],v[214:215]
s_waitcnt lgkmcnt(5)
v_pk_add_f32 v[116:117],v[116:117],v[216:217]
v_pk_add_f32 v[118:119],v[118:119],v[218:219]
s_waitcnt lgkmcnt(4)
v_pk_add_f32 v[100:101],v[100:101],v[180:181]
v_pk_add_f32 v[102:103],v[102:103],v[182:183]
s_waitcnt lgkmcnt(3)
v_pk_add_f32 v[104:105],v[104:105],v[184:185]
v_pk_add_f32 v[106:107],v[106:107],v[186:187]
s_waitcnt lgkmcnt(2)
v_pk_add_f32 v[108:109],v[108:109],v[188:189]
v_pk_add_f32 v[110:111],v[110:111],v[190:191]
s_waitcnt lgkmcnt(1)
v_pk_add_f32 v[112:113],v[112:113],v[192:193]
v_pk_add_f32 v[114:115],v[114:115],v[194:195]
s_waitcnt lgkmcnt(0)
v_pk_add_f32 v[116:117],v[116:117],v[196:197]
v_pk_add_f32 v[118:119],v[118:119],v[198:199]
s_lshl_b32 s68,s50,2
s_add_u32 s68,s68,s49
s_mul_i32 s69,s68,92160
s_add_u32 s60,s40,s69
s_addc_u32 s61,s41,0
s_and_b32 s61,s61,0xffff
s_mov_b32 s62,0x16800
s_mov_b32 s63,0x20000
s_lshl_b32 s69,s68,7
s_add_u32 s69,s69,16384
s_add_u32 s64,s42,s69
s_addc_u32 s65,s43,0
s_and_b32 s65,s65,0xffff
s_mov_b32 s66,0x80
s_mov_b32 s67,0x20000
s_cmp_eq_u32 s46,s48
s_cbranch_scc1 K2_E1_RECV
s_mul_i32 s69,s48,46080
s_add_u32 s70,s69,5120
buffer_store_dwordx4 v[20:23],v240,s[60:63],s70 offen sc1
buffer_store_dwordx4 v[24:27],v240,s[60:63],s70 offen offset:1024 sc1
buffer_store_dwordx4 v[28:31],v240,s[60:63],s70 offen offset:2048 sc1
buffer_store_dwordx4 v[32:35],v240,s[60:63],s70 offen offset:3072 sc1
s_add_u32 s70,s70,4096
buffer_store_dwordx4 v[36:39],v240,s[60:63],s70 offen sc1
s_add_u32 s70,s69,25600
buffer_store_dwordx4 v[100:103],v240,s[60:63],s70 offen sc1
buffer_store_dwordx4 v[104:107],v240,s[60:63],s70 offen offset:1024 sc1
buffer_store_dwordx4 v[108:111],v240,s[60:63],s70 offen offset:2048 sc1
buffer_store_dwordx4 v[112:115],v240,s[60:63],s70 offen offset:3072 sc1
s_add_u32 s70,s70,4096
buffer_store_dwordx4 v[116:119],v240,s[60:63],s70 offen sc1
s_waitcnt vmcnt(0)
s_barrier
s_branch K2_END
K2_E1_RECV:
s_barrier
s_xor_b32 s69,s48,1
s_lshl_b32 s70,s69,6
v_mov_b32 v250,0
s_mov_b32 s79,0x4000
K2_E1_POLL:
buffer_load_dword v249,v250,s[64:67],s70 offen sc1
s_waitcnt vmcnt(0)
v_readfirstlane_b32 s71,v249
s_cmp_eq_u32 s71,s30
s_cbranch_scc1 K2_E1_GO
s_sleep 2
s_sub_u32 s79,s79,1
s_cmp_lg_u32 s79,0
s_cbranch_scc1 K2_E1_POLL
K2_E1_GO:
s_mul_i32 s69,s69,46080
s_add_u32 s70,s69,5120
buffer_load_dwordx4 v[180:183],v240,s[60:63],s70 offen sc1
s_add_u32 s70,s69,6144
buffer_load_dwordx4 v[184:187],v240,s[60:63],s70 offen sc1
s_add_u32 s70,s69,7168
buffer_load_dwordx4 v[188:191],v240,s[60:63],s70 offen sc1
s_add_u32 s70,s69,8192
buffer_load_dwordx4 v[192:195],v240,s[60:63],s70 offen sc1
s_add_u32 s70,s69,9216
buffer_load_dwordx4 v[196:199],v240,s[60:63],s70 offen sc1
s_add_u32 s70,s69,25600
buffer_load_dwordx4 v[200:203],v240,s[60:63],s70 offen sc1
s_add_u32 s70,s69,26624
buffer_load_dwordx4 v[204:207],v240,s[60:63],s70 offen sc1
s_add_u32 s70,s69,27648
buffer_load_dwordx4 v[208:211],v240,s[60:63],s70 offen sc1
s_add_u32 s70,s69,28672
buffer_load_dwordx4 v[212:215],v240,s[60:63],s70 offen sc1
s_add_u32 s70,s69,29696
buffer_load_dwordx4 v[216:219],v240,s[60:63],s70 offen sc1
s_mul_i32 s69,s81,368640
s_mul_i32 s70,s80,1280
s_add_u32 s69,s69,s70
s_mul_i32 s70,s49,320
s_add_u32 s69,s69,s70
s_mul_i32 s70,s46,160
s_add_u32 s69,s69,s70
s_sub_u32 s70,0x5a0000,s69
s_add_u32 s52,s38,s69
s_addc_u32 s53,s39,0
s_and_b32 s53,s53,0xffff
s_mov_b32 s54,s70
s_mov_b32 s55,0x20000
v_mbcnt_lo_u32_b32 v249,-1,0
v_mbcnt_hi_u32_b32 v249,-1,v249
v_and_b32 v250,15,v249
v_lshrrev_b32 v251,4,v249
s_movk_i32 s69,2560
v_mul_u32_u24 v250,s69,v250
v_lshl_add_u32 v250,v251,3,v250
s_waitcnt vmcnt(0)
v_pk_add_f32 v[20:21],v[20:21],v[180:181]
v_pk_add_f32 v[22:23],v[22:23],v[182:183]
v_cvt_pk_bf16_f32 v20,v20,v21
v_cvt_pk_bf16_f32 v21,v22,v23
s_mov_b32 s70,40960
buffer_store_dwordx2 v[20:21],v250,s[52:55],s70 offen
v_pk_add_f32 v[24:25],v[24:25],v[184:185]
v_pk_add_f32 v[26:27],v[26:27],v[186:187]
v_cvt_pk_bf16_f32 v24,v24,v25
v_cvt_pk_bf16_f32 v25,v26,v27
s_mov_b32 s70,40960
buffer_store_dwordx2 v[24:25],v250,s[52:55],s70 offen offset:32
v_pk_add_f32 v[28:29],v[28:29],v[188:189]
v_pk_add_f32 v[30:31],v[30:31],v[190:191]
v_cvt_pk_bf16_f32 v28,v28,v29
v_cvt_pk_bf16_f32 v29,v30,v31
s_mov_b32 s70,40960
buffer_store_dwordx2 v[28:29],v250,s[52:55],s70 offen offset:64
v_pk_add_f32 v[32:33],v[32:33],v[192:193]
v_pk_add_f32 v[34:35],v[34:35],v[194:195]
v_cvt_pk_bf16_f32 v32,v32,v33
v_cvt_pk_bf16_f32 v33,v34,v35
s_mov_b32 s70,40960
buffer_store_dwordx2 v[32:33],v250,s[52:55],s70 offen offset:96
v_pk_add_f32 v[36:37],v[36:37],v[196:197]
v_pk_add_f32 v[38:39],v[38:39],v[198:199]
v_cvt_pk_bf16_f32 v36,v36,v37
v_cvt_pk_bf16_f32 v37,v38,v39
s_mov_b32 s70,40960
buffer_store_dwordx2 v[36:37],v250,s[52:55],s70 offen offset:128
v_pk_add_f32 v[100:101],v[100:101],v[200:201]
v_pk_add_f32 v[102:103],v[102:103],v[202:203]
v_cvt_pk_bf16_f32 v100,v100,v101
v_cvt_pk_bf16_f32 v101,v102,v103
s_mov_b32 s70,204800
buffer_store_dwordx2 v[100:101],v250,s[52:55],s70 offen
v_pk_add_f32 v[104:105],v[104:105],v[204:205]
v_pk_add_f32 v[106:107],v[106:107],v[206:207]
v_cvt_pk_bf16_f32 v104,v104,v105
v_cvt_pk_bf16_f32 v105,v106,v107
s_mov_b32 s70,204800
buffer_store_dwordx2 v[104:105],v250,s[52:55],s70 offen offset:32
v_pk_add_f32 v[108:109],v[108:109],v[208:209]
v_pk_add_f32 v[110:111],v[110:111],v[210:211]
v_cvt_pk_bf16_f32 v108,v108,v109
v_cvt_pk_bf16_f32 v109,v110,v111
s_mov_b32 s70,204800
buffer_store_dwordx2 v[108:109],v250,s[52:55],s70 offen offset:64
v_pk_add_f32 v[112:113],v[112:113],v[212:213]
v_pk_add_f32 v[114:115],v[114:115],v[214:215]
v_cvt_pk_bf16_f32 v112,v112,v113
v_cvt_pk_bf16_f32 v113,v114,v115
s_mov_b32 s70,204800
buffer_store_dwordx2 v[112:113],v250,s[52:55],s70 offen offset:96
v_pk_add_f32 v[116:117],v[116:117],v[216:217]
v_pk_add_f32 v[118:119],v[118:119],v[218:219]
v_cvt_pk_bf16_f32 v116,v116,v117
v_cvt_pk_bf16_f32 v117,v118,v119
s_mov_b32 s70,204800
buffer_store_dwordx2 v[116:117],v250,s[52:55],s70 offen offset:128
s_branch K2_END
K2_E2:
s_nop 7
s_waitcnt lgkmcnt(0)
s_barrier
s_mul_i32 s68,s46,76800
s_add_u32 s69,s68,5120
v_add_u32 v249,s69,v240
ds_write_b128 v249,v[0:3]
ds_write_b128 v249,v[4:7] offset:1024
ds_write_b128 v249,v[8:11] offset:2048
ds_write_b128 v249,v[12:15] offset:3072
ds_write_b128 v249,v[16:19] offset:4096
s_add_u32 s69,s68,20480
v_add_u32 v250,s69,v240
ds_write_b128 v250,v[20:23]
ds_write_b128 v250,v[24:27] offset:1024
ds_write_b128 v250,v[28:31] offset:2048
ds_write_b128 v250,v[32:35] offset:3072
ds_write_b128 v250,v[36:39] offset:4096
s_add_u32 s69,s68,56320
v_add_u32 v250,s69,v240
ds_write_b128 v250,v[60:63]
ds_write_b128 v250,v[64:67] offset:1024
ds_write_b128 v250,v[68:71] offset:2048
ds_write_b128 v250,v[72:75] offset:3072
ds_write_b128 v250,v[76:79] offset:4096
s_add_u32 s69,s68,66560
v_add_u32 v249,s69,v240
ds_write_b128 v249,v[80:83]
ds_write_b128 v249,v[84:87] offset:1024
ds_write_b128 v249,v[88:91] offset:2048
ds_write_b128 v249,v[92:95] offset:3072
ds_write_b128 v249,v[96:99] offset:4096
s_waitcnt lgkmcnt(0)
s_barrier
s_add_u32 s69,s68,30720
v_add_u32 v249,s69,v240
ds_read_b128 v[180:183],v249
ds_read_b128 v[184:187],v249 offset:1024
ds_read_b128 v[188:191],v249 offset:2048
ds_read_b128 v[192:195],v249 offset:3072
ds_read_b128 v[196:199],v249 offset:4096
ds_read_b128 v[200:203],v249 offset:5120
ds_read_b128 v[204:207],v249 offset:6144
s_waitcnt lgkmcnt(6)
v_pk_add_f32 v[40:41],v[40:41],v[180:181]
v_pk_add_f32 v[42:43],v[42:43],v[182:183]
ds_read_b128 v[208:211],v249 offset:7168
s_waitcnt lgkmcnt(6)
v_pk_add_f32 v[44:45],v[44:45],v[184:185]
v_pk_add_f32 v[46:47],v[46:47],v[186:187]
ds_read_b128 v[212:215],v249 offset:8192
s_waitcnt lgkmcnt(6)
v_pk_add_f32 v[48:49],v[48:49],v[188:189]
v_pk_add_f32 v[50:51],v[50:51],v[190:191]
ds_read_b128 v[216:219],v249 offset:9216
s_waitcnt lgkmcnt(6)
v_pk_add_f32 v[52:53],v[52:53],v[192:193]
v_pk_add_f32 v[54:55],v[54:55],v[194:195]
ds_read_b128 v[180:183],v249 offset:10240
s_waitcnt lgkmcnt(6)
v_pk_add_f32 v[56:57],v[56:57],v[196:197]
v_pk_add_f32 v[58:59],v[58:59],v[198:199]
ds_read_b128 v[184:187],v249 offset:11264
s_waitcnt lgkmcnt(6)
v_pk_add_f32 v[40:41],v[40:41],v[200:201]
v_pk_add_f32 v[42:43],v[42:43],v[202:203]
ds_read_b128 v[188:191],v249 offset:12288
s_waitcnt lgkmcnt(6)
v_pk_add_f32 v[44:45],v[44:45],v[204:205]
v_pk_add_f32 v[46:47],v[46:47],v[206:207]
ds_read_b128 v[192:195],v249 offset:13312
s_waitcnt lgkmcnt(6)
v_pk_add_f32 v[48:49],v[48:49],v[208:209]
v_pk_add_f32 v[50:51],v[50:51],v[210:211]
ds_read_b128 v[196:199],v249 offset:14336
s_waitcnt lgkmcnt(6)
v_pk_add_f32 v[52:53],v[52:53],v[212:213]
v_pk_add_f32 v[54:55],v[54:55],v[214:215]
s_waitcnt lgkmcnt(5)
v_pk_add_f32 v[56:57],v[56:57],v[216:217]
v_pk_add_f32 v[58:59],v[58:59],v[218:219]
s_waitcnt lgkmcnt(4)
v_pk_add_f32 v[40:41],v[40:41],v[180:181]
v_pk_add_f32 v[42:43],v[42:43],v[182:183]
s_waitcnt lgkmcnt(3)
v_pk_add_f32 v[44:45],v[44:45],v[184:185]
v_pk_add_f32 v[46:47],v[46:47],v[186:187]
s_waitcnt lgkmcnt(2)
v_pk_add_f32 v[48:49],v[48:49],v[188:189]
v_pk_add_f32 v[50:51],v[50:51],v[190:191]
s_waitcnt lgkmcnt(1)
v_pk_add_f32 v[52:53],v[52:53],v[192:193]
v_pk_add_f32 v[54:55],v[54:55],v[194:195]
s_waitcnt lgkmcnt(0)
v_pk_add_f32 v[56:57],v[56:57],v[196:197]
v_pk_add_f32 v[58:59],v[58:59],v[198:199]
s_waitcnt lgkmcnt(0)
s_barrier
s_mul_i32 s68,s46,61440
s_add_u32 s69,s68,5120
v_add_u32 v249,s69,v240
ds_write_b128 v249,v[100:103]
ds_write_b128 v249,v[104:107] offset:1024
ds_write_b128 v249,v[108:111] offset:2048
ds_write_b128 v249,v[112:115] offset:3072
ds_write_b128 v249,v[116:119] offset:4096
s_add_u32 s69,s68,40960
v_add_u32 v249,s69,v240
ds_write_b128 v249,v[140:143]
ds_write_b128 v249,v[144:147] offset:1024
ds_write_b128 v249,v[148:151] offset:2048
ds_write_b128 v249,v[152:155] offset:3072
ds_write_b128 v249,v[156:159] offset:4096
s_add_u32 s69,s68,51200
v_add_u32 v250,s69,v240
ds_write_b128 v250,v[160:163]
ds_write_b128 v250,v[164:167] offset:1024
ds_write_b128 v250,v[168:171] offset:2048
ds_write_b128 v250,v[172:175] offset:3072
ds_write_b128 v250,v[176:179] offset:4096
s_waitcnt lgkmcnt(0)
s_barrier
s_add_u32 s69,s68,15360
v_add_u32 v250,s69,v240
ds_read_b128 v[180:183],v250
ds_read_b128 v[184:187],v250 offset:1024
ds_read_b128 v[188:191],v250 offset:2048
ds_read_b128 v[192:195],v250 offset:3072
ds_read_b128 v[196:199],v250 offset:4096
ds_read_b128 v[200:203],v250 offset:5120
ds_read_b128 v[204:207],v250 offset:6144
s_waitcnt lgkmcnt(6)
v_pk_add_f32 v[120:121],v[120:121],v[180:181]
v_pk_add_f32 v[122:123],v[122:123],v[182:183]
ds_read_b128 v[208:211],v250 offset:7168
s_waitcnt lgkmcnt(6)
v_pk_add_f32 v[124:125],v[124:125],v[184:185]
v_pk_add_f32 v[126:127],v[126:127],v[186:187]
ds_read_b128 v[212:215],v250 offset:8192
s_waitcnt lgkmcnt(6)
v_pk_add_f32 v[128:129],v[128:129],v[188:189]
v_pk_add_f32 v[130:131],v[130:131],v[190:191]
ds_read_b128 v[216:219],v250 offset:9216
s_waitcnt lgkmcnt(6)
v_pk_add_f32 v[132:133],v[132:133],v[192:193]
v_pk_add_f32 v[134:135],v[134:135],v[194:195]
ds_read_b128 v[180:183],v250 offset:10240
s_waitcnt lgkmcnt(6)
v_pk_add_f32 v[136:137],v[136:137],v[196:197]
v_pk_add_f32 v[138:139],v[138:139],v[198:199]
ds_read_b128 v[184:187],v250 offset:11264
s_waitcnt lgkmcnt(6)
v_pk_add_f32 v[120:121],v[120:121],v[200:201]
v_pk_add_f32 v[122:123],v[122:123],v[202:203]
ds_read_b128 v[188:191],v250 offset:12288
s_waitcnt lgkmcnt(6)
v_pk_add_f32 v[124:125],v[124:125],v[204:205]
v_pk_add_f32 v[126:127],v[126:127],v[206:207]
ds_read_b128 v[192:195],v250 offset:13312
s_waitcnt lgkmcnt(6)
v_pk_add_f32 v[128:129],v[128:129],v[208:209]
v_pk_add_f32 v[130:131],v[130:131],v[210:211]
ds_read_b128 v[196:199],v250 offset:14336
s_waitcnt lgkmcnt(6)
v_pk_add_f32 v[132:133],v[132:133],v[212:213]
v_pk_add_f32 v[134:135],v[134:135],v[214:215]
s_waitcnt lgkmcnt(5)
v_pk_add_f32 v[136:137],v[136:137],v[216:217]
v_pk_add_f32 v[138:139],v[138:139],v[218:219]
s_waitcnt lgkmcnt(4)
v_pk_add_f32 v[120:121],v[120:121],v[180:181]
v_pk_add_f32 v[122:123],v[122:123],v[182:183]
s_waitcnt lgkmcnt(3)
v_pk_add_f32 v[124:125],v[124:125],v[184:185]
v_pk_add_f32 v[126:127],v[126:127],v[186:187]
s_waitcnt lgkmcnt(2)
v_pk_add_f32 v[128:129],v[128:129],v[188:189]
v_pk_add_f32 v[130:131],v[130:131],v[190:191]
s_waitcnt lgkmcnt(1)
v_pk_add_f32 v[132:133],v[132:133],v[192:193]
v_pk_add_f32 v[134:135],v[134:135],v[194:195]
s_waitcnt lgkmcnt(0)
v_pk_add_f32 v[136:137],v[136:137],v[196:197]
v_pk_add_f32 v[138:139],v[138:139],v[198:199]
)A"
R"A(s_lshl_b32 s68,s50,2
s_add_u32 s68,s68,s49
s_mul_i32 s69,s68,92160
s_add_u32 s60,s40,s69
s_addc_u32 s61,s41,0
s_and_b32 s61,s61,0xffff
s_mov_b32 s62,0x16800
s_mov_b32 s63,0x20000
s_lshl_b32 s69,s68,7
s_add_u32 s69,s69,16384
s_add_u32 s64,s42,s69
s_addc_u32 s65,s43,0
s_and_b32 s65,s65,0xffff
s_mov_b32 s66,0x80
s_mov_b32 s67,0x20000
s_cmp_eq_u32 s46,s48
s_cbranch_scc1 K2_E2_RECV
s_mul_i32 s69,s48,46080
s_add_u32 s70,s69,10240
buffer_store_dwordx4 v[40:43],v240,s[60:63],s70 offen sc1
buffer_store_dwordx4 v[44:47],v240,s[60:63],s70 offen offset:1024 sc1
buffer_store_dwordx4 v[48:51],v240,s[60:63],s70 offen offset:2048 sc1
buffer_store_dwordx4 v[52:55],v240,s[60:63],s70 offen offset:3072 sc1
s_add_u32 s70,s70,4096
buffer_store_dwordx4 v[56:59],v240,s[60:63],s70 offen sc1
s_add_u32 s70,s69,30720
buffer_store_dwordx4 v[120:123],v240,s[60:63],s70 offen sc1
buffer_store_dwordx4 v[124:127],v240,s[60:63],s70 offen offset:1024 sc1
buffer_store_dwordx4 v[128:131],v240,s[60:63],s70 offen offset:2048 sc1
buffer_store_dwordx4 v[132:135],v240,s[60:63],s70 offen offset:3072 sc1
s_add_u32 s70,s70,4096
buffer_store_dwordx4 v[136:139],v240,s[60:63],s70 offen sc1
s_waitcnt vmcnt(0)
s_barrier
s_branch K2_END
K2_E2_RECV:
s_barrier
s_xor_b32 s69,s48,1
s_lshl_b32 s70,s69,6
v_mov_b32 v250,0
s_mov_b32 s79,0x4000
K2_E2_POLL:
buffer_load_dword v249,v250,s[64:67],s70 offen sc1
s_waitcnt vmcnt(0)
v_readfirstlane_b32 s71,v249
s_cmp_eq_u32 s71,s30
s_cbranch_scc1 K2_E2_GO
s_sleep 2
s_sub_u32 s79,s79,1
s_cmp_lg_u32 s79,0
s_cbranch_scc1 K2_E2_POLL
K2_E2_GO:
s_mul_i32 s69,s69,46080
s_add_u32 s70,s69,10240
buffer_load_dwordx4 v[180:183],v240,s[60:63],s70 offen sc1
s_add_u32 s70,s69,11264
buffer_load_dwordx4 v[184:187],v240,s[60:63],s70 offen sc1
s_add_u32 s70,s69,12288
buffer_load_dwordx4 v[188:191],v240,s[60:63],s70 offen sc1
s_add_u32 s70,s69,13312
buffer_load_dwordx4 v[192:195],v240,s[60:63],s70 offen sc1
s_add_u32 s70,s69,14336
buffer_load_dwordx4 v[196:199],v240,s[60:63],s70 offen sc1
s_add_u32 s70,s69,30720
buffer_load_dwordx4 v[200:203],v240,s[60:63],s70 offen sc1
s_add_u32 s70,s69,31744
buffer_load_dwordx4 v[204:207],v240,s[60:63],s70 offen sc1
s_add_u32 s70,s69,32768
buffer_load_dwordx4 v[208:211],v240,s[60:63],s70 offen sc1
s_add_u32 s70,s69,33792
buffer_load_dwordx4 v[212:215],v240,s[60:63],s70 offen sc1
s_add_u32 s70,s69,34816
buffer_load_dwordx4 v[216:219],v240,s[60:63],s70 offen sc1
s_mul_i32 s69,s81,368640
s_mul_i32 s70,s80,1280
s_add_u32 s69,s69,s70
s_mul_i32 s70,s49,320
s_add_u32 s69,s69,s70
s_mul_i32 s70,s46,160
s_add_u32 s69,s69,s70
s_sub_u32 s70,0x5a0000,s69
s_add_u32 s52,s38,s69
s_addc_u32 s53,s39,0
s_and_b32 s53,s53,0xffff
s_mov_b32 s54,s70
s_mov_b32 s55,0x20000
v_mbcnt_lo_u32_b32 v249,-1,0
v_mbcnt_hi_u32_b32 v249,-1,v249
v_and_b32 v250,15,v249
v_lshrrev_b32 v251,4,v249
s_movk_i32 s69,2560
v_mul_u32_u24 v250,s69,v250
v_lshl_add_u32 v250,v251,3,v250
s_waitcnt vmcnt(0)
v_pk_add_f32 v[40:41],v[40:41],v[180:181]
v_pk_add_f32 v[42:43],v[42:43],v[182:183]
v_cvt_pk_bf16_f32 v40,v40,v41
v_cvt_pk_bf16_f32 v41,v42,v43
s_mov_b32 s70,81920
buffer_store_dwordx2 v[40:41],v250,s[52:55],s70 offen
v_pk_add_f32 v[44:45],v[44:45],v[184:185]
v_pk_add_f32 v[46:47],v[46:47],v[186:187]
v_cvt_pk_bf16_f32 v44,v44,v45
v_cvt_pk_bf16_f32 v45,v46,v47
s_mov_b32 s70,81920
buffer_store_dwordx2 v[44:45],v250,s[52:55],s70 offen offset:32
v_pk_add_f32 v[48:49],v[48:49],v[188:189]
v_pk_add_f32 v[50:51],v[50:51],v[190:191]
v_cvt_pk_bf16_f32 v48,v48,v49
v_cvt_pk_bf16_f32 v49,v50,v51
s_mov_b32 s70,81920
buffer_store_dwordx2 v[48:49],v250,s[52:55],s70 offen offset:64
v_pk_add_f32 v[52:53],v[52:53],v[192:193]
v_pk_add_f32 v[54:55],v[54:55],v[194:195]
v_cvt_pk_bf16_f32 v52,v52,v53
v_cvt_pk_bf16_f32 v53,v54,v55
s_mov_b32 s70,81920
buffer_store_dwordx2 v[52:53],v250,s[52:55],s70 offen offset:96
v_pk_add_f32 v[56:57],v[56:57],v[196:197]
v_pk_add_f32 v[58:59],v[58:59],v[198:199]
v_cvt_pk_bf16_f32 v56,v56,v57
v_cvt_pk_bf16_f32 v57,v58,v59
s_mov_b32 s70,81920
buffer_store_dwordx2 v[56:57],v250,s[52:55],s70 offen offset:128
v_pk_add_f32 v[120:121],v[120:121],v[200:201]
v_pk_add_f32 v[122:123],v[122:123],v[202:203]
v_cvt_pk_bf16_f32 v120,v120,v121
v_cvt_pk_bf16_f32 v121,v122,v123
s_mov_b32 s70,245760
buffer_store_dwordx2 v[120:121],v250,s[52:55],s70 offen
v_pk_add_f32 v[124:125],v[124:125],v[204:205]
v_pk_add_f32 v[126:127],v[126:127],v[206:207]
v_cvt_pk_bf16_f32 v124,v124,v125
v_cvt_pk_bf16_f32 v125,v126,v127
s_mov_b32 s70,245760
buffer_store_dwordx2 v[124:125],v250,s[52:55],s70 offen offset:32
v_pk_add_f32 v[128:129],v[128:129],v[208:209]
v_pk_add_f32 v[130:131],v[130:131],v[210:211]
v_cvt_pk_bf16_f32 v128,v128,v129
v_cvt_pk_bf16_f32 v129,v130,v131
s_mov_b32 s70,245760
buffer_store_dwordx2 v[128:129],v250,s[52:55],s70 offen offset:64
v_pk_add_f32 v[132:133],v[132:133],v[212:213]
v_pk_add_f32 v[134:135],v[134:135],v[214:215]
v_cvt_pk_bf16_f32 v132,v132,v133
v_cvt_pk_bf16_f32 v133,v134,v135
s_mov_b32 s70,245760
buffer_store_dwordx2 v[132:133],v250,s[52:55],s70 offen offset:96
v_pk_add_f32 v[136:137],v[136:137],v[216:217]
v_pk_add_f32 v[138:139],v[138:139],v[218:219]
v_cvt_pk_bf16_f32 v136,v136,v137
v_cvt_pk_bf16_f32 v137,v138,v139
s_mov_b32 s70,245760
buffer_store_dwordx2 v[136:137],v250,s[52:55],s70 offen offset:128
s_branch K2_END
K2_E3:
s_nop 7
s_waitcnt lgkmcnt(0)
s_barrier
s_mul_i32 s68,s46,76800
s_add_u32 s69,s68,10240
v_add_u32 v249,s69,v240
ds_write_b128 v249,v[0:3]
ds_write_b128 v249,v[4:7] offset:1024
ds_write_b128 v249,v[8:11] offset:2048
ds_write_b128 v249,v[12:15] offset:3072
ds_write_b128 v249,v[16:19] offset:4096
s_add_u32 s69,s68,25600
v_add_u32 v250,s69,v240
ds_write_b128 v250,v[20:23]
ds_write_b128 v250,v[24:27] offset:1024
ds_write_b128 v250,v[28:31] offset:2048
ds_write_b128 v250,v[32:35] offset:3072
ds_write_b128 v250,v[36:39] offset:4096
s_add_u32 s69,s68,40960
v_add_u32 v249,s69,v240
ds_write_b128 v249,v[40:43]
ds_write_b128 v249,v[44:47] offset:1024
ds_write_b128 v249,v[48:51] offset:2048
ds_write_b128 v249,v[52:55] offset:3072
ds_write_b128 v249,v[56:59] offset:4096
s_add_u32 s69,s68,71680
v_add_u32 v249,s69,v240
ds_write_b128 v249,v[80:83]
ds_write_b128 v249,v[84:87] offset:1024
ds_write_b128 v249,v[88:91] offset:2048
ds_write_b128 v249,v[92:95] offset:3072
ds_write_b128 v249,v[96:99] offset:4096
s_waitcnt lgkmcnt(0)
s_barrier
s_add_u32 s69,s68,46080
v_add_u32 v250,s69,v240
ds_read_b128 v[180:183],v250
ds_read_b128 v[184:187],v250 offset:1024
ds_read_b128 v[188:191],v250 offset:2048
ds_read_b128 v[192:195],v250 offset:3072
ds_read_b128 v[196:199],v250 offset:4096
ds_read_b128 v[200:203],v250 offset:5120
ds_read_b128 v[204:207],v250 offset:6144
s_waitcnt lgkmcnt(6)
v_pk_add_f32 v[60:61],v[60:61],v[180:181]
v_pk_add_f32 v[62:63],v[62:63],v[182:183]
ds_read_b128 v[208:211],v250 offset:7168
s_waitcnt lgkmcnt(6)
v_pk_add_f32 v[64:65],v[64:65],v[184:185]
v_pk_add_f32 v[66:67],v[66:67],v[186:187]
ds_read_b128 v[212:215],v250 offset:8192
s_waitcnt lgkmcnt(6)
v_pk_add_f32 v[68:69],v[68:69],v[188:189]
v_pk_add_f32 v[70:71],v[70:71],v[190:191]
ds_read_b128 v[216:219],v250 offset:9216
s_waitcnt lgkmcnt(6)
v_pk_add_f32 v[72:73],v[72:73],v[192:193]
v_pk_add_f32 v[74:75],v[74:75],v[194:195]
ds_read_b128 v[180:183],v250 offset:10240
s_waitcnt lgkmcnt(6)
v_pk_add_f32 v[76:77],v[76:77],v[196:197]
v_pk_add_f32 v[78:79],v[78:79],v[198:199]
ds_read_b128 v[184:187],v250 offset:11264
s_waitcnt lgkmcnt(6)
v_pk_add_f32 v[60:61],v[60:61],v[200:201]
v_pk_add_f32 v[62:63],v[62:63],v[202:203]
ds_read_b128 v[188:191],v250 offset:12288
s_waitcnt lgkmcnt(6)
v_pk_add_f32 v[64:65],v[64:65],v[204:205]
v_pk_add_f32 v[66:67],v[66:67],v[206:207]
ds_read_b128 v[192:195],v250 offset:13312
s_waitcnt lgkmcnt(6)
v_pk_add_f32 v[68:69],v[68:69],v[208:209]
v_pk_add_f32 v[70:71],v[70:71],v[210:211]
ds_read_b128 v[196:199],v250 offset:14336
s_waitcnt lgkmcnt(6)
v_pk_add_f32 v[72:73],v[72:73],v[212:213]
v_pk_add_f32 v[74:75],v[74:75],v[214:215]
s_waitcnt lgkmcnt(5)
v_pk_add_f32 v[76:77],v[76:77],v[216:217]
v_pk_add_f32 v[78:79],v[78:79],v[218:219]
s_waitcnt lgkmcnt(4)
v_pk_add_f32 v[60:61],v[60:61],v[180:181]
v_pk_add_f32 v[62:63],v[62:63],v[182:183]
s_waitcnt lgkmcnt(3)
v_pk_add_f32 v[64:65],v[64:65],v[184:185]
v_pk_add_f32 v[66:67],v[66:67],v[186:187]
s_waitcnt lgkmcnt(2)
v_pk_add_f32 v[68:69],v[68:69],v[188:189]
v_pk_add_f32 v[70:71],v[70:71],v[190:191]
s_waitcnt lgkmcnt(1)
v_pk_add_f32 v[72:73],v[72:73],v[192:193]
v_pk_add_f32 v[74:75],v[74:75],v[194:195]
s_waitcnt lgkmcnt(0)
v_pk_add_f32 v[76:77],v[76:77],v[196:197]
v_pk_add_f32 v[78:79],v[78:79],v[198:199]
s_waitcnt lgkmcnt(0)
s_barrier
s_mul_i32 s68,s46,61440
s_add_u32 s69,s68,10240
v_add_u32 v249,s69,v240
ds_write_b128 v249,v[100:103]
ds_write_b128 v249,v[104:107] offset:1024
ds_write_b128 v249,v[108:111] offset:2048
ds_write_b128 v249,v[112:115] offset:3072
ds_write_b128 v249,v[116:119] offset:4096
s_add_u32 s69,s68,25600
v_add_u32 v250,s69,v240
ds_write_b128 v250,v[120:123]
ds_write_b128 v250,v[124:127] offset:1024
ds_write_b128 v250,v[128:131] offset:2048
ds_write_b128 v250,v[132:135] offset:3072
ds_write_b128 v250,v[136:139] offset:4096
s_add_u32 s69,s68,56320
v_add_u32 v250,s69,v240
ds_write_b128 v250,v[160:163]
ds_write_b128 v250,v[164:167] offset:1024
ds_write_b128 v250,v[168:171] offset:2048
ds_write_b128 v250,v[172:175] offset:3072
ds_write_b128 v250,v[176:179] offset:4096
s_waitcnt lgkmcnt(0)
s_barrier
s_add_u32 s69,s68,30720
v_add_u32 v249,s69,v240
ds_read_b128 v[180:183],v249
ds_read_b128 v[184:187],v249 offset:1024
ds_read_b128 v[188:191],v249 offset:2048
ds_read_b128 v[192:195],v249 offset:3072
ds_read_b128 v[196:199],v249 offset:4096
ds_read_b128 v[200:203],v249 offset:5120
ds_read_b128 v[204:207],v249 offset:6144
s_waitcnt lgkmcnt(6)
v_pk_add_f32 v[140:141],v[140:141],v[180:181]
v_pk_add_f32 v[142:143],v[142:143],v[182:183]
ds_read_b128 v[208:211],v249 offset:7168
s_waitcnt lgkmcnt(6)
v_pk_add_f32 v[144:145],v[144:145],v[184:185]
v_pk_add_f32 v[146:147],v[146:147],v[186:187]
ds_read_b128 v[212:215],v249 offset:8192
s_waitcnt lgkmcnt(6)
v_pk_add_f32 v[148:149],v[148:149],v[188:189]
v_pk_add_f32 v[150:151],v[150:151],v[190:191]
ds_read_b128 v[216:219],v249 offset:9216
s_waitcnt lgkmcnt(6)
v_pk_add_f32 v[152:153],v[152:153],v[192:193]
v_pk_add_f32 v[154:155],v[154:155],v[194:195]
ds_read_b128 v[180:183],v249 offset:10240
s_waitcnt lgkmcnt(6)
v_pk_add_f32 v[156:157],v[156:157],v[196:197]
v_pk_add_f32 v[158:159],v[158:159],v[198:199]
ds_read_b128 v[184:187],v249 offset:11264
s_waitcnt lgkmcnt(6)
v_pk_add_f32 v[140:141],v[140:141],v[200:201]
v_pk_add_f32 v[142:143],v[142:143],v[202:203]
ds_read_b128 v[188:191],v249 offset:12288
s_waitcnt lgkmcnt(6)
v_pk_add_f32 v[144:145],v[144:145],v[204:205]
v_pk_add_f32 v[146:147],v[146:147],v[206:207]
ds_read_b128 v[192:195],v249 offset:13312
s_waitcnt lgkmcnt(6)
v_pk_add_f32 v[148:149],v[148:149],v[208:209]
v_pk_add_f32 v[150:151],v[150:151],v[210:211]
ds_read_b128 v[196:199],v249 offset:14336
s_waitcnt lgkmcnt(6)
v_pk_add_f32 v[152:153],v[152:153],v[212:213]
v_pk_add_f32 v[154:155],v[154:155],v[214:215]
s_waitcnt lgkmcnt(5)
v_pk_add_f32 v[156:157],v[156:157],v[216:217]
v_pk_add_f32 v[158:159],v[158:159],v[218:219]
s_waitcnt lgkmcnt(4)
v_pk_add_f32 v[140:141],v[140:141],v[180:181]
v_pk_add_f32 v[142:143],v[142:143],v[182:183]
s_waitcnt lgkmcnt(3)
v_pk_add_f32 v[144:145],v[144:145],v[184:185]
v_pk_add_f32 v[146:147],v[146:147],v[186:187]
s_waitcnt lgkmcnt(2)
v_pk_add_f32 v[148:149],v[148:149],v[188:189]
v_pk_add_f32 v[150:151],v[150:151],v[190:191]
s_waitcnt lgkmcnt(1)
v_pk_add_f32 v[152:153],v[152:153],v[192:193]
v_pk_add_f32 v[154:155],v[154:155],v[194:195]
s_waitcnt lgkmcnt(0)
v_pk_add_f32 v[156:157],v[156:157],v[196:197]
v_pk_add_f32 v[158:159],v[158:159],v[198:199]
s_lshl_b32 s68,s50,2
s_add_u32 s68,s68,s49
s_mul_i32 s69,s68,92160
s_add_u32 s60,s40,s69
s_addc_u32 s61,s41,0
s_and_b32 s61,s61,0xffff
s_mov_b32 s62,0x16800
s_mov_b32 s63,0x20000
s_lshl_b32 s69,s68,7
s_add_u32 s69,s69,16384
s_add_u32 s64,s42,s69
s_addc_u32 s65,s43,0
s_and_b32 s65,s65,0xffff
s_mov_b32 s66,0x80
s_mov_b32 s67,0x20000
s_cmp_eq_u32 s46,s48
s_cbranch_scc1 K2_E3_RECV
s_mul_i32 s69,s48,46080
s_add_u32 s70,s69,15360
buffer_store_dwordx4 v[60:63],v240,s[60:63],s70 offen sc1
buffer_store_dwordx4 v[64:67],v240,s[60:63],s70 offen offset:1024 sc1
buffer_store_dwordx4 v[68:71],v240,s[60:63],s70 offen offset:2048 sc1
buffer_store_dwordx4 v[72:75],v240,s[60:63],s70 offen offset:3072 sc1
s_add_u32 s70,s70,4096
buffer_store_dwordx4 v[76:79],v240,s[60:63],s70 offen sc1
s_add_u32 s70,s69,35840
buffer_store_dwordx4 v[140:143],v240,s[60:63],s70 offen sc1
buffer_store_dwordx4 v[144:147],v240,s[60:63],s70 offen offset:1024 sc1
buffer_store_dwordx4 v[148:151],v240,s[60:63],s70 offen offset:2048 sc1
buffer_store_dwordx4 v[152:155],v240,s[60:63],s70 offen offset:3072 sc1
s_add_u32 s70,s70,4096
buffer_store_dwordx4 v[156:159],v240,s[60:63],s70 offen sc1
s_waitcnt vmcnt(0)
s_barrier
s_branch K2_END
K2_E3_RECV:
s_barrier
s_xor_b32 s69,s48,1
s_lshl_b32 s70,s69,6
v_mov_b32 v250,0
s_mov_b32 s79,0x4000
K2_E3_POLL:
buffer_load_dword v249,v250,s[64:67],s70 offen sc1
s_waitcnt vmcnt(0)
v_readfirstlane_b32 s71,v249
s_cmp_eq_u32 s71,s30
s_cbranch_scc1 K2_E3_GO
s_sleep 2
s_sub_u32 s79,s79,1
s_cmp_lg_u32 s79,0
s_cbranch_scc1 K2_E3_POLL
K2_E3_GO:
s_mul_i32 s69,s69,46080
s_add_u32 s70,s69,15360
buffer_load_dwordx4 v[180:183],v240,s[60:63],s70 offen sc1
s_add_u32 s70,s69,16384
buffer_load_dwordx4 v[184:187],v240,s[60:63],s70 offen sc1
s_add_u32 s70,s69,17408
buffer_load_dwordx4 v[188:191],v240,s[60:63],s70 offen sc1
s_add_u32 s70,s69,18432
)A"
R"A(buffer_load_dwordx4 v[192:195],v240,s[60:63],s70 offen sc1
s_add_u32 s70,s69,19456
buffer_load_dwordx4 v[196:199],v240,s[60:63],s70 offen sc1
s_add_u32 s70,s69,35840
buffer_load_dwordx4 v[200:203],v240,s[60:63],s70 offen sc1
s_add_u32 s70,s69,36864
buffer_load_dwordx4 v[204:207],v240,s[60:63],s70 offen sc1
s_add_u32 s70,s69,37888
buffer_load_dwordx4 v[208:211],v240,s[60:63],s70 offen sc1
s_add_u32 s70,s69,38912
buffer_load_dwordx4 v[212:215],v240,s[60:63],s70 offen sc1
s_add_u32 s70,s69,39936
buffer_load_dwordx4 v[216:219],v240,s[60:63],s70 offen sc1
s_mul_i32 s69,s81,368640
s_mul_i32 s70,s80,1280
s_add_u32 s69,s69,s70
s_mul_i32 s70,s49,320
s_add_u32 s69,s69,s70
s_mul_i32 s70,s46,160
s_add_u32 s69,s69,s70
s_sub_u32 s70,0x5a0000,s69
s_add_u32 s52,s38,s69
s_addc_u32 s53,s39,0
s_and_b32 s53,s53,0xffff
s_mov_b32 s54,s70
s_mov_b32 s55,0x20000
v_mbcnt_lo_u32_b32 v249,-1,0
v_mbcnt_hi_u32_b32 v249,-1,v249
v_and_b32 v250,15,v249
v_lshrrev_b32 v251,4,v249
s_movk_i32 s69,2560
v_mul_u32_u24 v250,s69,v250
v_lshl_add_u32 v250,v251,3,v250
s_waitcnt vmcnt(0)
v_pk_add_f32 v[60:61],v[60:61],v[180:181]
v_pk_add_f32 v[62:63],v[62:63],v[182:183]
v_cvt_pk_bf16_f32 v60,v60,v61
v_cvt_pk_bf16_f32 v61,v62,v63
s_mov_b32 s70,122880
buffer_store_dwordx2 v[60:61],v250,s[52:55],s70 offen
v_pk_add_f32 v[64:65],v[64:65],v[184:185]
v_pk_add_f32 v[66:67],v[66:67],v[186:187]
v_cvt_pk_bf16_f32 v64,v64,v65
v_cvt_pk_bf16_f32 v65,v66,v67
s_mov_b32 s70,122880
buffer_store_dwordx2 v[64:65],v250,s[52:55],s70 offen offset:32
v_pk_add_f32 v[68:69],v[68:69],v[188:189]
v_pk_add_f32 v[70:71],v[70:71],v[190:191]
v_cvt_pk_bf16_f32 v68,v68,v69
v_cvt_pk_bf16_f32 v69,v70,v71
s_mov_b32 s70,122880
buffer_store_dwordx2 v[68:69],v250,s[52:55],s70 offen offset:64
v_pk_add_f32 v[72:73],v[72:73],v[192:193]
v_pk_add_f32 v[74:75],v[74:75],v[194:195]
v_cvt_pk_bf16_f32 v72,v72,v73
v_cvt_pk_bf16_f32 v73,v74,v75
s_mov_b32 s70,122880
buffer_store_dwordx2 v[72:73],v250,s[52:55],s70 offen offset:96
v_pk_add_f32 v[76:77],v[76:77],v[196:197]
v_pk_add_f32 v[78:79],v[78:79],v[198:199]
v_cvt_pk_bf16_f32 v76,v76,v77
v_cvt_pk_bf16_f32 v77,v78,v79
s_mov_b32 s70,122880
buffer_store_dwordx2 v[76:77],v250,s[52:55],s70 offen offset:128
v_pk_add_f32 v[140:141],v[140:141],v[200:201]
v_pk_add_f32 v[142:143],v[142:143],v[202:203]
v_cvt_pk_bf16_f32 v140,v140,v141
v_cvt_pk_bf16_f32 v141,v142,v143
s_mov_b32 s70,286720
buffer_store_dwordx2 v[140:141],v250,s[52:55],s70 offen
v_pk_add_f32 v[144:145],v[144:145],v[204:205]
v_pk_add_f32 v[146:147],v[146:147],v[206:207]
v_cvt_pk_bf16_f32 v144,v144,v145
v_cvt_pk_bf16_f32 v145,v146,v147
s_mov_b32 s70,286720
buffer_store_dwordx2 v[144:145],v250,s[52:55],s70 offen offset:32
v_pk_add_f32 v[148:149],v[148:149],v[208:209]
v_pk_add_f32 v[150:151],v[150:151],v[210:211]
v_cvt_pk_bf16_f32 v148,v148,v149
v_cvt_pk_bf16_f32 v149,v150,v151
s_mov_b32 s70,286720
buffer_store_dwordx2 v[148:149],v250,s[52:55],s70 offen offset:64
v_pk_add_f32 v[152:153],v[152:153],v[212:213]
v_pk_add_f32 v[154:155],v[154:155],v[214:215]
v_cvt_pk_bf16_f32 v152,v152,v153
v_cvt_pk_bf16_f32 v153,v154,v155
s_mov_b32 s70,286720
buffer_store_dwordx2 v[152:153],v250,s[52:55],s70 offen offset:96
v_pk_add_f32 v[156:157],v[156:157],v[216:217]
v_pk_add_f32 v[158:159],v[158:159],v[218:219]
v_cvt_pk_bf16_f32 v156,v156,v157
v_cvt_pk_bf16_f32 v157,v158,v159
s_mov_b32 s70,286720
buffer_store_dwordx2 v[156:157],v250,s[52:55],s70 offen offset:128
s_branch K2_END
K2_END:
s_or_b32 s92,s44,s45
s_cbranch_scc1 K2_NOT
s_mov_b32 s68,0
s_add_u32 s60,s42,s68
s_addc_u32 s61,s43,0
s_and_b32 s61,s61,0xffff
s_mov_b32 s62,0x8040
s_mov_b32 s63,0x20000
v_mbcnt_lo_u32_b32 v249,-1,0
v_mbcnt_hi_u32_b32 v249,-1,v249
v_lshlrev_b32 v250,6,v249
s_mov_b32 s79,0x4000
K2_TW:
s_mov_b32 s69,0
buffer_load_dword v180,v250,s[60:63],s69 offen sc1
s_mov_b32 s69,4096
buffer_load_dword v181,v250,s[60:63],s69 offen sc1
s_mov_b32 s69,8192
buffer_load_dword v182,v250,s[60:63],s69 offen sc1
s_mov_b32 s69,12288
buffer_load_dword v183,v250,s[60:63],s69 offen sc1
s_waitcnt vmcnt(0)
v_cmp_eq_u32 s[92:93],s30,v180
v_cmp_eq_u32 vcc,s30,v181
s_nop 1
s_and_b64 s[92:93],s[92:93],vcc
v_cmp_eq_u32 vcc,s30,v182
s_nop 1
s_and_b64 s[92:93],s[92:93],vcc
v_cmp_eq_u32 vcc,s30,v183
s_nop 1
s_and_b64 s[92:93],s[92:93],vcc
s_cmp_eq_u64 s[92:93],-1
s_cbranch_scc1 K2_TOK
s_sleep 2
s_sub_u32 s79,s79,1
s_cmp_lg_u32 s79,0
s_cbranch_scc1 K2_TW
K2_TOK:
v_mov_b32 v249,s30
v_mov_b32 v250,0x8000
s_mov_b64 exec,1
buffer_store_dword v249,v250,s[60:63],0 offen sc1
s_mov_b64 exec,-1
K2_NOT:
s_waitcnt vmcnt(0) lgkmcnt(0)
)A"
      :
      : "s"(__builtin_amdgcn_kernarg_segment_ptr()), "s"(blockIdx.x), "v"(threadIdx.x)
      : "v1", "v2", "v3", "v4", "v5", "v6", "v7", "v8", "v9", "v10", "v11", "v12", "v13", "v14", "v15", "v16", "v17", "v18", "v19", "v20", "v21", "v22", "v23", "v24", "v25", "v26", "v27", "v28", "v29", "v30", "v31", "v32", "v33", "v34", "v35", "v36", "v37", "v38", "v39", "v40", "v41", "v42", "v43", "v44", "v45", "v46", "v47", "v48", "v49", "v50", "v51", "v52", "v53", "v54", "v55", "v56", "v57", "v58", "v59", "v60", "v61", "v62", "v63", "v64", "v65", "v66", "v67", "v68", "v69", "v70", "v71", "v72", "v73", "v74", "v75", "v76", "v77", "v78", "v79", "v80", "v81", "v82", "v83", "v84", "v85", "v86", "v87", "v88", "v89", "v90", "v91", "v92", "v93", "v94", "v95", "v96", "v97", "v98", "v99", "v100", "v101", "v102", "v103", "v104", "v105", "v106", "v107", "v108", "v109", "v110", "v111", "v112", "v113", "v114", "v115", "v116", "v117", "v118", "v119", "v120", "v121", "v122", "v123", "v124", "v125", "v126", "v127", "v128", "v129", "v130", "v131", "v132", "v133", "v134", "v135", "v136", "v137", "v138", "v139", "v140", "v141", "v142", "v143", "v144", "v145", "v146", "v147", "v148", "v149", "v150", "v151", "v152", "v153", "v154", "v155", "v156", "v157", "v158", "v159", "v160", "v161", "v162", "v163", "v164", "v165", "v166", "v167", "v168", "v169", "v170", "v171", "v172", "v173", "v174", "v175", "v176", "v177", "v178", "v179", "v180", "v181", "v182", "v183", "v184", "v185", "v186", "v187", "v188", "v189", "v190", "v191", "v192", "v193", "v194", "v195", "v196", "v197", "v198", "v199", "v200", "v201", "v202", "v203", "v204", "v205", "v206", "v207", "v208", "v209", "v210", "v211", "v212", "v213", "v214", "v215", "v216", "v217", "v218", "v219", "v220", "v221", "v222", "v223", "v224", "v225", "v226", "v227", "v228", "v229", "v230", "v231", "v232", "v233", "v234", "v235", "v236", "v237", "v238", "v239", "v240", "v241", "v242", "v243", "v244", "v245", "v246", "v247", "v248", "v249", "v250", "v251", "v252", "v253", "v254", "v255", "s30", "s31", "s33", "s34", "s35", "s36", "s37", "s38", "s39", "s40", "s41", "s42", "s43", "s44", "s45", "s46", "s47", "s48", "s49", "s50", "s51", "s52", "s53", "s54", "s55", "s56", "s57", "s58", "s59", "s60", "s61", "s62", "s63", "s64", "s65", "s66", "s67", "s68", "s69", "s70", "s71", "s72", "s73", "s74", "s75", "s76", "s77", "s78", "s79", "s80", "s81", "s82", "s83", "s84", "s85", "s86", "s87", "s88", "s89", "s90", "s91", "s92", "s93", "s94", "s95", "s96", "s97", "s98", "s99", "vcc", "scc", "memory");
}

extern "C" void kernel_launch(void* const* d_in, const int* in_sizes, int n_in, void* d_out, int out_size, void* d_ws, size_t ws_size, hipStream_t stream) {
  (void)hipFuncSetAttribute((const void*)fused_kernel, hipFuncAttributeMaxDynamicSharedMemorySize, LDS_BYTES);
  u16* Ows = (u16*)d_ws;
  unsigned* flags = (unsigned*)((char*)d_ws + (58u << 20));
  float* slab = (float*)((char*)d_ws + (60u << 20));
  fused_kernel<<<256, 512, LDS_BYTES, stream>>>((const u16*)d_in[0], (const u16*)d_in[1], (const u16*)d_in[2], (const u16*)d_in[3],
                                                (const u16*)d_in[5], (const u16*)d_in[6], (const u16*)d_in[7], (const u16*)d_in[8], Ows,
                                                (const u16*)d_in[4], (const u16*)d_in[9], flags, (u16*)d_out, slab);
}
```
